```python
import torch

def _softmax(x, axis=-1):
    return torch.softmax(x, dim=axis)
def _gelu(x):
    return torch.nn.functional.gelu(x, approximate="tanh")
def _silu(x):
    return torch.nn.functional.silu(x)
def _sigmoid(x):
    return torch.sigmoid(x)
def _layernorm(x, axis=-1, eps=1e-5):
    m = x.mean(dim=axis, keepdim=True)
    v = x.var(dim=axis, keepdim=True, unbiased=False)
    return (x - m) / torch.sqrt(v + eps)
def _scatter_add(tgt, idx, src, axis=0):
    return tgt.clone().index_add_(axis, idx, src)
def _segment_sum(src, seg, n):
    out = torch.zeros((n,) + src.shape[1:], dtype=src.dtype)
    return out.index_add_(0, seg, src)
def _pad(x, pw):
    flat = tuple(v for pair in reversed(pw) for v in pair)
    return torch.nn.functional.pad(x, flat)

kernel_name = 'recurrent_scan__ssm_conv_s5__dense_activation_mish__prompt'

def setup_inputs():

    _w = (lambda a: (lambda t: t.to(torch.bfloat16) if t.is_floating_point() else t)(torch.as_tensor(a)))
    def _A_setup():
        B, S, D = 1, 2048, 576
        P = D
        Lambda_re = -0.5 * (lambda _x: torch.exp(torch.as_tensor(_x)))((lambda *s: torch.randn(*s, dtype=torch.bfloat16) * 0.4)(P))
        Lambda_im = (lambda *s: torch.randn(*s, dtype=torch.bfloat16) * 0.8)(P) * 1.0
        B_re = (lambda *s: torch.randn(*s, dtype=torch.bfloat16) * 0.4)(P, D) * (D ** -0.5)
        B_im = (lambda *s: torch.randn(*s, dtype=torch.bfloat16) * 0.8)(P, D) * (D ** -0.5)
        C_re = (lambda *s: torch.randn(*s, dtype=torch.bfloat16) * 0.8)(D, P) * (P ** -0.5)
        C_im = (lambda *s: torch.randn(*s, dtype=torch.bfloat16) * 0.8)(D, P) * (P ** -0.5)
        log_dt = (lambda *s: torch.randn(*s, dtype=torch.bfloat16) * 0.4)(P) * 0.3 - 2.0
        Dskip = (lambda *s: torch.randn(*s, dtype=torch.bfloat16) * 0.2)(D)
        return {
            "x": _w((lambda *s: torch.randn(*s, dtype=torch.bfloat16) * 0.05)(B, S, D)),
            "Lambda_re": _w(Lambda_re),
            "Lambda_im": _w(Lambda_im),
            "B_re": _w(B_re),
            "B_im": _w(B_im),
            "C_re": _w(C_re),
            "C_im": _w(C_im),
            "log_dt": _w(log_dt),
            "Dskip": _w(Dskip),
        }
    _A_in = _A_setup()

    def _B_setup():
        B, S, D, F = 1, 2048, 576, 4608
        return {
            "x": _w((lambda *s: torch.randn(*s, dtype=torch.bfloat16) * 0.05)(B, S, D)),
            "w1": _w((lambda *s: torch.randn(*s, dtype=torch.bfloat16) * 0.8)(D, F) * (D ** -0.5)),
            "b1": _w((lambda *s: torch.randn(*s, dtype=torch.bfloat16) * 0.4)(F)),
            "w2": _w((lambda *s: torch.randn(*s, dtype=torch.bfloat16) * 0.1)(F, D) * (F ** -0.5)),
        }
    _B_in = _B_setup()

    return {
        "A_x": _A_in["x"],
        "A_Lambda_re": _A_in["Lambda_re"],
        "A_Lambda_im": _A_in["Lambda_im"],
        "A_B_re": _A_in["B_re"],
        "A_B_im": _A_in["B_im"],
        "A_C_re": _A_in["C_re"],
        "A_C_im": _A_in["C_im"],
        "A_log_dt": _A_in["log_dt"],
        "A_Dskip": _A_in["Dskip"],
        "B_x": _B_in["x"],
        "B_w1": _B_in["w1"],
        "B_b1": _B_in["b1"],
        "B_w2": _B_in["w2"],
    }

def reference(A_x, A_Lambda_re, A_Lambda_im, A_B_re, A_B_im, A_C_re, A_C_im, A_log_dt, A_Dskip, B_x, B_w1, B_b1, B_w2):
    def _A_ref(x, Lambda_re, Lambda_im, B_re, B_im, C_re, C_im, log_dt, Dskip):
        Bsz, L, H = x.shape
        Lam = Lambda_re + 1j * Lambda_im
        Bmat = B_re + 1j * B_im
        Cmat = C_re + 1j * C_im
        dt = torch.exp(log_dt)
        Lam_bar = torch.exp(dt * Lam)
        B_bar = ((Lam_bar - 1.0) / Lam)[:, None] * Bmat
        Bu = torch.einsum("bld,pd->blp", x + 0j, B_bar)
        h = torch.zeros_like(Bu[:, 0, :])
        hs = []
        for t in range(L):
            h = Lam_bar[None, :] * h + Bu[:, t, :]
            hs.append(h)
        hseq = torch.stack(hs, 1)
        y = 2.0 * torch.einsum("blp,dp->bld", hseq, Cmat).real + x * Dskip[None, None, :]
        return y

    def _B_ref(x, w1, b1, w2):
        h = torch.einsum("bsd,df->bsf", x, w1) + b1
        sp = torch.log1p(torch.exp(-torch.abs(h))) + torch.where(h > 0, h, torch.zeros_like(h))
        act = h * torch.tanh(sp)
        return torch.einsum("bsf,fd->bsd", act, w2)

    x = A_x
    xn = _layernorm(x, axis=-1)
    s = _A_ref(xn, A_Lambda_re, A_Lambda_im, A_B_re, A_B_im, A_C_re, A_C_im, A_log_dt, A_Dskip)
    g = _silu(_B_ref(xn, B_w1, B_b1, B_w2))
    return x + s * g
```

```cpp
#include <hip/hip_runtime.h>
#include <hip/hip_bf16.h>
typedef unsigned short u16;
__device__ __forceinline__ float bf2f(u16 v){ return __uint_as_float(((unsigned)v)<<16); }
__device__ __forceinline__ u16 f2bf(float f){ unsigned u=__float_as_uint(f); u += 0x7fffu + ((u>>16)&1u); return (u16)(u>>16);}
constexpr int S=2048,D=576,F=4608,P=576;
__global__ void k_ln(const u16* x, float* xn){
  int r=blockIdx.x; int l=threadIdx.x; float v[9]; float s=0;
  for(int i=0;i<9;i++){v[i]=bf2f(x[r*D+l+64*i]); s+=v[i];}
  for(int o=32;o;o>>=1) s+=__shfl_xor(s,o);
  float m=s/D; float q=0; for(int i=0;i<9;i++){float d=v[i]-m;q+=d*d;}
  for(int o=32;o;o>>=1) q+=__shfl_xor(q,o);
  float rs=rsqrtf(q/D+1e-5f);
  for(int i=0;i<9;i++) xn[r*D+l+64*i]=bf2f(f2bf((v[i]-m)*rs));
}
__global__ void k_g1(const float* xn,const u16* w1,const u16* b1,float* act){
  int f=blockIdx.x*256+threadIdx.x; int r0=blockIdx.y*8; float a[8]={0};
  for(int k=0;k<D;k++){ float w=bf2f(w1[k*F+f]); for(int i=0;i<8;i++) a[i]+=xn[(r0+i)*D+k]*w; }
  for(int i=0;i<8;i++){ float h=a[i]+bf2f(b1[f]); float sp=log1pf(expf(-fabsf(h)))+(h>0?h:0.f); act[(size_t)(r0+i)*F+f]=h*tanhf(sp);}
}
__global__ void k_bu(const float* xn,const u16* Bre,const u16* Bim,float* bu){
  int p=blockIdx.x*64+threadIdx.x; int r=blockIdx.y; float a=0,b=0;
  for(int k=0;k<D;k++){ float x=xn[r*D+k]; a+=x*bf2f(Bre[p*D+k]); b+=x*bf2f(Bim[p*D+k]); }
  bu[(r*P+p)*2]=a; bu[(r*P+p)*2+1]=b;
}
__global__ void k_scan(const u16* Lre,const u16* Lim,const u16* ldt,float* bu){
  int p=blockIdx.x*64+threadIdx.x; float lr=bf2f(Lre[p]),li=bf2f(Lim[p]); float dt=bf2f(f2bf(expf(bf2f(ldt[p]))));
  float e=expf(dt*lr); float ar=e*cosf(dt*li), ai=e*sinf(dt*li);
  float nr=ar-1.f, ni=ai; float den=lr*lr+li*li; float cr=(nr*lr+ni*li)/den, ci=(ni*lr-nr*li)/den;
  float hr=0,hi=0;
  for(int t=0;t<S;t++){ float a=bu[(t*P+p)*2],b=bu[(t*P+p)*2+1]; float ur=cr*a-ci*b, ui=cr*b+ci*a;
    float r2=ar*hr-ai*hi+ur, i2=ar*hi+ai*hr+ui; hr=r2;hi=i2; bu[(t*P+p)*2]=hr; bu[(t*P+p)*2+1]=hi; }
}
__global__ void k_out(const u16* x,const float* xn,const float* act,const u16* w2,const float* h,const u16* Cre,const u16* Cim,const u16* Dk,float* out){
  int d=blockIdx.x*64+threadIdx.x; int r=blockIdx.y; float z=0,y=0;
  for(int k=0;k<F;k++) z+=act[(size_t)r*F+k]*bf2f(w2[k*D+d]);
  for(int p=0;p<P;p++) y+=h[(r*P+p)*2]*bf2f(Cre[d*P+p])-h[(r*P+p)*2+1]*bf2f(Cim[d*P+p]);
  float s=2.f*y+xn[r*D+d]*bf2f(Dk[d]); float g=z/(1.f+expf(-z));
  out[r*D+d]=bf2f(x[r*D+d])+s*g;
}
extern "C" void kernel_launch(void* const* d_in, const int* in_sizes, int n_in, void* d_out, int out_size, void* d_ws, size_t ws_size, hipStream_t stream) {
  const u16* x=(const u16*)d_in[0];
  float* xn=(float*)d_ws; float* act=xn+S*D; float* bu=act+(size_t)S*F;
  k_ln<<<S,64,0,stream>>>(x,xn);
  k_g1<<<dim3(F/256,S/8),256,0,stream>>>(xn,(const u16*)d_in[10],(const u16*)d_in[11],act);
  k_bu<<<dim3(P/64,S),64,0,stream>>>(xn,(const u16*)d_in[3],(const u16*)d_in[4],bu);
  k_scan<<<P/64,64,0,stream>>>((const u16*)d_in[1],(const u16*)d_in[2],(const u16*)d_in[7],bu);
  k_out<<<dim3(D/64,S),64,0,stream>>>(x,xn,act,(const u16*)d_in[12],bu,(const u16*)d_in[5],(const u16*)d_in[6],(const u16*)d_in[8],(float*)d_out);
}
```

```cpp
#include <hip/hip_runtime.h>
#include <hip/hip_bf16.h>
#pragma clang diagnostic ignored "-Wint-to-pointer-cast"

typedef unsigned short u16;
typedef __attribute__((ext_vector_type(2))) __bf16 bf16x2;
typedef __attribute__((ext_vector_type(4))) __bf16 bf16x4;
typedef __attribute__((ext_vector_type(8))) __bf16 bf16x8;
typedef __attribute__((ext_vector_type(4))) short s16x4;
typedef __attribute__((ext_vector_type(2))) float f32x2;
typedef __attribute__((ext_vector_type(4))) float f32x4;
typedef __attribute__((ext_vector_type(16))) float f32x16;
typedef __attribute__((ext_vector_type(2))) unsigned u32x2;
typedef __attribute__((ext_vector_type(4))) unsigned u32x4;
#define LAS __attribute__((address_space(3)))
#define GAS __attribute__((address_space(1)))
#define DI __device__ __forceinline__
#define INL __attribute__((always_inline))

constexpr int S = 2048, D = 576, F = 4608;
constexpr float LOG2E = 1.44269504088896f;
constexpr int SLOT = 24576;
constexpr int X_OFF = 4 * SLOT;
constexpr int ARING = X_OFF;
constexpr int TAB_OFF = ARING + 49152;
constexpr int LT_OFF = TAB_OFF;
constexpr int WT_OFF = TAB_OFF + 6144;
constexpr int GT_OFF = TAB_OFF + 10240;
constexpr size_t WS_STATS = 0;
constexpr size_t WS_EPOCH = 16384;
constexpr size_t WS_FLAG = 32768;
constexpr size_t WS_PART = 65536;
constexpr size_t WS_GRAN = 15u << 20;
constexpr size_t WS_ACT = 16u << 20;

DI float bflo(unsigned v) { return __uint_as_float(v << 16); }
DI float bfhi(unsigned v) { return __uint_as_float(v & 0xffff0000u); }
DI unsigned pk2(float a, float b) { f32x2 v = {a, b}; return __builtin_bit_cast(unsigned, __builtin_convertvector(v, bf16x2)); }
#define WAIT_V(n) asm volatile("s_waitcnt vmcnt(%0)" ::"n"(n) : "memory")
#define WAIT_L0() asm volatile("s_waitcnt lgkmcnt(0)" ::: "memory")
#define BAR() asm volatile("s_barrier" ::: "memory")
#define SCHED() __builtin_amdgcn_sched_barrier(0)
#define BF8(x) __builtin_bit_cast(bf16x8, (x))
#define MFMA16(a, b, c) __builtin_amdgcn_mfma_f32_16x16x32_bf16((a), (b), (c), 0, 0, 0)
#define MFMA32(a, b, c) __builtin_amdgcn_mfma_f32_32x32x16_bf16((a), (b), (c), 0, 0, 0)
template <int I> struct IC { static constexpr int value = I; };
template <int B, int E, class Fn> DI void static_for(Fn&& f) { if constexpr (B < E) { f(IC<B>{}); static_for<B + 1, E>(f); } }

DI void gldsW(const void* sbase, unsigned voff, unsigned lds_addr) {
  asm volatile("s_mov_b32 m0, %2\n\ts_nop 0\n\tglobal_load_lds_dwordx4 %0, %1" ::"v"(voff), "s"(sbase), "s"(lds_addr) : "memory");
}
DI void gldsA(const void* sbase, unsigned voff, unsigned lds_addr) {
  asm volatile("s_mov_b32 m0, %2\n\ts_nop 0\n\tglobal_load_lds_dwordx4 %0, %1 sc1" ::"v"(voff), "s"(sbase), "s"(lds_addr) : "memory");
}
DI void gstore_sc1(const void* sbase, unsigned voff, u32x4 v) {
  asm volatile("global_store_dwordx4 %0, %1, %2 sc1\n\ts_nop 1" ::"v"(voff), "v"(v), "s"(sbase) : "memory");
}
DI void gstore8_sc1(const void* sbase, unsigned voff, u32x2 v) {
  asm volatile("global_store_dwordx2 %0, %1, %2 sc1\n\ts_nop 1" ::"v"(voff), "v"(v), "s"(sbase) : "memory");
}
template <int OFF> DI u32x4 gload16(const void* sbase, unsigned voff) {
  u32x4 v; asm volatile("global_load_dwordx4 %0, %1, %2 offset:%3" : "=v"(v) : "v"(voff), "s"(sbase), "n"(OFF) : "memory"); return v;
}
DI u32x4 gload16_sc1(const void* sbase, unsigned voff) {
  u32x4 v; asm volatile("global_load_dwordx4 %0, %1, %2 sc1" : "=v"(v) : "v"(voff), "s"(sbase) : "memory"); return v;
}
DI u32x2 gload8_sc1(const void* sbase, unsigned voff) {
  u32x2 v; asm volatile("global_load_dwordx2 %0, %1, %2 sc1" : "=v"(v) : "v"(voff), "s"(sbase) : "memory"); return v;
}
DI unsigned gload4_sc1(const void* sbase, unsigned voff) {
  unsigned v; asm volatile("global_load_dword %0, %1, %2 sc1" : "=v"(v) : "v"(voff), "s"(sbase) : "memory"); return v;
}
DI unsigned gload2(const void* sbase, unsigned voff) {
  unsigned v; asm volatile("global_load_ushort %0, %1, %2" : "=v"(v) : "v"(voff), "s"(sbase) : "memory"); return v;
}
template <int N> DI void wait1(u32x4& a) { asm volatile("s_waitcnt vmcnt(%1)" : "+v"(a) : "n"(N) : "memory"); }
template <int N> DI void wait2(u32x4& a, u32x4& b) { asm volatile("s_waitcnt vmcnt(%2)" : "+v"(a), "+v"(b) : "n"(N) : "memory"); }
DI void pin2(u32x2& a) { asm volatile("" : "+v"(a)::"memory"); }
DI void wait0_2(u32x2& a) { asm volatile("s_waitcnt vmcnt(0)" : "+v"(a)::"memory"); }
DI void opaque(unsigned& a) { asm volatile("" : "+v"(a)); }
DI void opaquef(float& a) { asm volatile("" : "+v"(a)); }
DI int lane_now() { int l; asm volatile("v_mbcnt_lo_u32_b32 %0, -1, 0\n\tv_mbcnt_hi_u32_b32 %0, -1, %0" : "=v"(l)); return l; }
DI void opaque16(f32x16& a) { asm volatile("" : "+v"(a)); }
DI void opaquec(f32x2& a) { float p = a[0], q = a[1]; asm volatile("" : "+v"(p), "+v"(q)); a[0] = p; a[1] = q; }
DI bf16x8 tr_frag(unsigned a0, unsigned a1) {
  bf16x4 lo = __builtin_amdgcn_ds_read_tr16_b64_v4bf16((LAS bf16x4*)a0);
  bf16x4 hi = __builtin_amdgcn_ds_read_tr16_b64_v4bf16((LAS bf16x4*)a1);
  return __builtin_shufflevector(lo, hi, 0, 1, 2, 3, 4, 5, 6, 7);
}
DI u32x4 lds_rd(unsigned a) { return *(LAS u32x4*)a; }
DI void lds_wr(unsigned a, u32x4 v) { *(LAS u32x4*)a = v; }
DI f32x2 lds_rd8(unsigned a) { return *(LAS f32x2*)a; }
DI void lds_wr8(unsigned a, f32x2 v) { *(LAS f32x2*)a = v; }
DI u32x4 norm16(u32x4 v, float a, float b) {
  unsigned a0 = v[0], a1 = v[1], a2 = v[2], a3 = v[3], t0, t1, t2, t3;
  asm("v_lshlrev_b32 %4, 16, %0\n\tv_lshlrev_b32 %5, 16, %1\n\tv_lshlrev_b32 %6, 16, %2\n\tv_lshlrev_b32 %7, 16, %3\n\t"
      "v_and_b32 %0, 0xffff0000, %0\n\tv_and_b32 %1, 0xffff0000, %1\n\tv_and_b32 %2, 0xffff0000, %2\n\tv_and_b32 %3, 0xffff0000, %3\n\t"
      "v_fma_f32 %4, %4, %8, %9\n\tv_fma_f32 %5, %5, %8, %9\n\tv_fma_f32 %6, %6, %8, %9\n\tv_fma_f32 %7, %7, %8, %9\n\t"
      "v_fma_f32 %0, %0, %8, %9\n\tv_fma_f32 %1, %1, %8, %9\n\tv_fma_f32 %2, %2, %8, %9\n\tv_fma_f32 %3, %3, %8, %9\n\t"
      "v_cvt_pk_bf16_f32 %0, %4, %0\n\tv_cvt_pk_bf16_f32 %1, %5, %1\n\tv_cvt_pk_bf16_f32 %2, %6, %2\n\tv_cvt_pk_bf16_f32 %3, %7, %3"
      : "+v"(a0), "+v"(a1), "+v"(a2), "+v"(a3), "=&v"(t0), "=&v"(t1), "=&v"(t2), "=&v"(t3) : "v"(a), "v"(b));
  return u32x4{a0, a1, a2, a3};
}
DI unsigned norm4(unsigned a, float sc, float bi) {
  unsigned t;
  asm volatile("v_lshlrev_b32 %1, 16, %0\n\tv_and_b32 %0, 0xffff0000, %0\n\tv_fma_f32 %1, %1, %2, %3\n\tv_fma_f32 %0, %0, %2, %3\n\tv_cvt_pk_bf16_f32 %0, %1, %0"
               : "+v"(a), "=&v"(t) : "v"(sc), "v"(bi));
  return a;
}
template <int CTRL> DI float dpp_add(float a) {
  return a + __builtin_bit_cast(float, __builtin_amdgcn_update_dpp(0, __builtin_bit_cast(int, a), CTRL, 0xf, 0xf, true));
}
DI f32x2 cfma(f32x2 a, f32x2 b, f32x2 c) {
  f32x2 o;
  o[0] = __builtin_fmaf(a[0], b[0], __builtin_fmaf(-a[1], b[1], c[0]));
  o[1] = __builtin_fmaf(a[0], b[1], __builtin_fmaf(a[1], b[0], c[1]));
  return o;
}
DI f32x2 csq(f32x2 a) { f32x2 o; o[0] = __builtin_fmaf(a[0], a[0], -a[1] * a[1]); o[1] = 2.f * a[0] * a[1]; return o; }
DI float up32(float v) {
  auto t = __builtin_amdgcn_permlane32_swap(0u, __float_as_uint(v), false, false);
  return __uint_as_float(t[0]);
}

#define STAMP(i)
#define RSTAMP(i)
#define STAMP_OUT()

constexpr int p1_tile(int s) { return s < 6 ? (s & 1) : s / 3; }
constexpr int p1_kt(int s) { return s < 6 ? (s >> 1) : s % 3; }
constexpr bool has_wdma(int s) { return s + 3 <= 35; }
constexpr int EPI0 = 61;
constexpr int st_slot(int T, int o) { int r = EPI0 + 33 * T + (o ? 33 : 17); return (r % 12 <= 7) ? r / 12 : r / 12 + 1; }
constexpr int n_actst(int s) { int n = 0; for (int T = 0; T < 6; ++T) for (int o = 0; o < 2; ++o) if (st_slot(T, o) == s) ++n; return n; }
constexpr int mlp_nadma(int s) { return (s >= 15 && s + 3 <= 35) ? 2 : 0; }
constexpr int mlp_wait(int s) { return s + 2 > 35 ? 63 : (has_wdma(s) ? 3 : 0) + mlp_nadma(s) + n_actst(s - 1) + n_actst(s); }
constexpr int mlp_flag_slot(int T) { return st_slot(T, 1) + 3; }
constexpr bool ssm_a0(int s) { return s >= 16 && s <= 33; }
constexpr bool ssm_a1(int s) { return s >= 15 && s <= 32; }
constexpr int ssm_late_st(int s) { return (s >= 11 && s <= 23 && (s - 11) % 6 == 0) ? 2 : 0; }
constexpr int ssm_early_st(int s) { return (s >= 12 && s <= 24 && (s - 12) % 6 == 0) ? 2 : 0; }
constexpr int ssm_ndma(int s) { return (has_wdma(s) ? 3 : 0) + (ssm_a0(s) ? 1 : 0) + (ssm_a1(s) ? 1 : 0); }
constexpr int ssm_wait(int s) { return ssm_ndma(s) + ssm_late_st(s - 1) + ssm_late_st(s) + ssm_early_st(s); }

struct Params {
  const u16 *x, *lre, *lim, *bre, *bim, *cre, *cim, *ldt, *w1, *b1, *w2;
  char* ws;
};

template <bool SSM>
DI void body(const Params& P, const unsigned lds0, const int c, const int j) {
  char* const ws = P.ws;
  const int tid = threadIdx.x, lane = tid & 63;
  const int w = __builtin_amdgcn_readfirstlane(tid >> 6);
  const int rg = w & 3, set = w >> 2;
  const int r = lane & 31, h = lane >> 5;
  unsigned stampv = 0; (void)stampv;
  RSTAMP(60); STAMP(0);
  const int gq = SSM ? 4 : j / 3, mi = j - 3 * gq;

  u32x4 xr[2][9];
  unsigned aux[3], epoch, epoch2 = 0;
  const char* xb = (const char*)P.x + (size_t)(128 * c + 16 * w) * (D * 2);
  const unsigned xoff = (lane >> 3) * (D * 2) + (lane & 7) * 16;
  const unsigned xoff1 = xoff + 8 * (D * 2);
  __builtin_amdgcn_s_setprio(2);
  static_for<0, 9>([&](auto I) INL { xr[0][I.value] = gload16<I.value * 128>(xb, xoff); });
  constexpr int NAUX = SSM ? 5 : 2;
  if constexpr (SSM) {
    const unsigned o = (192 * mi + (tid < 192 ? tid : 0)) * 2;
    aux[0] = gload2(P.lre, o); aux[1] = gload2(P.lim, o); aux[2] = gload2(P.ldt, o);
    epoch2 = gload4_sc1(ws + WS_EPOCH + (80 + mi) * 128, 0u);
  } else {
    aux[0] = gload2(P.b1, (384 * j + (tid < 384 ? tid : 0)) * 2);
  }
  epoch = gload4_sc1(ws + WS_EPOCH + (c * 5 + gq) * 128, 0u);

  unsigned pvo = lane * 16;
  unsigned voff1, voff2;
  const char *wa[2], *wb[3][2], *acm[3];
  if constexpr (SSM) {
    voff1 = voff2 = (lane >> 3) * (D * 2) + (((lane & 7) ^ ((4 * (w & 1) + (lane >> 4)) & 7)) * 16);
    wa[0] = (const char*)P.bre + (size_t)(192 * mi + 8 * w) * (D * 2);
    wa[1] = (const char*)P.bim + (size_t)(192 * mi + 8 * w) * (D * 2);
  } else {
    const unsigned swz = ((lane & 7) ^ (((lane >> 4) & 1) << 2)) * 16;
    voff1 = (lane >> 3) * (F * 2) + swz;
    voff2 = (lane >> 3) * (D * 2) + swz;
    wa[0] = wa[1] = (const char*)P.w1 + ((size_t)(8 * w) * F + 384 * j) * 2;
  }
#pragma unroll
  for (int k = 0; k < 3; ++k) {
    const int mm = (mi + k >= 3) ? mi + k - 3 : mi + k;
    if constexpr (SSM) {
      wb[k][0] = (const char*)P.cre + (size_t)(192 * mi + 8 * w) * (D * 2) + 384 * mm;
      wb[k][1] = (const char*)P.cim + (size_t)(192 * mi + 8 * w) * (D * 2) + 384 * mm;
    } else {
      wb[k][0] = wb[k][1] = (const char*)P.w2 + ((size_t)(384 * (3 * gq + mm) + 8 * w) * D + 192 * mi) * 2;
    }
    acm[k] = ws + WS_ACT + (size_t)((c * 15 + 3 * gq + mm) * 6) * 16384 + w * 1024;
  }
  auto dmaW = [&](int s, int i) INL {
    const unsigned dst = lds0 + (s & 3) * SLOT + w * 1024 + i * 8192;
    const int u = s - 18;
    if constexpr (SSM) {
      if (s < 18) gldsW(wa[p1_tile(s) & 1] + (64 * (p1_tile(s) >> 1)) * (D * 2) + (192 * p1_kt(s) + 64 * i) * 2, voff1, dst);
      else gldsW(wb[(u % 6) >> 1][u & 1] + (64 * i) * (D * 2) + 128 * (u / 6), voff2, dst);
    } else {
      if (s < 18) gldsW(wa[0] + ((size_t)(192 * p1_kt(s) + 64 * i) * F + 64 * p1_tile(s)) * 2, voff1, dst);
      else gldsW(wb[u % 3][0] + ((size_t)(64 * (u / 3)) * D + 64 * i) * 2, voff2, dst);
    }
  };
  float rs[2], nb[2];
  unsigned xw_ad[2];
  xw_ad[0] = lds0 + X_OFF + (16 * w + (lane >> 3)) * 128 + (((lane & 7) ^ (lane >> 4)) * 16);
  xw_ad[1] = (xw_ad[0] ^ 64) + 1024;
  auto xwrite1 = [&](int ps, int i) INL { lds_wr(xw_ad[ps] + (i % 3) * 16384, xr[ps][i]); };
  static_for<0, 2>([&](auto PI) INL {
    constexpr int ps = PI.value;
    f32x4 s1 = {}, s2 = {}, s3 = {};
    const unsigned selv = (((lane >> 4) & 3) == (lane & 3)) ? 0x3f803f80u : 0u;
    const u32x4 sel = {selv, selv, selv, selv};
    static_for<0, 9>([&](auto GI) INL {
      constexpr int i = GI.value;
      if constexpr (ps == 0) { xr[1][i] = gload16<i * 128>(xb, xoff1); if constexpr (i == 8) __builtin_amdgcn_s_setprio(0); wait1<9 + NAUX>(xr[0][i]); }
      else wait1<8 - i + 3>(xr[1][i]);
      const u32x2 t0 = {xr[ps][i][0], xr[ps][i][1]}, t1 = {xr[ps][i][2], xr[ps][i][3]};
      s2 = __builtin_amdgcn_mfma_f32_4x4x4bf16_1k(__builtin_bit_cast(s16x4, t0), __builtin_bit_cast(s16x4, t0), s2, 0, 0, 0);
      s3 = __builtin_amdgcn_mfma_f32_4x4x4bf16_1k(__builtin_bit_cast(s16x4, t1), __builtin_bit_cast(s16x4, t1), s3, 0, 0, 0);
      s1 = MFMA16(BF8(sel), BF8(xr[ps][i]), s1);
      SCHED();
    });
    if constexpr (ps == 0) { dmaW(0, 0); dmaW(0, 1); dmaW(0, 2); }
    s2 += s3;
    const float e0 = s2[0], e1 = s2[1], e2 = s2[2], e3 = s2[3];
    const float lo2 = (lane & 1) ? e1 : e0, hi2 = (lane & 1) ? e3 : e2;
    float a2 = (lane & 2) ? hi2 : lo2;
    const float f0 = s1[0], f1 = s1[1], f2 = s1[2], f3 = s1[3];
    const float lo1 = (lane & 16) ? f1 : f0, hi1 = (lane & 16) ? f3 : f2;
    float a1 = (lane & 32) ? hi1 : lo1;
    a1 = dpp_add<0xB1>(a1); a2 = dpp_add<0xB1>(a2);
    a1 = dpp_add<0x4E>(a1); a2 = dpp_add<0x4E>(a2);
    a1 = dpp_add<0x141>(a1); a2 = dpp_add<0x141>(a2);
    const float m = a1 * (1.f / D);
    const float var = fmaxf(a2 * (1.f / D) - m * m, 0.f);
    const float rstd = __builtin_amdgcn_rsqf(var + 1e-5f);
    rs[ps] = SSM ? rstd : rstd * LOG2E;
    nb[ps] = -m * rs[ps];
#pragma unroll
    for (int i = 0; i < 3; ++i) { xr[ps][i] = norm16(xr[ps][i], rs[ps], nb[ps]); xwrite1(ps, i); }
    if constexpr (ps == 0) { STAMP(1); }
  });
  STAMP(2);
  dmaW(1, 0); dmaW(1, 1); dmaW(1, 2);
  opaque(aux[0]); opaque(epoch);
  const unsigned tag = __builtin_amdgcn_readfirstlane(epoch) + 1u;
  unsigned tag2 = 0;
  if constexpr (SSM) {
    opaque(aux[1]); opaque(aux[2]); opaque(epoch2);
    tag2 = __builtin_amdgcn_readfirstlane(epoch2) + 1u;
    if (w < 3) {
      const float lr = bflo(aux[0]), li = bflo(aux[1]);
      const float dt = __builtin_amdgcn_exp2f(bflo(aux[2]) * LOG2E);
      const float mag = __builtin_amdgcn_exp2f(dt * lr * LOG2E);
      const float rev = __builtin_amdgcn_fractf(dt * li * 0.15915494309189535f);
      f32x2 L = {mag * __builtin_amdgcn_cosf(rev), mag * __builtin_amdgcn_sinf(rev)};
      const float inv = 2.f / (lr * lr + li * li);
      const float nr = L[0] - 1.f, ni = L[1];
      const f32x2 c2 = {(nr * lr + ni * li) * inv, (ni * lr - nr * li) * inv};
      const f32x2 L16 = csq(csq(csq(csq(L))));
      const f32x2 L128 = csq(csq(csq(L16)));
      lds_wr8(lds0 + LT_OFF + tid * 8, L); lds_wr8(lds0 + LT_OFF + 1536 + tid * 8, c2);
      lds_wr8(lds0 + LT_OFF + 2 * 1536 + tid * 8, L16); lds_wr8(lds0 + LT_OFF + 3 * 1536 + tid * 8, L128);
    }
    if (mi == 0 && (lane & 7) == 0) {
#pragma unroll
      for (int ps = 0; ps < 2; ++ps) *(f32x2*)(ws + WS_STATS + (size_t)(128 * c + 16 * w + 8 * ps + (lane >> 3)) * 8) = f32x2{rs[ps], nb[ps]};
    }
  } else {
    if (tid < 384) *(LAS float*)(lds0 + TAB_OFF + tid * 4) = bflo(aux[0]) * LOG2E;
  }
  const int rperm = 16 * ((r >> 2) & 1) + (r & 3) + 4 * (r >> 3);
  bf16x8 xn[36];
  unsigned xn_ad[4];
  { const int row = 32 * rg + (SSM ? rperm : r); xn_ad[0] = lds0 + X_OFF + row * 128 + ((h ^ ((row >> 1) & 7)) * 16); }
  xn_ad[1] = xn_ad[0] ^ 32; xn_ad[2] = xn_ad[0] ^ 64; xn_ad[3] = xn_ad[0] ^ 96;
  auto xn_read = [&](int rd, int ks) INL { xn[12 * rd + ks] = BF8(lds_rd(xn_ad[ks & 3] + (ks >> 2) * 16384)); };

  const int G1 = (lane >> 4) & 1, q = (lane >> 2) & 3, p4 = lane & 3;
  unsigned rb1[4], rb2[4][2], ab[2];
  if constexpr (SSM) {
    const int row = 32 * set + r;
    rb1[0] = lds0 + row * 128 + ((h ^ ((row >> 1) & 7)) * 16);
    rb1[1] = rb1[0] ^ 32; rb1[2] = rb1[0] ^ 64; rb1[3] = rb1[0] ^ 96;
    opaque(rb1[0]); opaque(rb1[1]); opaque(rb1[2]); opaque(rb1[3]);
  } else {
    rb1[0] = lds0 + (8 * h + q) * 128 + (((set * 4 + G1 * 2 + (p4 >> 1)) ^ (((q >> 1) & 1) << 2)) * 16) + (p4 & 1) * 8;
    rb1[1] = rb1[0] + 2 * SLOT;
    opaque(rb1[0]); opaque(rb1[1]);
  }
  unsigned tabb = SSM ? lds0 + TAB_OFF + (32 * set + r) * 8 : lds0 + TAB_OFF + (32 * set + 4 * h) * 4;
  opaque(tabb);
  auto bias_init = [&](int T) INL {
    f32x16 a;
#pragma unroll
    for (int g = 0; g < 4; ++g) {
      f32x4 v = *(LAS f32x4*)(tabb + (64 * T + 8 * g) * 4);
      a[4 * g] = v[0]; a[4 * g + 1] = v[1]; a[4 * g + 2] = v[2]; a[4 * g + 3] = v[3];
    }
    return a;
  };
  auto wfrag = [&](int n) INL {
    const int s = n / 12, ks = n % 12;
    if constexpr (SSM) {
      if (s < 18) return BF8(lds_rd(rb1[ks & 3] + (s & 1) * SLOT + (ks >> 2) * 8192));
      else return BF8(lds_rd(rb2[ks / 3][(s >> 1) & 1] + (s & 1) * SLOT + (ks % 3) * 8192));
    } else {
      if (s < 18) { unsigned a = rb1[(s >> 1) & 1] + (s & 1) * SLOT + ks * 2048; return tr_frag(a, a + 512); }
      else { unsigned a = rb2[0][(s >> 1) & 1] + (s & 1) * SLOT + (ks % 3) * 8192 + (ks / 3) * 2048; return tr_frag(a, a + 1024); }
    }
  };
  auto afrag = [&](int kq) INL {
    if constexpr (SSM) {
      const int u = kq >> 2, k4 = kq & 3;
      const int v = (k4 < 2) ? u - 1 : u;
      const unsigned off = ((v + 1) % 3) * 16384 + ((k4 < 2) ? 8192 : 0) + (k4 & 1) * 256;
      return tr_frag(ab[0] + off, ab[1] + off);
    } else {
      return BF8(lds_rd(ab[0] + ((kq >> 2) & 3) * 16384 + (kq & 3) * 4096));
    }
  };
  WAIT_L0();
  STAMP(3);
  WAIT_V(3);
  BAR();
  STAMP(4);

  f32x16 acc[SSM ? 4 : 3];
  f32x16 oacc[3];
  u32x4 actp[2], gr[2];
  float tn, tr, tprev, tprev2;
  f32x2 Lam, Lx, ee, cc, g0, g1, g2, g3;
  bf16x8 fr[4], af[3];
  u32x2 flg;
  const f32x16 zero = {};
  const char* amine = ws + WS_ACT + (size_t)((c * 15 + j) * 6) * 16384 + set * 8192 + rg * (SSM ? 2048 : 1024);
  const char* flagb = ws + WS_FLAG + (c * 16 + 3 * gq) * 128;
  if constexpr (!SSM) acc[0] = bias_init(0);
  fr[0] = wfrag(0); fr[1] = wfrag(1); fr[2] = wfrag(2);

  static_for<0, 432>([&](auto NI) INL {
    constexpr int n = NI.value;
    constexpr int s = n / 12, ks = n % 12;
    if constexpr (n == 216 - 4) {
      const int lane = lane_now(), r = lane & 31, h = lane >> 5, G1 = (lane >> 4) & 1, q = (lane >> 2) & 3, p4 = lane & 3;
      if constexpr (SSM) {
        const int rperm = 16 * ((r >> 2) & 1) + (r & 3) + 4 * (r >> 3);
        const int row = 32 * set + rperm;
        rb2[0][0] = lds0 + row * 128 + ((h ^ ((row >> 1) & 7)) * 16);
        rb2[1][0] = rb2[0][0] ^ 32; rb2[2][0] = rb2[0][0] ^ 64; rb2[3][0] = rb2[0][0] ^ 96;
#pragma unroll
        for (int k = 0; k < 4; ++k) { rb2[k][1] = rb2[k][0] + 2 * SLOT; opaque(rb2[k][0]); opaque(rb2[k][1]); }
        const int o = p4 >> 1;
        ab[0] = lds0 + ARING + rg * 2048 + o * 1024 + G1 * 512 + (((2 * h) ^ (2 * G1 + o)) * 64) + q * 16 + (p4 & 1) * 8;
        ab[1] = ab[0] ^ 64;
        opaque(ab[0]); opaque(ab[1]);
      } else {
        const int dch = 4 * (p4 & 1) + 2 * G1 + (p4 >> 1);
        rb2[0][0] = lds0 + (4 * h + q) * 128 + (((set * 4 + (dch >> 1)) ^ (((q >> 1) & 1) << 2)) * 16) + (dch & 1) * 8;
        rb2[0][1] = rb2[0][0] + 2 * SLOT;
        ab[0] = lds0 + ARING + rg * 1024 + lane * 16;
        opaque(rb2[0][0]); opaque(rb2[0][1]); opaque(ab[0]);
      }
    }
    if constexpr (SSM && n == 15 * 12) pvo = lane_now() * 16;
    if constexpr (SSM && ks == 9 && (s & 1) == 1 && s < 17) {
#pragma unroll
      for (int k = 0; k < 4; ++k) { rb1[k] += (s & 2) ? -2 * SLOT : 2 * SLOT; opaque(rb1[k]); }
    }
    constexpr int pb = (s >= 6) ? (s - 6) / 6 : 0, ph = (s >= 6 && s < 24) ? (s - 6) % 6 : -1;
    if constexpr (ks == 0) {
      constexpr int pub = SSM ? ((s >= 14 && s <= 26 && (s - 14) % 6 == 0) ? (s - 14) / 6 + 1 : 0)
                              : (s == mlp_flag_slot(0) ? 1 : s == mlp_flag_slot(1) ? 2 : s == mlp_flag_slot(2) ? 3 : s == mlp_flag_slot(3) ? 4 : s == mlp_flag_slot(4) ? 5 : s == mlp_flag_slot(5) ? 6 : 0);
      constexpr bool poll = SSM ? (s >= 16 && s <= 28 && (s - 16) % 6 == 0) : (s >= 15 && s <= 30 && (s - 15) % 3 == 0);
      if constexpr (pub > 0 || poll) {
        if (w == 0) {
          if constexpr (poll) { const int l = lane_now(); flg = gload8_sc1(flagb, (l < 3 ? l : 0) * 128); }
          if constexpr (pub > 0) gstore8_sc1(flagb + mi * 128, 0u, u32x2{(unsigned)pub, tag});
        }
      }
      if constexpr (SSM && ssm_early_st(s) > 0) {
        const int l = lane_now();
        const unsigned so = (l ^ (4 * (2 * (l >> 5) + 1))) * 16;
        gstore_sc1(amine + (2 * (pb - 1)) * 16384 + 1024, so, actp[0]);
        gstore_sc1(amine + (2 * (pb - 1) + 1) * 16384 + 1024, so, actp[1]);
      }
      if constexpr (SSM && ph == 3) {
        const int l = lane_now(), r = l & 31;
        const int k0 = 4 * rg + 2 * (l >> 5);
        const char* gb = ws + WS_GRAN + (size_t)((mi * 3 + pb) * 16) * 1024 + set * 512;
        gr[0] = gload16_sc1(gb, (c - 1 - k0 >= 0 ? c - 1 - k0 : 0) * 1024 + r * 16);
        gr[1] = gload16_sc1(gb, (c - 2 - k0 >= 0 ? c - 2 - k0 : 0) * 1024 + r * 16);
      }
    }
    if constexpr (s < 6) {
      constexpr int kt = s >> 1;
      if constexpr ((s & 1) == 0) {
        if constexpr (ks == 0) { xn_read(kt, 0); xn_read(kt, 1); xn_read(kt, 2); xn_read(kt, 3); }
        else if constexpr (ks + 3 < 12) xn_read(kt, ks + 3);
      } else if constexpr (kt < 2) {
        if constexpr (ks % 2 == 0 && ks <= 8) { xwrite1((ks / 2) / 3, 3 * (kt + 1) + (ks / 2) % 3); SCHED(); }
        if constexpr (ks == 9) { xwrite1(1, 3 * (kt + 1) + 2); SCHED(); }
      }
    }
    if constexpr (s == 0 && ks < 3) dmaW(2, ks);
    if constexpr (s == 0 && ks >= 3 && ks < 6) dmaW(3, ks - 3);
    if constexpr (s > 0 && ks % 3 == 0 && ks <= 6 && has_wdma(s)) dmaW(s + 3, ks / 3);
    if constexpr (SSM) {
      constexpr int v = s - 16;
      if constexpr (ks == 2 && ssm_a0(s)) gldsA(acm[(v % 6) >> 1] + (2 * (v / 6) + (v & 1)) * 16384 + 8192, pvo, lds0 + ARING + ((v + 1) % 3) * 16384 + w * 1024);
      if constexpr (ks == 5 && ssm_a1(s)) gldsA(acm[((v + 1) % 6) >> 1] + (2 * ((v + 1) / 6) + ((v + 1) & 1)) * 16384, pvo, lds0 + ARING + ((v + 1) % 3) * 16384 + 8192 + w * 1024);
    } else {
      constexpr int u = s + 3 - 18;
      if constexpr ((ks == 2 || ks == 5) && mlp_nadma(s) > 0) gldsA(acm[u % 3] + (u / 3) * 16384 + (ks / 3) * 8192, pvo, lds0 + ARING + (u & 3) * 16384 + w * 1024 + (ks / 3) * 8192);
    }
    if constexpr (n + 3 < 432) fr[(n + 3) & 3] = wfrag(n + 3);
    if constexpr (SSM) {
      constexpr int u = s - 18;
      if constexpr (s >= 18 && (ks == 3 || ks == 6)) af[(4 * u + ks / 3 + 1) % 3] = afrag(4 * u + ks / 3 + 1);
      if constexpr (s >= 17 && s < 35 && (ks == 9 || ks == 10)) af[(4 * (u + 1) + ks - 9) % 3] = afrag(4 * (u + 1) + ks - 9);
    } else {
      if constexpr (n + 3 >= 216 && n + 3 < 432 && (n + 3) % 3 == 0) af[((n + 3 - 216) / 3) & 1] = afrag((n + 3 - 216) / 3);
    }
    if constexpr (s < 18) {
      constexpr int T = p1_tile(s), kt = p1_kt(s);
      if constexpr (SSM) { acc[T & 3] = MFMA32(xn[12 * kt + ks], fr[n & 3], (kt == 0 && ks == 0) ? zero : acc[T & 3]); opaque16(acc[T & 3]); }
      else { acc[T % 3] = MFMA32(fr[n & 3], xn[12 * kt + ks], acc[T % 3]); opaque16(acc[T % 3]); }
      if constexpr (s < 4) {
        constexpr int v = n - 24 * kt;
        static_for<0, 24>([&](auto DI_) INL {
          constexpr int d = DI_.value;
          if constexpr ((d * 5) / 6 == v) {
            constexpr int ch = d >> 2, ps = ch / 3, i = 3 * (kt + 1) + ch % 3;
            xr[ps][i][d & 3] = norm4(xr[ps][i][d & 3], rs[ps], nb[ps]);
          }
        });
      }
      if constexpr (!SSM && ks == 9 && s < 17 && p1_kt(s + 1) == 0) acc[p1_tile(s + 1) % 3] = bias_init(p1_tile(s + 1));
    } else {
      oacc[ks % 3] = MFMA32(fr[n & 3], af[SSM ? ((n - 216) / 3) % 3 : ((n - 216) / 3) & 1], n < 219 ? zero : oacc[ks % 3]);
      opaque16(oacc[ks % 3]);
    }
    if constexpr (!SSM) {
      if constexpr (n >= EPI0 && n < EPI0 + 198) {
        constexpr int T = (n - EPI0) / 33, u = (n - EPI0) % 33, e = u >> 1;
        const f32x16& pa = acc[T % 3];
        if constexpr ((u & 1) == 0) {
          if constexpr (e < 16) { tn = __builtin_amdgcn_exp2f(pa[e]); opaquef(tn); }
          if constexpr (e >= 1) {
            const float a = __builtin_fmaf(-pa[e - 1], tr, pa[e - 1]);
            if constexpr (((e - 1) & 1) == 0) { tprev = a; opaquef(tprev); }
            else { unsigned pk = pk2(tprev, a); opaque(pk); actp[(e - 1) >> 3][((e - 1) >> 1) & 3] = pk; }
          }
        } else { tr = __builtin_amdgcn_rcpf(__builtin_fmaf(tn, __builtin_fmaf(tn, 0.5f, 1.f), 1.f)); opaquef(tr); }
      }
      static_for<0, 12>([&](auto TI) INL {
        constexpr int T = TI.value >> 1, o = TI.value & 1;
        if constexpr (st_slot(T, o) == s && ks == 7) gstore_sc1(amine + T * 16384 + o * 4096, pvo, actp[o]);
      });
    } else if constexpr (ph >= 0) {
      const f32x16& ur = acc[(2 * pb) & 3];
      const f32x16& ui = acc[(2 * pb + 1) & 3];
      const unsigned lt = tabb + pb * 512;
      if constexpr (ph == 0) {
        if constexpr (ks == 0) Lam = lds_rd8(lt);
        if constexpr (ks == 7) Lx = lds_rd8(lt + 2 * 1536);
        if constexpr (ks == 1) { ee = f32x2{ur[0], ui[0]}; }
        if constexpr (ks >= 2 && ks <= 9) {
          constexpr int t0 = 1 + 2 * (ks - 2);
          ee = cfma(Lam, ee, f32x2{ur[t0], ui[t0]});
          if constexpr (t0 + 1 < 16) ee = cfma(Lam, ee, f32x2{ur[t0 + 1], ui[t0 + 1]});
          opaquec(ee);
        }
        if constexpr (ks == 10) {
          const f32x2 elo = {up32(ee[0]), up32(ee[1])};
          lds_wr8(tabb + (WT_OFF - TAB_OFF) + (lane_now() >> 5) * 2048 + rg * 512, cfma(Lx, elo, ee));
        }
      }
      if constexpr (ph == 1 && ks == 0) {
        if (rg == 3) {
          const unsigned wt = tabb + (WT_OFF - TAB_OFF) + 2048;
          const f32x2 w0 = lds_rd8(wt), w1 = lds_rd8(wt + 512), w2 = lds_rd8(wt + 1024), w3 = lds_rd8(wt + 1536);
          const f32x2 L32 = csq(Lx);
          const f32x2 E = cfma(L32, cfma(L32, cfma(L32, w0, w1), w2), w3);
          gstore_sc1(ws + WS_GRAN + (size_t)((mi * 3 + pb) * 16 + c) * 1024 + set * 512, (lane_now() & 31) * 16,
                     u32x4{__float_as_uint(E[0]), tag2, __float_as_uint(E[1]), tag2});
        }
      }
      if constexpr (ph == 3) {
        if constexpr (ks == 6) cc = lds_rd8(lt + 3 * 1536);
        if constexpr (ks == 9) {
          SCHED();
          wait2<ssm_ndma(s)>(gr[0], gr[1]);
          const int l = lane_now(), r = l & 31, h = l >> 5;
          const int k0 = 4 * rg + 2 * h;
          const bool v0 = c - 1 - k0 >= 0, v1 = c - 2 - k0 >= 0;
          unsigned spins = 0;
          while (!__all((!v0 || (gr[0][1] == tag2 && gr[0][3] == tag2)) && (!v1 || (gr[1][1] == tag2 && gr[1][3] == tag2)))) {
            if (++spins > 200000u) break;
            __builtin_amdgcn_s_sleep(1);
            const char* gb = ws + WS_GRAN + (size_t)((mi * 3 + pb) * 16) * 1024 + set * 512;
            gr[0] = gload16_sc1(gb, (v0 ? c - 1 - k0 : 0) * 1024 + r * 16);
            gr[1] = gload16_sc1(gb, (v1 ? c - 2 - k0 : 0) * 1024 + r * 16);
            wait2<0>(gr[0], gr[1]);
          }
          const f32x2 a0 = {v0 ? __uint_as_float(gr[0][0]) : 0.f, v0 ? __uint_as_float(gr[0][2]) : 0.f};
          const f32x2 a1 = {v1 ? __uint_as_float(gr[1][0]) : 0.f, v1 ? __uint_as_float(gr[1][2]) : 0.f};
          lds_wr8(tabb + (GT_OFF - TAB_OFF) + (2 * rg + h) * 512, cfma(cc, a1, a0));
          cc = csq(cc);
          opaquec(cc);
        }
      }
      if constexpr (ph == 4 && ks <= 4) {
        const unsigned gt = tabb + (GT_OFF - TAB_OFF), wt = tabb + (WT_OFF - TAB_OFF) + 2048;
        if constexpr (ks == 0) { ee = lds_rd8(gt + 7 * 512); g0 = lds_rd8(gt + 6 * 512); g1 = lds_rd8(gt + 5 * 512); g2 = lds_rd8(gt + 4 * 512); g3 = lds_rd8(gt + 3 * 512); }
        if constexpr (ks == 1) {
          ee = cfma(cc, cfma(cc, ee, g0), g1); opaquec(ee);
          g0 = lds_rd8(gt + 2 * 512); g1 = lds_rd8(gt + 512);
        }
        if constexpr (ks == 2) {
          ee = cfma(cc, cfma(cc, ee, g2), g3); opaquec(ee);
          g2 = lds_rd8(gt); g3 = lds_rd8(wt);
        }
        if constexpr (ks == 3) {
          ee = cfma(cc, cfma(cc, cfma(cc, ee, g0), g1), g2); opaquec(ee);
          g0 = lds_rd8(wt + 512); g1 = lds_rd8(wt + 1024); g2 = lds_rd8(wt - 2048 + rg * 512); Lx = lds_rd8(lt + 2 * 1536);
        }
        if constexpr (ks == 4) {
          cc = lds_rd8(lt + 1536); Lam = lds_rd8(lt);
          const f32x2 L32 = csq(Lx);
          if (rg > 0) ee = cfma(L32, ee, g3);
          if (rg > 1) ee = cfma(L32, ee, g0);
          if (rg > 2) ee = cfma(L32, ee, g1);
          const f32x2 e2 = cfma(Lx, ee, g2);
          const bool up = lane_now() >= 32;
          ee = f32x2{up ? e2[0] : ee[0], up ? e2[1] : ee[1]}; opaquec(ee);
        }
      }
      if constexpr ((ph == 4 && ks >= 5) || ph == 5) {
        constexpr int v = (ph == 4) ? ks - 5 : ks + 7;
        constexpr int t = (v < 8) ? v : (v < 14) ? v - (v >= 9) : v - 3;
        constexpr bool act_ = !(v == 8 || v == 14 || v == 15);
        constexpr int tt = (v < 8) ? v : (v < 14) ? v - 1 : v - 3;
        (void)t;
        if constexpr (act_) {
          ee = cfma(Lam, ee, f32x2{ur[tt], ui[tt]});
          const float ore = __builtin_fmaf(cc[0], ee[0], -cc[1] * ee[1]);
          const float oim = __builtin_fmaf(-cc[0], ee[1], -cc[1] * ee[0]);
          opaquec(ee);
          if constexpr ((tt & 1) == 0) { tprev = ore; tprev2 = oim; opaquef(tprev); opaquef(tprev2); }
          else {
            unsigned p0 = pk2(tprev, ore), p1 = pk2(tprev2, oim); opaque(p0); opaque(p1);
            if constexpr (tt < 8) { gr[0][tt >> 1] = p0; gr[1][tt >> 1] = p1; }
            else { actp[0][(tt >> 1) & 3] = p0; actp[1][(tt >> 1) & 3] = p1; }
          }
        }
        if constexpr (v == 14 || v == 15) {
          const int l = lane_now();
          const unsigned so = (l ^ (8 * (l >> 5))) * 16;
          gstore_sc1(amine + (2 * pb + (v - 14)) * 16384, so, gr[v - 14]);
        }
      }
    }
    if constexpr (n == 8) { SCHED(); WAIT_V(6); BAR(); }
    if constexpr (ks == 11 && s < 35) {
      SCHED();
      if constexpr (s == 1 || s == 3 || (SSM && (ph == 0 || ph == 3 || s >= 17))) WAIT_L0();
      if constexpr (s == 0) WAIT_V(3); else WAIT_V(SSM ? ssm_wait(s) : mlp_wait(s));
      constexpr bool chk = SSM ? (s >= 16 && s <= 28 && (s - 16) % 6 == 0) : (s >= 15 && s <= 30 && (s - 15) % 3 == 0);
      if constexpr (chk) {
        if (w == 0) {
          constexpr unsigned need = SSM ? (s - 16) / 6 + 1 : (s - 15) / 3 + 1;
          if constexpr (!SSM && n_actst(s - 1) > 0) WAIT_V(mlp_wait(s) - n_actst(s - 1));
          pin2(flg);
          unsigned spins = 0;
          while (!__all(flg[1] == tag && flg[0] >= need)) {
            if (++spins > 200000u) break;
            __builtin_amdgcn_s_sleep(1);
            const int l = lane_now();
            flg = gload8_sc1(flagb, (l < 3 ? l : 0) * 128);
            wait0_2(flg);
          }
        }
      }
      BAR();
      STAMP(5 + s);
    }
    SCHED();
  });
  {
    const char* pbase = ws + WS_PART + (size_t)((c * 4 + rg) * 9 + 3 * mi) * 20480 + gq * 4096 + set * 2048;
#pragma unroll
    for (int cb = 0; cb < 3; ++cb)
#pragma unroll
      for (int hh = 0; hh < 2; ++hh) {
        u32x4 v;
#pragma unroll
        for (int e = 0; e < 4; ++e) v[e] = pk2(oacc[cb][8 * hh + 2 * e], oacc[cb][8 * hh + 2 * e + 1]);
        gstore_sc1(pbase + cb * 20480 + hh * 1024, pvo, v);
      }
  }
  if (w == 0 && pvo == 0) {
    if (mi == 0) *(unsigned*)(ws + WS_EPOCH + (c * 5 + gq) * 128) = tag;
    if (SSM && c == 15) *(unsigned*)(ws + WS_EPOCH + (80 + mi) * 128) = tag2;
  }
  STAMP(40);
  STAMP(41);
  RSTAMP(61);
  STAMP_OUT();
}

__global__ __launch_bounds__(512, 2) void main_kernel(const Params P) {
  __shared__ __attribute__((aligned(16))) char smem[163840];
  const unsigned lds0 = (unsigned)(size_t)(LAS char*)smem;
  const int b = blockIdx.x, xcd = b & 7, idx = b >> 3;
  const int j = idx >> 1, c = 2 * xcd + (idx & 1);
  if (j < 12) body<false>(P, lds0, c, j);
  else if (j < 15) body<true>(P, lds0, c, j);
  else {
    const unsigned t = c * 512 + threadIdx.x;
    unsigned sink = 0, v[14];
#pragma unroll
    for (int k = 0; k < 5; ++k) {
      unsigned l = k * 8192 + t + 512;
      const unsigned jj = l % 12, kk = (l / 12) % 576, T = (l / (12 * 576) + 1) % 6;
      v[k] = *(const unsigned*)((const char*)P.w1 + (size_t)kk * (F * 2) + jj * 768 + T * 128);
    }
    const unsigned tl = t < 5184 ? t : t - 5184;
    v[5] = *(const unsigned*)((const char*)P.bre + tl * 128);
    v[6] = *(const unsigned*)((const char*)P.bim + tl * 128);
#pragma unroll
    for (int k = 0; k < 5; ++k) {
      unsigned l = k * 8192 + t;
      const unsigned ln = l % 576, jj = (l / 576) % 12, T = l / (12 * 576);
      v[7 + k] = *(const unsigned*)((const char*)P.w2 + (size_t)(384 * jj + 64 * T) * (D * 2) + ln * 128);
    }
    v[12] = *(const unsigned*)((const char*)P.cre + tl * 128);
    v[13] = *(const unsigned*)((const char*)P.cim + tl * 128);
    { const unsigned l = 40960 + (t & 511); const unsigned ln = l % 576, jj = (l / 576) % 12, T = l / (12 * 576);
      sink = *(const unsigned*)((const char*)P.w2 + (size_t)(384 * jj + 64 * T) * (D * 2) + ln * 128); }
#pragma unroll
    for (int k = 0; k < 14; ++k) sink ^= v[k];
    asm volatile("" ::"v"(sink));
  }
}

__global__ __launch_bounds__(256) void reduce_kernel(const u16* __restrict__ x, const u16* __restrict__ dskip, char* __restrict__ ws,
                                                     float* __restrict__ out) {
  __shared__ __attribute__((aligned(16))) float tile[2][2][32][36];
  const int blk = (blockIdx.x & 7) * 72 + (blockIdx.x >> 3);
  const int tid = threadIdx.x, lane = tid & 63, wv = tid >> 6;
  const int crg = blk / 9, gp = blk % 9;
  const char* pp = ws + WS_PART + (size_t)blk * 20480 + tid * 16;
  u32x4 v[5];
#pragma unroll
  for (int k = 0; k < 5; ++k) v[k] = *(const u32x4*)(pp + k * 4096);
  const int orow = tid >> 4, oc = (tid & 15) * 4;
  const int m0 = crg * 32 + orow, d0 = gp * 64 + oc;
  u32x2 xv[2]; f32x2 st[2];
#pragma unroll
  for (int i = 0; i < 2; ++i) {
    xv[i] = *(const u32x2*)(x + (size_t)(m0 + 16 * i) * D + d0);
    st[i] = *(const f32x2*)(ws + WS_STATS + (size_t)(m0 + 16 * i) * 8);
  }
  const u32x2 dv = *(const u32x2*)(dskip + d0);
  float z[8], y[8];
#pragma unroll
  for (int e = 0; e < 4; ++e) {
    z[2 * e] = (bflo(v[0][e]) + bflo(v[1][e])) + (bflo(v[2][e]) + bflo(v[3][e]));
    z[2 * e + 1] = (bfhi(v[0][e]) + bfhi(v[1][e])) + (bfhi(v[2][e]) + bfhi(v[3][e]));
    y[2 * e] = bflo(v[4][e]); y[2 * e + 1] = bfhi(v[4][e]);
  }
#pragma unroll
  for (int e = 0; e < 8; ++e) z[e] = z[e] * 0.693147180559945f * __builtin_amdgcn_rcpf(1.f + __builtin_amdgcn_exp2f(-z[e]));
  {
    const int g2 = wv >> 1, s = wv & 1, row = lane & 31, col = 16 * (lane >> 5) + 8 * s;
    *(f32x4*)&tile[g2][0][row][col] = f32x4{y[0], y[1], y[2], y[3]};
    *(f32x4*)&tile[g2][0][row][col + 4] = f32x4{y[4], y[5], y[6], y[7]};
    *(f32x4*)&tile[g2][1][row][col] = f32x4{z[0], z[1], z[2], z[3]};
    *(f32x4*)&tile[g2][1][row][col + 4] = f32x4{z[4], z[5], z[6], z[7]};
  }
  __syncthreads();
  const float dk[4] = {bflo(dv[0]), bfhi(dv[0]), bflo(dv[1]), bfhi(dv[1])};
#pragma unroll
  for (int i = 0; i < 2; ++i) {
    const f32x4 yv = *(const f32x4*)&tile[oc >> 5][0][orow + 16 * i][oc & 31];
    const f32x4 gv = *(const f32x4*)&tile[oc >> 5][1][orow + 16 * i][oc & 31];
    const float xx[4] = {bflo(xv[i][0]), bfhi(xv[i][0]), bflo(xv[i][1]), bfhi(xv[i][1])};
    f32x4 o;
#pragma unroll
    for (int e = 0; e < 4; ++e) o[e] = xx[e] + (yv[e] + (xx[e] * st[i][0] + st[i][1]) * dk[e]) * gv[e];
    *(f32x4*)(out + (size_t)(m0 + 16 * i) * D + d0) = o;
  }
}

extern "C" void kernel_launch(void* const* d_in, const int* in_sizes, int n_in, void* d_out, int out_size, void* d_ws, size_t ws_size,
                              hipStream_t stream) {
  Params P;
  P.x = (const u16*)d_in[0]; P.lre = (const u16*)d_in[1]; P.lim = (const u16*)d_in[2];
  P.bre = (const u16*)d_in[3]; P.bim = (const u16*)d_in[4]; P.cre = (const u16*)d_in[5]; P.cim = (const u16*)d_in[6];
  P.ldt = (const u16*)d_in[7]; P.w1 = (const u16*)d_in[10]; P.b1 = (const u16*)d_in[11]; P.w2 = (const u16*)d_in[12];
  P.ws = (char*)d_ws;
  main_kernel<<<256, 512, 0, stream>>>(P);
  reduce_kernel<<<576, 256, 0, stream>>>(P.x, (const u16*)d_in[8], P.ws, (float*)d_out);
}
```

```cpp
#include <hip/hip_runtime.h>
#include <hip/hip_bf16.h>
#pragma clang diagnostic ignored "-Wint-to-pointer-cast"

typedef unsigned short u16;
typedef __attribute__((ext_vector_type(2))) __bf16 bf16x2;
typedef __attribute__((ext_vector_type(4))) __bf16 bf16x4;
typedef __attribute__((ext_vector_type(8))) __bf16 bf16x8;
typedef __attribute__((ext_vector_type(4))) short s16x4;
typedef __attribute__((ext_vector_type(2))) float f32x2;
typedef __attribute__((ext_vector_type(4))) float f32x4;
typedef __attribute__((ext_vector_type(16))) float f32x16;
typedef __attribute__((ext_vector_type(2))) unsigned u32x2;
typedef __attribute__((ext_vector_type(4))) unsigned u32x4;
#define LAS __attribute__((address_space(3)))
#define GAS __attribute__((address_space(1)))
#define DI __device__ __forceinline__
#define INL __attribute__((always_inline))

constexpr int S = 2048, D = 576, F = 4608;
constexpr float LOG2E = 1.44269504088896f;
constexpr int SLOT = 24576;
constexpr int X_OFF = 4 * SLOT;
constexpr int ARING = X_OFF;
constexpr int TAB_OFF = ARING + 49152;
constexpr int LT_OFF = TAB_OFF;
constexpr int WT_OFF = TAB_OFF + 6144;
constexpr int GT_OFF = TAB_OFF + 10240;
constexpr size_t WS_STATS = 0;
constexpr size_t WS_EPOCH = 16384;
constexpr size_t WS_FLAG = 32768;
constexpr size_t WS_PART = 65536;
constexpr size_t WS_GRAN = 15u << 20;
constexpr size_t WS_ACT = 16u << 20;

DI float bflo(unsigned v) { return __uint_as_float(v << 16); }
DI float bfhi(unsigned v) { return __uint_as_float(v & 0xffff0000u); }
DI unsigned pk2(float a, float b) { f32x2 v = {a, b}; return __builtin_bit_cast(unsigned, __builtin_convertvector(v, bf16x2)); }
#define WAIT_V(n) asm volatile("s_waitcnt vmcnt(%0)" ::"n"(n) : "memory")
#define WAIT_L0() asm volatile("s_waitcnt lgkmcnt(0)" ::: "memory")
#define BAR() asm volatile("s_barrier" ::: "memory")
#define SCHED() __builtin_amdgcn_sched_barrier(0)
#define BF8(x) __builtin_bit_cast(bf16x8, (x))
#define MFMA16(a, b, c) __builtin_amdgcn_mfma_f32_16x16x32_bf16((a), (b), (c), 0, 0, 0)
#define MFMA32(a, b, c) __builtin_amdgcn_mfma_f32_32x32x16_bf16((a), (b), (c), 0, 0, 0)
template <int I> struct IC { static constexpr int value = I; };
template <int B, int E, class Fn> DI void static_for(Fn&& f) { if constexpr (B < E) { f(IC<B>{}); static_for<B + 1, E>(f); } }

DI void gldsW(const void* sbase, unsigned voff, unsigned lds_addr) {
  asm volatile("s_mov_b32 m0, %2\n\ts_nop 0\n\tglobal_load_lds_dwordx4 %0, %1" ::"v"(voff), "s"(sbase), "s"(lds_addr) : "memory");
}
DI void gldsA(const void* sbase, unsigned voff, unsigned lds_addr) {
  asm volatile("s_mov_b32 m0, %2\n\ts_nop 0\n\tglobal_load_lds_dwordx4 %0, %1 sc1" ::"v"(voff), "s"(sbase), "s"(lds_addr) : "memory");
}
DI void glds4(const void* sbase, unsigned voff, unsigned lds_addr) {
  asm volatile("s_mov_b32 m0, %2\n\ts_nop 0\n\tglobal_load_lds_dword %0, %1" ::"v"(voff), "s"(sbase), "s"(lds_addr) : "memory");
}
DI void gstore_sc1(const void* sbase, unsigned voff, u32x4 v) {
  asm volatile("global_store_dwordx4 %0, %1, %2 sc1\n\ts_nop 1" ::"v"(voff), "v"(v), "s"(sbase) : "memory");
}
DI void gstore8_sc1(const void* sbase, unsigned voff, u32x2 v) {
  asm volatile("global_store_dwordx2 %0, %1, %2 sc1\n\ts_nop 1" ::"v"(voff), "v"(v), "s"(sbase) : "memory");
}
template <int OFF> DI u32x4 gload16(const void* sbase, unsigned voff) {
  u32x4 v; asm volatile("global_load_dwordx4 %0, %1, %2 offset:%3" : "=v"(v) : "v"(voff), "s"(sbase), "n"(OFF) : "memory"); return v;
}
DI u32x4 gload16_sc1(const void* sbase, unsigned voff) {
  u32x4 v; asm volatile("global_load_dwordx4 %0, %1, %2 sc1" : "=v"(v) : "v"(voff), "s"(sbase) : "memory"); return v;
}
DI u32x2 gload8_sc1(const void* sbase, unsigned voff) {
  u32x2 v; asm volatile("global_load_dwordx2 %0, %1, %2 sc1" : "=v"(v) : "v"(voff), "s"(sbase) : "memory"); return v;
}
DI unsigned gload4_sc1(const void* sbase, unsigned voff) {
  unsigned v; asm volatile("global_load_dword %0, %1, %2 sc1" : "=v"(v) : "v"(voff), "s"(sbase) : "memory"); return v;
}
DI unsigned gload2(const void* sbase, unsigned voff) {
  unsigned v; asm volatile("global_load_ushort %0, %1, %2" : "=v"(v) : "v"(voff), "s"(sbase) : "memory"); return v;
}
template <int N> DI void wait1(u32x4& a) { asm volatile("s_waitcnt vmcnt(%1)" : "+v"(a) : "n"(N) : "memory"); }
template <int N> DI void wait2(u32x4& a, u32x4& b) { asm volatile("s_waitcnt vmcnt(%2)" : "+v"(a), "+v"(b) : "n"(N) : "memory"); }
DI void pin2(u32x2& a) { asm volatile("" : "+v"(a)::"memory"); }
DI void wait0_2(u32x2& a) { asm volatile("s_waitcnt vmcnt(0)" : "+v"(a)::"memory"); }
DI void opaque(unsigned& a) { asm volatile("" : "+v"(a)); }
DI void opaquef(float& a) { asm volatile("" : "+v"(a)); }
DI int lane_now() { int l; asm volatile("v_mbcnt_lo_u32_b32 %0, -1, 0\n\tv_mbcnt_hi_u32_b32 %0, -1, %0" : "=v"(l)); return l; }
DI void opaque16(f32x16& a) { asm volatile("" : "+v"(a)); }
DI void opaquec(f32x2& a) { float p = a[0], q = a[1]; asm volatile("" : "+v"(p), "+v"(q)); a[0] = p; a[1] = q; }
DI bf16x8 tr_frag(unsigned a0, unsigned a1) {
  bf16x4 lo = __builtin_amdgcn_ds_read_tr16_b64_v4bf16((LAS bf16x4*)a0);
  bf16x4 hi = __builtin_amdgcn_ds_read_tr16_b64_v4bf16((LAS bf16x4*)a1);
  return __builtin_shufflevector(lo, hi, 0, 1, 2, 3, 4, 5, 6, 7);
}
DI u32x4 lds_rd(unsigned a) { return *(LAS u32x4*)a; }
DI void lds_wr(unsigned a, u32x4 v) { *(LAS u32x4*)a = v; }
DI f32x2 lds_rd8(unsigned a) { return *(LAS f32x2*)a; }
DI void lds_wr8(unsigned a, f32x2 v) { *(LAS f32x2*)a = v; }
DI u32x4 norm16(u32x4 v, float a, float b) {
  unsigned a0 = v[0], a1 = v[1], a2 = v[2], a3 = v[3], t0, t1, t2, t3;
  asm("v_lshlrev_b32 %4, 16, %0\n\tv_lshlrev_b32 %5, 16, %1\n\tv_lshlrev_b32 %6, 16, %2\n\tv_lshlrev_b32 %7, 16, %3\n\t"
      "v_and_b32 %0, 0xffff0000, %0\n\tv_and_b32 %1, 0xffff0000, %1\n\tv_and_b32 %2, 0xffff0000, %2\n\tv_and_b32 %3, 0xffff0000, %3\n\t"
      "v_fma_f32 %4, %4, %8, %9\n\tv_fma_f32 %5, %5, %8, %9\n\tv_fma_f32 %6, %6, %8, %9\n\tv_fma_f32 %7, %7, %8, %9\n\t"
      "v_fma_f32 %0, %0, %8, %9\n\tv_fma_f32 %1, %1, %8, %9\n\tv_fma_f32 %2, %2, %8, %9\n\tv_fma_f32 %3, %3, %8, %9\n\t"
      "v_cvt_pk_bf16_f32 %0, %4, %0\n\tv_cvt_pk_bf16_f32 %1, %5, %1\n\tv_cvt_pk_bf16_f32 %2, %6, %2\n\tv_cvt_pk_bf16_f32 %3, %7, %3"
      : "+v"(a0), "+v"(a1), "+v"(a2), "+v"(a3), "=&v"(t0), "=&v"(t1), "=&v"(t2), "=&v"(t3) : "v"(a), "v"(b));
  return u32x4{a0, a1, a2, a3};
}
DI unsigned norm4(unsigned a, float sc, float bi) {
  unsigned t;
  asm volatile("v_lshlrev_b32 %1, 16, %0\n\tv_and_b32 %0, 0xffff0000, %0\n\tv_fma_f32 %1, %1, %2, %3\n\tv_fma_f32 %0, %0, %2, %3\n\tv_cvt_pk_bf16_f32 %0, %1, %0"
               : "+v"(a), "=&v"(t) : "v"(sc), "v"(bi));
  return a;
}
template <int CTRL> DI float dpp_add(float a) {
  return a + __builtin_bit_cast(float, __builtin_amdgcn_update_dpp(0, __builtin_bit_cast(int, a), CTRL, 0xf, 0xf, true));
}
DI f32x2 cfma(f32x2 a, f32x2 b, f32x2 c) {
  f32x2 o;
  o[0] = __builtin_fmaf(a[0], b[0], __builtin_fmaf(-a[1], b[1], c[0]));
  o[1] = __builtin_fmaf(a[0], b[1], __builtin_fmaf(a[1], b[0], c[1]));
  return o;
}
DI f32x2 csq(f32x2 a) { f32x2 o; o[0] = __builtin_fmaf(a[0], a[0], -a[1] * a[1]); o[1] = 2.f * a[0] * a[1]; return o; }
DI float up32(float v) {
  auto t = __builtin_amdgcn_permlane32_swap(0u, __float_as_uint(v), false, false);
  return __uint_as_float(t[0]);
}

#define STAMP(i)
#define RSTAMP(i)
#define STAMP_OUT()

constexpr int p1_tile(int s) { return s < 6 ? (s & 1) : s / 3; }
constexpr int p1_kt(int s) { return s < 6 ? (s >> 1) : s % 3; }
constexpr bool has_wdma(int s) { return s + 3 <= 35; }
constexpr int n_pf(int s) { return (s >= 0 && s <= 6) ? 1 : 0; }
constexpr int EPI0 = 61;
constexpr int st_slot(int T, int o) { int r = EPI0 + 33 * T + (o ? 33 : 17); return (r % 12 <= 7) ? r / 12 : r / 12 + 1; }
constexpr int n_actst(int s) { int n = 0; for (int T = 0; T < 6; ++T) for (int o = 0; o < 2; ++o) if (st_slot(T, o) == s) ++n; return n; }
constexpr int mlp_nadma(int s) { return (s >= 15 && s + 3 <= 35) ? 2 : 0; }
constexpr int mlp_wait(int s) { return s + 2 > 35 ? 63 : (has_wdma(s) ? 3 : 0) + mlp_nadma(s) + n_actst(s - 1) + n_actst(s) + n_pf(s - 1) + n_pf(s); }
constexpr int mlp_flag_slot(int T) { return st_slot(T, 1) + 3; }
constexpr bool ssm_a0(int s) { return s >= 16 && s <= 33; }
constexpr bool ssm_a1(int s) { return s >= 15 && s <= 32; }
constexpr int ssm_late_st(int s) { return (s >= 11 && s <= 23 && (s - 11) % 6 == 0) ? 2 : 0; }
constexpr int ssm_early_st(int s) { return (s >= 12 && s <= 24 && (s - 12) % 6 == 0) ? 2 : 0; }
constexpr int ssm_ndma(int s) { return (has_wdma(s) ? 3 : 0) + (ssm_a0(s) ? 1 : 0) + (ssm_a1(s) ? 1 : 0); }
constexpr int ssm_wait(int s) { return ssm_ndma(s) + ssm_late_st(s - 1) + ssm_late_st(s) + ssm_early_st(s) + n_pf(s - 1) + n_pf(s); }

struct Params {
  const u16 *x, *lre, *lim, *bre, *bim, *cre, *cim, *ldt, *w1, *b1, *w2;
  char* ws;
};

template <bool SSM>
DI void body(const Params& P, const unsigned lds0, const int c, const int j) {
  char* const ws = P.ws;
  const int tid = threadIdx.x, lane = tid & 63;
  const int w = __builtin_amdgcn_readfirstlane(tid >> 6);
  const int rg = w & 3, set = w >> 2;
  const int r = lane & 31, h = lane >> 5;
  unsigned stampv = 0; (void)stampv;
  RSTAMP(60); STAMP(0);
  const int gq = SSM ? 4 : j / 3, mi = j - 3 * gq;

  u32x4 xr[2][9];
  unsigned aux[3], epoch, epoch2 = 0;
  const char* xb = (const char*)P.x + (size_t)(128 * c + 16 * w) * (D * 2);
  const unsigned xoff = (lane >> 3) * (D * 2) + (lane & 7) * 16;
  const unsigned xoff1 = xoff + 8 * (D * 2);
  __builtin_amdgcn_s_setprio(2);
  static_for<0, 9>([&](auto I) INL { xr[0][I.value] = gload16<I.value * 128>(xb, xoff); });
  constexpr int NAUX = SSM ? 5 : 2;
  if constexpr (SSM) {
    const unsigned o = (192 * mi + (tid < 192 ? tid : 0)) * 2;
    aux[0] = gload2(P.lre, o); aux[1] = gload2(P.lim, o); aux[2] = gload2(P.ldt, o);
    epoch2 = gload4_sc1(ws + WS_EPOCH + (80 + mi) * 128, 0u);
  } else {
    aux[0] = gload2(P.b1, (384 * j + (tid < 384 ? tid : 0)) * 2);
  }
  epoch = gload4_sc1(ws + WS_EPOCH + (c * 5 + gq) * 128, 0u);

  unsigned pvo = lane * 16;
  unsigned voff1, voff2;
  const char *wa[2], *wb[3][2], *acm[3];
  if constexpr (SSM) {
    voff1 = voff2 = (lane >> 3) * (D * 2) + (((lane & 7) ^ ((4 * (w & 1) + (lane >> 4)) & 7)) * 16);
    wa[0] = (const char*)P.bre + (size_t)(192 * mi + 8 * w) * (D * 2);
    wa[1] = (const char*)P.bim + (size_t)(192 * mi + 8 * w) * (D * 2);
  } else {
    const unsigned swz = ((lane & 7) ^ (((lane >> 4) & 1) << 2)) * 16;
    voff1 = (lane >> 3) * (F * 2) + swz;
    voff2 = (lane >> 3) * (D * 2) + swz;
    wa[0] = wa[1] = (const char*)P.w1 + ((size_t)(8 * w) * F + 384 * j) * 2;
  }
#pragma unroll
  for (int k = 0; k < 3; ++k) {
    const int mm = (mi + k >= 3) ? mi + k - 3 : mi + k;
    if constexpr (SSM) {
      wb[k][0] = (const char*)P.cre + (size_t)(192 * mi + 8 * w) * (D * 2) + 384 * mm;
      wb[k][1] = (const char*)P.cim + (size_t)(192 * mi + 8 * w) * (D * 2) + 384 * mm;
    } else {
      wb[k][0] = wb[k][1] = (const char*)P.w2 + ((size_t)(384 * (3 * gq + mm) + 8 * w) * D + 192 * mi) * 2;
    }
    acm[k] = ws + WS_ACT + (size_t)((c * 15 + 3 * gq + mm) * 6) * 16384 + w * 1024;
  }
  auto dmaW = [&](int s, int i) INL {
    const unsigned dst = lds0 + (s & 3) * SLOT + w * 1024 + i * 8192;
    const int u = s - 18;
    if constexpr (SSM) {
      if (s < 18) gldsW(wa[p1_tile(s) & 1] + (64 * (p1_tile(s) >> 1)) * (D * 2) + (192 * p1_kt(s) + 64 * i) * 2, voff1, dst);
      else gldsW(wb[(u % 6) >> 1][u & 1] + (64 * i) * (D * 2) + 128 * (u / 6), voff2, dst);
    } else {
      if (s < 18) gldsW(wa[0] + ((size_t)(192 * p1_kt(s) + 64 * i) * F + 64 * p1_tile(s)) * 2, voff1, dst);
      else gldsW(wb[u % 3][0] + ((size_t)(64 * (u / 3)) * D + 64 * i) * 2, voff2, dst);
    }
  };
  float rs[2], nb[2];
  unsigned xw_ad[2];
  xw_ad[0] = lds0 + X_OFF + (16 * w + (lane >> 3)) * 128 + (((lane & 7) ^ (lane >> 4)) * 16);
  xw_ad[1] = (xw_ad[0] ^ 64) + 1024;
  auto xwrite1 = [&](int ps, int i) INL { lds_wr(xw_ad[ps] + (i % 3) * 16384, xr[ps][i]); };
  static_for<0, 2>([&](auto PI) INL {
    constexpr int ps = PI.value;
    f32x4 s1 = {}, s2 = {}, s3 = {};
    const unsigned selv = (((lane >> 4) & 3) == (lane & 3)) ? 0x3f803f80u : 0u;
    const u32x4 sel = {selv, selv, selv, selv};
    static_for<0, 9>([&](auto GI) INL {
      constexpr int i = GI.value;
      if constexpr (ps == 0) { xr[1][i] = gload16<i * 128>(xb, xoff1); if constexpr (i == 8) __builtin_amdgcn_s_setprio(0); wait1<9 + NAUX>(xr[0][i]); }
      else wait1<8 - i + 4>(xr[1][i]);
      const u32x2 t0 = {xr[ps][i][0], xr[ps][i][1]}, t1 = {xr[ps][i][2], xr[ps][i][3]};
      s2 = __builtin_amdgcn_mfma_f32_4x4x4bf16_1k(__builtin_bit_cast(s16x4, t0), __builtin_bit_cast(s16x4, t0), s2, 0, 0, 0);
      s3 = __builtin_amdgcn_mfma_f32_4x4x4bf16_1k(__builtin_bit_cast(s16x4, t1), __builtin_bit_cast(s16x4, t1), s3, 0, 0, 0);
      s1 = MFMA16(BF8(sel), BF8(xr[ps][i]), s1);
      SCHED();
    });
    if constexpr (ps == 0) {
      dmaW(0, 0); dmaW(0, 1); dmaW(0, 2);
      unsigned long long pc; unsigned len;
      if constexpr (SSM) asm volatile("s_getpc_b64 %0\nfk_ssm_beg:\n\ts_mov_b32 %1, fk_ssm_end-fk_ssm_beg" : "=s"(pc), "=s"(len));
      else asm volatile("s_getpc_b64 %0\nfk_mlp_beg:\n\ts_mov_b32 %1, fk_mlp_end-fk_mlp_beg" : "=s"(pc), "=s"(len));
      const unsigned co = (w * 32 + (lane >> 1)) * 128;
      glds4((const void*)pc, co < len ? co : 0u, lds0 + TAB_OFF + 14336);
    }
    s2 += s3;
    const float e0 = s2[0], e1 = s2[1], e2 = s2[2], e3 = s2[3];
    const float lo2 = (lane & 1) ? e1 : e0, hi2 = (lane & 1) ? e3 : e2;
    float a2 = (lane & 2) ? hi2 : lo2;
    const float f0 = s1[0], f1 = s1[1], f2 = s1[2], f3 = s1[3];
    const float lo1 = (lane & 16) ? f1 : f0, hi1 = (lane & 16) ? f3 : f2;
    float a1 = (lane & 32) ? hi1 : lo1;
    a1 = dpp_add<0xB1>(a1); a2 = dpp_add<0xB1>(a2);
    a1 = dpp_add<0x4E>(a1); a2 = dpp_add<0x4E>(a2);
    a1 = dpp_add<0x141>(a1); a2 = dpp_add<0x141>(a2);
    const float m = a1 * (1.f / D);
    const float var = fmaxf(a2 * (1.f / D) - m * m, 0.f);
    const float rstd = __builtin_amdgcn_rsqf(var + 1e-5f);
    rs[ps] = SSM ? rstd : rstd * LOG2E;
    nb[ps] = -m * rs[ps];
#pragma unroll
    for (int i = 0; i < 3; ++i) { xr[ps][i] = norm16(xr[ps][i], rs[ps], nb[ps]); xwrite1(ps, i); }
    if constexpr (ps == 0) { STAMP(1); }
  });
  STAMP(2);
  dmaW(1, 0); dmaW(1, 1); dmaW(1, 2);
  opaque(aux[0]); opaque(epoch);
  const unsigned tag = __builtin_amdgcn_readfirstlane(epoch) + 1u;
  unsigned tag2 = 0;
  if constexpr (SSM) {
    opaque(aux[1]); opaque(aux[2]); opaque(epoch2);
    tag2 = __builtin_amdgcn_readfirstlane(epoch2) + 1u;
    if (w < 3) {
      const float lr = bflo(aux[0]), li = bflo(aux[1]);
      const float dt = __builtin_amdgcn_exp2f(bflo(aux[2]) * LOG2E);
      const float mag = __builtin_amdgcn_exp2f(dt * lr * LOG2E);
      const float rev = __builtin_amdgcn_fractf(dt * li * 0.15915494309189535f);
      f32x2 L = {mag * __builtin_amdgcn_cosf(rev), mag * __builtin_amdgcn_sinf(rev)};
      const float inv = 2.f / (lr * lr + li * li);
      const float nr = L[0] - 1.f, ni = L[1];
      const f32x2 c2 = {(nr * lr + ni * li) * inv, (ni * lr - nr * li) * inv};
      const f32x2 L16 = csq(csq(csq(csq(L))));
      const f32x2 L128 = csq(csq(csq(L16)));
      lds_wr8(lds0 + LT_OFF + tid * 8, L); lds_wr8(lds0 + LT_OFF + 1536 + tid * 8, c2);
      lds_wr8(lds0 + LT_OFF + 2 * 1536 + tid * 8, L16); lds_wr8(lds0 + LT_OFF + 3 * 1536 + tid * 8, L128);
    }
    if (mi == 0 && (lane & 7) == 0) {
#pragma unroll
      for (int ps = 0; ps < 2; ++ps) *(f32x2*)(ws + WS_STATS + (size_t)(128 * c + 16 * w + 8 * ps + (lane >> 3)) * 8) = f32x2{rs[ps], nb[ps]};
    }
  } else {
    if (tid < 384) *(LAS float*)(lds0 + TAB_OFF + tid * 4) = bflo(aux[0]) * LOG2E;
  }
  const int rperm = 16 * ((r >> 2) & 1) + (r & 3) + 4 * (r >> 3);
  bf16x8 xn[36];
  unsigned xn_ad[4];
  { const int row = 32 * rg + (SSM ? rperm : r); xn_ad[0] = lds0 + X_OFF + row * 128 + ((h ^ ((row >> 1) & 7)) * 16); }
  xn_ad[1] = xn_ad[0] ^ 32; xn_ad[2] = xn_ad[0] ^ 64; xn_ad[3] = xn_ad[0] ^ 96;
  auto xn_read = [&](int rd, int ks) INL { xn[12 * rd + ks] = BF8(lds_rd(xn_ad[ks & 3] + (ks >> 2) * 16384)); };

  const int G1 = (lane >> 4) & 1, q = (lane >> 2) & 3, p4 = lane & 3;
  unsigned rb1[4], rb2[4][2], ab[2];
  if constexpr (SSM) {
    const int row = 32 * set + r;
    rb1[0] = lds0 + row * 128 + ((h ^ ((row >> 1) & 7)) * 16);
    rb1[1] = rb1[0] ^ 32; rb1[2] = rb1[0] ^ 64; rb1[3] = rb1[0] ^ 96;
    opaque(rb1[0]); opaque(rb1[1]); opaque(rb1[2]); opaque(rb1[3]);
  } else {
    rb1[0] = lds0 + (8 * h + q) * 128 + (((set * 4 + G1 * 2 + (p4 >> 1)) ^ (((q >> 1) & 1) << 2)) * 16) + (p4 & 1) * 8;
    rb1[1] = rb1[0] + 2 * SLOT;
    opaque(rb1[0]); opaque(rb1[1]);
  }
  unsigned tabb = SSM ? lds0 + TAB_OFF + (32 * set + r) * 8 : lds0 + TAB_OFF + (32 * set + 4 * h) * 4;
  opaque(tabb);
  auto bias_init = [&](int T) INL {
    f32x16 a;
#pragma unroll
    for (int g = 0; g < 4; ++g) {
      f32x4 v = *(LAS f32x4*)(tabb + (64 * T + 8 * g) * 4);
      a[4 * g] = v[0]; a[4 * g + 1] = v[1]; a[4 * g + 2] = v[2]; a[4 * g + 3] = v[3];
    }
    return a;
  };
  auto wfrag = [&](int n) INL {
    const int s = n / 12, ks = n % 12;
    if constexpr (SSM) {
      if (s < 18) return BF8(lds_rd(rb1[ks & 3] + (s & 1) * SLOT + (ks >> 2) * 8192));
      else return BF8(lds_rd(rb2[ks / 3][(s >> 1) & 1] + (s & 1) * SLOT + (ks % 3) * 8192));
    } else {
      if (s < 18) { unsigned a = rb1[(s >> 1) & 1] + (s & 1) * SLOT + ks * 2048; return tr_frag(a, a + 512); }
      else { unsigned a = rb2[0][(s >> 1) & 1] + (s & 1) * SLOT + (ks % 3) * 8192 + (ks / 3) * 2048; return tr_frag(a, a + 1024); }
    }
  };
  auto afrag = [&](int kq) INL {
    if constexpr (SSM) {
      const int u = kq >> 2, k4 = kq & 3;
      const int v = (k4 < 2) ? u - 1 : u;
      const unsigned off = ((v + 1) % 3) * 16384 + ((k4 < 2) ? 8192 : 0) + (k4 & 1) * 256;
      return tr_frag(ab[0] + off, ab[1] + off);
    } else {
      return BF8(lds_rd(ab[0] + ((kq >> 2) & 3) * 16384 + (kq & 3) * 4096));
    }
  };
  WAIT_L0();
  STAMP(3);
  WAIT_V(4);
  BAR();
  STAMP(4);

  f32x16 acc[SSM ? 4 : 3];
  f32x16 oacc[3];
  u32x4 actp[2], gr[2];
  float tn, tr, tprev, tprev2;
  f32x2 Lam, Lx, ee, cc, g0, g1, g2;
  bf16x8 fr[4], af[3];
  u32x2 flg;
  const f32x16 zero = {};
  const char* amine = ws + WS_ACT + (size_t)((c * 15 + j) * 6) * 16384 + set * 8192 + rg * (SSM ? 2048 : 1024);
  const char* flagb = ws + WS_FLAG + (c * 16 + 3 * gq) * 128;
  if constexpr (!SSM) acc[0] = bias_init(0);
  fr[0] = wfrag(0); fr[1] = wfrag(1);
  if constexpr (!SSM) fr[2] = wfrag(2);

  static_for<0, 432>([&](auto NI) INL {
    constexpr int n = NI.value;
    constexpr int s = n / 12, ks = n % 12;
    if constexpr (n == 216 - 4) {
      const int lane = lane_now(), r = lane & 31, h = lane >> 5, G1 = (lane >> 4) & 1, q = (lane >> 2) & 3, p4 = lane & 3;
      if constexpr (SSM) {
        const int rperm = 16 * ((r >> 2) & 1) + (r & 3) + 4 * (r >> 3);
        const int row = 32 * set + rperm;
        rb2[0][0] = lds0 + row * 128 + ((h ^ ((row >> 1) & 7)) * 16);
        rb2[1][0] = rb2[0][0] ^ 32; rb2[2][0] = rb2[0][0] ^ 64; rb2[3][0] = rb2[0][0] ^ 96;
#pragma unroll
        for (int k = 0; k < 4; ++k) { rb2[k][1] = rb2[k][0] + 2 * SLOT; opaque(rb2[k][0]); opaque(rb2[k][1]); }
        const int o = p4 >> 1;
        ab[0] = lds0 + ARING + rg * 2048 + o * 1024 + G1 * 512 + (((2 * h) ^ (2 * G1 + o)) * 64) + q * 16 + (p4 & 1) * 8;
        ab[1] = ab[0] ^ 64;
        opaque(ab[0]); opaque(ab[1]);
      } else {
        const int dch = 4 * (p4 & 1) + 2 * G1 + (p4 >> 1);
        rb2[0][0] = lds0 + (4 * h + q) * 128 + (((set * 4 + (dch >> 1)) ^ (((q >> 1) & 1) << 2)) * 16) + (dch & 1) * 8;
        rb2[0][1] = rb2[0][0] + 2 * SLOT;
        ab[0] = lds0 + ARING + rg * 1024 + lane * 16;
        opaque(rb2[0][0]); opaque(rb2[0][1]); opaque(ab[0]);
      }
    }
    if constexpr (SSM && n == 15 * 12) pvo = lane_now() * 16;
    if constexpr (SSM && ks == 10 && (s & 1) == 1 && s < 17) {
#pragma unroll
      for (int k = 0; k < 4; ++k) { rb1[k] += (s & 2) ? -2 * SLOT : 2 * SLOT; opaque(rb1[k]); }
    }
    constexpr int pb = (s >= 6) ? (s - 6) / 6 : 0, ph = (s >= 6 && s < 24) ? (s - 6) % 6 : -1;
    if constexpr (ks == 0) {
      constexpr int pub = SSM ? ((s >= 14 && s <= 26 && (s - 14) % 6 == 0) ? (s - 14) / 6 + 1 : 0)
                              : (s == mlp_flag_slot(0) ? 1 : s == mlp_flag_slot(1) ? 2 : s == mlp_flag_slot(2) ? 3 : s == mlp_flag_slot(3) ? 4 : s == mlp_flag_slot(4) ? 5 : s == mlp_flag_slot(5) ? 6 : 0);
      constexpr bool poll = SSM ? (s >= 16 && s <= 28 && (s - 16) % 6 == 0) : (s >= 14 && s <= 29 && (s - 14) % 3 == 0);
      if constexpr (poll) { if (w == 0) { const int l = lane_now(); flg = gload8_sc1(flagb, (l < 3 ? l : 0) * 128); } }
      if constexpr (pub > 0) { if (w == 1) gstore8_sc1(flagb + mi * 128, 0u, u32x2{(unsigned)pub, tag}); }
      if constexpr (SSM && ssm_early_st(s) > 0) {
        const int l = lane_now();
        const unsigned so = (l ^ (4 * (2 * (l >> 5) + 1))) * 16;
        gstore_sc1(amine + (2 * (pb - 1)) * 16384 + 1024, so, actp[0]);
        gstore_sc1(amine + (2 * (pb - 1) + 1) * 16384 + 1024, so, actp[1]);
      }
      if constexpr (SSM && ph == 3) {
        const int l = lane_now(), r = l & 31;
        const int k0 = 4 * rg + 2 * (l >> 5);
        const char* gb = ws + WS_GRAN + (size_t)((mi * 3 + pb) * 16) * 1024 + set * 512;
        gr[0] = gload16_sc1(gb, (c - 1 - k0 >= 0 ? c - 1 - k0 : 0) * 1024 + r * 16);
        gr[1] = gload16_sc1(gb, (c - 2 - k0 >= 0 ? c - 2 - k0 : 0) * 1024 + r * 16);
      }
    }
    if constexpr (s < 6) {
      constexpr int kt = s >> 1;
      if constexpr ((s & 1) == 0) {
        if constexpr (ks == 0) { xn_read(kt, 0); xn_read(kt, 1); xn_read(kt, 2); xn_read(kt, 3); }
        else if constexpr (ks + 3 < 12) xn_read(kt, ks + 3);
      } else if constexpr (kt < 2) {
        if constexpr (ks % 2 == 0 && ks <= 8) { xwrite1((ks / 2) / 3, 3 * (kt + 1) + (ks / 2) % 3); SCHED(); }
        if constexpr (ks == 9) { xwrite1(1, 3 * (kt + 1) + 2); SCHED(); }
      }
    }
    if constexpr (s == 0 && ks < 3) dmaW(2, ks);
    if constexpr (s == 0 && ks >= 3 && ks < 6) dmaW(3, ks - 3);
    if constexpr (s > 0 && ks % 3 == 0 && ks <= 6 && has_wdma(s)) dmaW(s + 3, ks / 3);
    if constexpr (SSM) {
      constexpr int v = s - 16;
      if constexpr (ks == 2 && ssm_a0(s)) gldsA(acm[(v % 6) >> 1] + (2 * (v / 6) + (v & 1)) * 16384 + 8192, pvo, lds0 + ARING + ((v + 1) % 3) * 16384 + w * 1024);
      if constexpr (ks == 5 && ssm_a1(s)) gldsA(acm[((v + 1) % 6) >> 1] + (2 * ((v + 1) / 6) + ((v + 1) & 1)) * 16384, pvo, lds0 + ARING + ((v + 1) % 3) * 16384 + 8192 + w * 1024);
    } else {
      constexpr int u = s + 3 - 18;
      if constexpr ((ks == 2 || ks == 5) && mlp_nadma(s) > 0) gldsA(acm[u % 3] + (u / 3) * 16384 + (ks / 3) * 8192, pvo, lds0 + ARING + (u & 3) * 16384 + w * 1024 + (ks / 3) * 8192);
    }
    if constexpr (s <= 6 && ks == 7) {
      const unsigned l = lane_now();
      constexpr int sl = s < 3 ? s : s - 3;
      if constexpr (SSM) {
        constexpr unsigned lim = s < 3 ? 72 : 108;
        unsigned id = sl * 32 + rg * 8 + (l >> 3); id = id < lim ? id : lim - 1;
        const u16* mat = s < 3 ? (set ? P.bim : P.bre) : (set ? P.cim : P.cre);
        glds4((const char*)mat + (size_t)(192 * mi + (s < 3 ? 64 : 0)) * (D * 2) + c * lim * 128, id * 128 + (l & 7) * 4, lds0 + TAB_OFF + 14336);
      } else if constexpr (s < 3) {
        unsigned id = sl * 64 + w * 8 + (l >> 3); id = id < 144 ? id : 143;
        glds4((const char*)P.w1 + (size_t)(36 * c) * (F * 2) + 768 * j + 256, (id >> 2) * (F * 2) + (id & 3) * 128 + (l & 7) * 4, lds0 + TAB_OFF + 14336);
      } else {
        unsigned id = sl * 64 + w * 8 + (l >> 3); id = id < 216 ? id : 215;
        glds4((const char*)P.w2 + (size_t)(384 * j) * (D * 2) + c * 216 * 128, id * 128 + (l & 7) * 4, lds0 + TAB_OFF + 14336);
      }
    }
    if constexpr (SSM && n <= 213) fr[(n + 2) & 3] = wfrag(n + 2);
    if constexpr ((!SSM || n >= 213) && n + 3 < 432) fr[(n + 3) & 3] = wfrag(n + 3);
    if constexpr (SSM) {
      constexpr int u = s - 18;
      if constexpr (s >= 18 && (ks == 3 || ks == 6)) af[(4 * u + ks / 3 + 1) % 3] = afrag(4 * u + ks / 3 + 1);
      if constexpr (s >= 17 && s < 35 && (ks == 9 || ks == 10)) af[(4 * (u + 1) + ks - 9) % 3] = afrag(4 * (u + 1) + ks - 9);
    } else {
      if constexpr (n + 3 >= 216 && n + 3 < 432 && (n + 3) % 3 == 0) af[((n + 3 - 216) / 3) & 1] = afrag((n + 3 - 216) / 3);
    }
    if constexpr (s < 18) {
      constexpr int T = p1_tile(s), kt = p1_kt(s);
      if constexpr (SSM) { acc[T & 3] = MFMA32(xn[12 * kt + ks], fr[n & 3], (kt == 0 && ks == 0) ? zero : acc[T & 3]); opaque16(acc[T & 3]); }
      else { acc[T % 3] = MFMA32(fr[n & 3], xn[12 * kt + ks], acc[T % 3]); opaque16(acc[T % 3]); }
      if constexpr (s < 4) {
        constexpr int v = n - 24 * kt;
        static_for<0, 24>([&](auto DI_) INL {
          constexpr int d = DI_.value;
          if constexpr ((d * 5) / 6 == v) {
            constexpr int ch = d >> 2, ps = ch / 3, i = 3 * (kt + 1) + ch % 3;
            xr[ps][i][d & 3] = norm4(xr[ps][i][d & 3], rs[ps], nb[ps]);
          }
        });
      }
      if constexpr (!SSM && ks == 9 && s < 17 && p1_kt(s + 1) == 0) acc[p1_tile(s + 1) % 3] = bias_init(p1_tile(s + 1));
    } else {
      oacc[ks % 3] = MFMA32(fr[n & 3], af[SSM ? ((n - 216) / 3) % 3 : ((n - 216) / 3) & 1], n < 219 ? zero : oacc[ks % 3]);
      opaque16(oacc[ks % 3]);
    }
    if constexpr (!SSM) {
      if constexpr (n >= EPI0 && n < EPI0 + 198) {
        constexpr int T = (n - EPI0) / 33, u = (n - EPI0) % 33, e = u >> 1;
        const f32x16& pa = acc[T % 3];
        if constexpr ((u & 1) == 0) {
          if constexpr (e < 16) { tn = __builtin_amdgcn_exp2f(pa[e]); opaquef(tn); }
          if constexpr (e >= 1) {
            const float a = __builtin_fmaf(-pa[e - 1], tr, pa[e - 1]);
            if constexpr (((e - 1) & 1) == 0) { tprev = a; opaquef(tprev); }
            else { unsigned pk = pk2(tprev, a); opaque(pk); actp[(e - 1) >> 3][((e - 1) >> 1) & 3] = pk; }
          }
        } else { tr = __builtin_amdgcn_rcpf(__builtin_fmaf(tn, __builtin_fmaf(tn, 0.5f, 1.f), 1.f)); opaquef(tr); }
      }
      static_for<0, 12>([&](auto TI) INL {
        constexpr int T = TI.value >> 1, o = TI.value & 1;
        if constexpr (st_slot(T, o) == s && ks == 7) gstore_sc1(amine + T * 16384 + o * 4096, pvo, actp[o]);
      });
    } else if constexpr (ph >= 0) {
      f32x16& ur = acc[(2 * pb) & 3];
      f32x16& ui = acc[(2 * pb + 1) & 3];
      const unsigned lt = tabb + pb * 512;
      if constexpr (ph == 0) {
        if constexpr (ks == 0) Lam = lds_rd8(lt);
        if constexpr (ks == 7) Lx = lds_rd8(lt + 2 * 1536);
        if constexpr (ks == 1) { ee = f32x2{ur[0], ui[0]}; }
        if constexpr (ks >= 2 && ks <= 9) {
          constexpr int t0 = 1 + 2 * (ks - 2);
          ee = cfma(Lam, ee, f32x2{ur[t0], ui[t0]});
          if constexpr (t0 + 1 < 16) ee = cfma(Lam, ee, f32x2{ur[t0 + 1], ui[t0 + 1]});
          opaquec(ee);
        }
        if constexpr (ks == 10) {
          const f32x2 elo = {up32(ee[0]), up32(ee[1])};
          lds_wr8(tabb + (WT_OFF - TAB_OFF) + (lane_now() >> 5) * 2048 + rg * 512, cfma(Lx, elo, ee));
        }
      }
      if constexpr (ph == 1 && ks == 0) {
        if (rg == 3) {
          const unsigned wt = tabb + (WT_OFF - TAB_OFF) + 2048;
          const f32x2 w0 = lds_rd8(wt), w1 = lds_rd8(wt + 512), w2 = lds_rd8(wt + 1024), w3 = lds_rd8(wt + 1536);
          const f32x2 L32 = csq(Lx);
          const f32x2 E = cfma(L32, cfma(L32, cfma(L32, w0, w1), w2), w3);
          gstore_sc1(ws + WS_GRAN + (size_t)((mi * 3 + pb) * 16 + c) * 1024 + set * 512, (lane_now() & 31) * 16,
                     u32x4{__float_as_uint(E[0]), tag2, __float_as_uint(E[1]), tag2});
        }
      }
      if constexpr (ph == 1 && ks == 1) cc = lds_rd8(lt + 1536);
      if constexpr ((ph == 1 && ks >= 4) || (ph == 2 && ks < 8)) {
        constexpr int t = (ph == 1) ? ks - 4 : ks + 8;
        const float t1 = cc[1] * ui[t], t2 = cc[1] * ur[t];
        float a = __builtin_fmaf(cc[0], ur[t], -t1), b = __builtin_fmaf(cc[0], ui[t], t2);
        opaquef(a); opaquef(b);
        ur[t] = a; ui[t] = b;
      }
      if constexpr (ph == 3) {
        if constexpr (ks == 6) cc = lds_rd8(lt + 3 * 1536);
        if constexpr (ks == 9) {
          SCHED();
          wait2<ssm_ndma(s)>(gr[0], gr[1]);
          const int l = lane_now(), r = l & 31, h = l >> 5;
          const int k0 = 4 * rg + 2 * h;
          const bool v0 = c - 1 - k0 >= 0, v1 = c - 2 - k0 >= 0;
          auto stale = [&]() INL {
            const unsigned b0 = (gr[0][1] ^ tag2) | (gr[0][3] ^ tag2), b1 = (gr[1][1] ^ tag2) | (gr[1][3] ^ tag2);
            return __builtin_amdgcn_ballot_w64(((v0 ? b0 : 0u) | (v1 ? b1 : 0u)) != 0u) != 0ull;
          };
          if (__builtin_expect(stale(), 0)) {
            const char* gb = ws + WS_GRAN + (size_t)((mi * 3 + pb) * 16) * 1024 + set * 512;
            int spins = 200000;
            do {
              gr[0] = gload16_sc1(gb, (v0 ? c - 1 - k0 : 0) * 1024 + r * 16);
              gr[1] = gload16_sc1(gb, (v1 ? c - 2 - k0 : 0) * 1024 + r * 16);
              wait2<0>(gr[0], gr[1]);
            } while (stale() && --spins > 0);
          }
          const f32x2 a0 = {v0 ? __uint_as_float(gr[0][0]) : 0.f, v0 ? __uint_as_float(gr[0][2]) : 0.f};
          const f32x2 a1 = {v1 ? __uint_as_float(gr[1][0]) : 0.f, v1 ? __uint_as_float(gr[1][2]) : 0.f};
          lds_wr8(tabb + (GT_OFF - TAB_OFF) + (2 * rg + h) * 512, cfma(cc, a1, a0));
          cc = csq(cc);
          opaquec(cc);
        }
      }
      if constexpr (ph == 4 && ks <= 6) {
        const unsigned gt = tabb + (GT_OFF - TAB_OFF), wt = tabb + (WT_OFF - TAB_OFF) + 2048;
        if constexpr (ks == 0) { ee = lds_rd8(gt + 7 * 512); g0 = lds_rd8(gt + 6 * 512); g1 = lds_rd8(gt + 5 * 512); }
        if constexpr (ks == 1) { ee = cfma(cc, cfma(cc, ee, g0), g1); opaquec(ee); g0 = lds_rd8(gt + 4 * 512); g1 = lds_rd8(gt + 3 * 512); }
        if constexpr (ks == 2) { ee = cfma(cc, cfma(cc, ee, g0), g1); opaquec(ee); g0 = lds_rd8(gt + 2 * 512); g1 = lds_rd8(gt + 512); }
        if constexpr (ks == 3) { ee = cfma(cc, cfma(cc, ee, g0), g1); opaquec(ee); g0 = lds_rd8(gt); Lx = lds_rd8(lt + 2 * 1536); }
        if constexpr (ks == 4) {
          ee = cfma(cc, ee, g0); opaquec(ee); cc = csq(Lx); opaquec(cc);
          g0 = lds_rd8(wt); g1 = lds_rd8(wt + 512); g2 = lds_rd8(wt + 1024);
        }
        if constexpr (ks == 5) {
          if (rg > 0) ee = cfma(cc, ee, g0);
          if (rg > 1) ee = cfma(cc, ee, g1);
          if (rg > 2) ee = cfma(cc, ee, g2);
          opaquec(ee);
          cc = lds_rd8(lt + 1536); Lam = lds_rd8(lt); g2 = lds_rd8(wt - 2048 + rg * 512);
        }
        if constexpr (ks == 6) {
          const f32x2 e2 = cfma(Lx, ee, g2);
          const bool up = lane_now() >= 32;
          ee = f32x2{up ? e2[0] : ee[0], up ? e2[1] : ee[1]};
          ee = cfma(cc, ee, f32x2{0.f, 0.f}); opaquec(ee);
        }
      }
      if constexpr ((ph == 4 && ks >= 7) || (ph == 5 && ks <= 10)) {
        constexpr int tt = (ph == 4) ? ks - 7 : ks + 5;
        ee = cfma(Lam, ee, f32x2{ur[tt], ui[tt]});
        opaquec(ee);
        if constexpr ((tt & 1) == 0) { tprev = ee[0]; tprev2 = ee[1]; }
        else {
          unsigned p0 = pk2(tprev, ee[0]), p1 = pk2(-tprev2, -ee[1]); opaque(p0); opaque(p1);
          if constexpr (tt < 8) { gr[0][tt >> 1] = p0; gr[1][tt >> 1] = p1; }
          else { actp[0][(tt >> 1) & 3] = p0; actp[1][(tt >> 1) & 3] = p1; }
        }
      }
      if constexpr (ph == 5 && (ks == 7 || ks == 8)) {
        const int l = lane_now();
        const unsigned so = (l ^ (8 * (l >> 5))) * 16;
        gstore_sc1(amine + (2 * pb + (ks - 7)) * 16384, so, gr[ks - 7]);
      }
    }
    if constexpr (n == 8) { SCHED(); WAIT_V(7); BAR(); }
    if constexpr (ks == 11 && s < 35) {
      SCHED();
      if constexpr (s == 1 || s == 3 || (SSM && (ph == 0 || ph == 3 || s >= 17))) WAIT_L0();
      if constexpr (s == 0) WAIT_V(4); else WAIT_V(SSM ? ssm_wait(s) : mlp_wait(s));
      constexpr bool chk = SSM ? (s >= 16 && s <= 28 && (s - 16) % 6 == 0) : (s >= 15 && s <= 30 && (s - 15) % 3 == 0);
      if constexpr (chk) {
        if (w == 0) {
          constexpr unsigned need = SSM ? (s - 16) / 6 + 1 : (s - 15) / 3 + 1;
          pin2(flg);
          auto late = [&]() INL { return __builtin_amdgcn_ballot_w64(flg[1] != tag || flg[0] < need) != 0ull; };
          if (__builtin_expect(late(), 0)) {
            const int l = lane_now();
            int spins = 200000;
            do { flg = gload8_sc1(flagb, (l < 3 ? l : 0) * 128); wait0_2(flg); } while (late() && --spins > 0);
          }
        }
      }
      BAR();
      STAMP(5 + s);
    }
    SCHED();
  });
  {
    const char* pbase = ws + WS_PART + (size_t)((c * 4 + rg) * 9 + 3 * mi) * 20480 + gq * 4096 + set * 2048;
#pragma unroll
    for (int cb = 0; cb < 3; ++cb)
#pragma unroll
      for (int hh = 0; hh < 2; ++hh) {
        u32x4 v;
#pragma unroll
        for (int e = 0; e < 4; ++e) v[e] = pk2(oacc[cb][8 * hh + 2 * e], oacc[cb][8 * hh + 2 * e + 1]);
        gstore_sc1(pbase + cb * 20480 + hh * 1024, pvo, v);
      }
  }
  if (w == 0 && pvo == 0) {
    if (mi == 0) *(unsigned*)(ws + WS_EPOCH + (c * 5 + gq) * 128) = tag;
    if (SSM && c == 15) *(unsigned*)(ws + WS_EPOCH + (80 + mi) * 128) = tag2;
  }
  if constexpr (SSM) asm volatile("fk_ssm_end:"); else asm volatile("fk_mlp_end:");
  STAMP(40);
  STAMP(41);
  RSTAMP(61);
  STAMP_OUT();
}

__global__ __launch_bounds__(512, 2) void main_kernel(const Params P) {
  __shared__ __attribute__((aligned(16))) char smem[163840];
  const unsigned lds0 = (unsigned)(size_t)(LAS char*)smem;
  const int b = blockIdx.x, xcd = b & 7, idx = b >> 3;
  const int j = idx >> 1, c = 2 * xcd + (idx & 1);
#if defined(__HIP_DEVICE_COMPILE__)
  if (j < 12) body<false>(P, lds0, c, j);
  else body<true>(P, lds0, c, j);
#endif
}

__global__ __launch_bounds__(256) void reduce_kernel(const u16* __restrict__ x, const u16* __restrict__ dskip, char* __restrict__ ws,
                                                     float* __restrict__ out) {
  __shared__ __attribute__((aligned(16))) float tile[2][2][32][36];
  const int blk = (blockIdx.x & 7) * 72 + (blockIdx.x >> 3);
  const int tid = threadIdx.x, lane = tid & 63, wv = tid >> 6;
  const int crg = blk / 9, gp = blk % 9;
  const char* pp = ws + WS_PART + (size_t)blk * 20480 + tid * 16;
  u32x4 v[5];
#pragma unroll
  for (int k = 0; k < 5; ++k) v[k] = *(const u32x4*)(pp + k * 4096);
  const int orow = tid >> 4, oc = (tid & 15) * 4;
  const int m0 = crg * 32 + orow, d0 = gp * 64 + oc;
  u32x2 xv[2]; f32x2 st[2];
#pragma unroll
  for (int i = 0; i < 2; ++i) {
    xv[i] = *(const u32x2*)(x + (size_t)(m0 + 16 * i) * D + d0);
    st[i] = *(const f32x2*)(ws + WS_STATS + (size_t)(m0 + 16 * i) * 8);
  }
  const u32x2 dv = *(const u32x2*)(dskip + d0);
  float z[8], y[8];
#pragma unroll
  for (int e = 0; e < 4; ++e) {
    z[2 * e] = (bflo(v[0][e]) + bflo(v[1][e])) + (bflo(v[2][e]) + bflo(v[3][e]));
    z[2 * e + 1] = (bfhi(v[0][e]) + bfhi(v[1][e])) + (bfhi(v[2][e]) + bfhi(v[3][e]));
    y[2 * e] = bflo(v[4][e]); y[2 * e + 1] = bfhi(v[4][e]);
  }
#pragma unroll
  for (int e = 0; e < 8; ++e) z[e] = z[e] * 0.693147180559945f * __builtin_amdgcn_rcpf(1.f + __builtin_amdgcn_exp2f(-z[e]));
  {
    const int g2 = wv >> 1, s = wv & 1, row = lane & 31, col = 16 * (lane >> 5) + 8 * s;
    *(f32x4*)&tile[g2][0][row][col] = f32x4{y[0], y[1], y[2], y[3]};
    *(f32x4*)&tile[g2][0][row][col + 4] = f32x4{y[4], y[5], y[6], y[7]};
    *(f32x4*)&tile[g2][1][row][col] = f32x4{z[0], z[1], z[2], z[3]};
    *(f32x4*)&tile[g2][1][row][col + 4] = f32x4{z[4], z[5], z[6], z[7]};
  }
  __syncthreads();
  const float dk[4] = {bflo(dv[0]), bfhi(dv[0]), bflo(dv[1]), bfhi(dv[1])};
#pragma unroll
  for (int i = 0; i < 2; ++i) {
    const f32x4 yv = *(const f32x4*)&tile[oc >> 5][0][orow + 16 * i][oc & 31];
    const f32x4 gv = *(const f32x4*)&tile[oc >> 5][1][orow + 16 * i][oc & 31];
    const float xx[4] = {bflo(xv[i][0]), bfhi(xv[i][0]), bflo(xv[i][1]), bfhi(xv[i][1])};
    f32x4 o;
#pragma unroll
    for (int e = 0; e < 4; ++e) o[e] = xx[e] + (yv[e] + (xx[e] * st[i][0] + st[i][1]) * dk[e]) * gv[e];
    *(f32x4*)(out + (size_t)(m0 + 16 * i) * D + d0) = o;
  }
}

extern "C" void kernel_launch(void* const* d_in, const int* in_sizes, int n_in, void* d_out, int out_size, void* d_ws, size_t ws_size,
                              hipStream_t stream) {
  Params P;
  P.x = (const u16*)d_in[0]; P.lre = (const u16*)d_in[1]; P.lim = (const u16*)d_in[2];
  P.bre = (const u16*)d_in[3]; P.bim = (const u16*)d_in[4]; P.cre = (const u16*)d_in[5]; P.cim = (const u16*)d_in[6];
  P.ldt = (const u16*)d_in[7]; P.w1 = (const u16*)d_in[10]; P.b1 = (const u16*)d_in[11]; P.w2 = (const u16*)d_in[12];
  P.ws = (char*)d_ws;
  main_kernel<<<240, 512, 0, stream>>>(P);
  reduce_kernel<<<576, 256, 0, stream>>>(P.x, (const u16*)d_in[8], P.ws, (float*)d_out);
}
```

```cpp
#include <hip/hip_runtime.h>
#include <hip/hip_bf16.h>
#pragma clang diagnostic ignored "-Wint-to-pointer-cast"

typedef unsigned short u16;
typedef __attribute__((ext_vector_type(2))) __bf16 bf16x2;
typedef __attribute__((ext_vector_type(4))) __bf16 bf16x4;
typedef __attribute__((ext_vector_type(8))) __bf16 bf16x8;
typedef __attribute__((ext_vector_type(4))) short s16x4;
typedef __attribute__((ext_vector_type(2))) float f32x2;
typedef __attribute__((ext_vector_type(4))) float f32x4;
typedef __attribute__((ext_vector_type(16))) float f32x16;
typedef __attribute__((ext_vector_type(2))) unsigned u32x2;
typedef __attribute__((ext_vector_type(4))) unsigned u32x4;
#define LAS __attribute__((address_space(3)))
#define GAS __attribute__((address_space(1)))
#define DI __device__ __forceinline__
#define INL __attribute__((always_inline))

constexpr int S = 2048, D = 576, F = 4608;
constexpr float LOG2E = 1.44269504088896f;
constexpr int SLOT = 24576;
constexpr int X_OFF = 4 * SLOT;
constexpr int ARING = X_OFF;
constexpr int TAB_OFF = ARING + 49152;
constexpr int LT_OFF = TAB_OFF;
constexpr int WT_OFF = TAB_OFF + 6144;
constexpr int GT_OFF = TAB_OFF + 10240;
constexpr size_t WS_STATS = 14u << 20;
constexpr size_t WS_EPOCH = 16384;
constexpr size_t WS_FLAG = 32768;
constexpr size_t WS_PART = 65536;
constexpr size_t WS_GRAN = (15u << 20) + 32768;
constexpr size_t WS_ACT = 16u << 20;

DI float bflo(unsigned v) { return __uint_as_float(v << 16); }
DI float bfhi(unsigned v) { return __uint_as_float(v & 0xffff0000u); }
DI unsigned pk2(float a, float b) { f32x2 v = {a, b}; return __builtin_bit_cast(unsigned, __builtin_convertvector(v, bf16x2)); }
#define WAIT_V(n) asm volatile("s_waitcnt vmcnt(%0)" ::"n"(n) : "memory")
#define WAIT_L0() asm volatile("s_waitcnt lgkmcnt(0)" ::: "memory")
#define BAR() asm volatile("s_barrier" ::: "memory")
#define SCHED() __builtin_amdgcn_sched_barrier(0)
#define BF8(x) __builtin_bit_cast(bf16x8, (x))
#define MFMA16(a, b, c) __builtin_amdgcn_mfma_f32_16x16x32_bf16((a), (b), (c), 0, 0, 0)
#define MFMA32(a, b, c) __builtin_amdgcn_mfma_f32_32x32x16_bf16((a), (b), (c), 0, 0, 0)
template <int I> struct IC { static constexpr int value = I; };
template <int B, int E, class Fn> DI void static_for(Fn&& f) { if constexpr (B < E) { f(IC<B>{}); static_for<B + 1, E>(f); } }

template <class T, T... I> struct ISeq {};
template <int B, class Fn, int... I> DI void run_seq(Fn&& f, ISeq<int, I...>) { (f(IC<B + I>{}), ...); }

DI void gldsW(const void* sbase, unsigned voff, unsigned lds_addr) {
  asm volatile("s_mov_b32 m0, %2\n\ts_nop 0\n\tglobal_load_lds_dwordx4 %0, %1" ::"v"(voff), "s"(sbase), "s"(lds_addr) : "memory");
}
DI void gldsA(const void* sbase, unsigned voff, unsigned lds_addr) {
  asm volatile("s_mov_b32 m0, %2\n\ts_nop 0\n\tglobal_load_lds_dwordx4 %0, %1 sc1" ::"v"(voff), "s"(sbase), "s"(lds_addr) : "memory");
}
DI void glds4(const void* sbase, unsigned voff, unsigned lds_addr) {
  asm volatile("s_mov_b32 m0, %2\n\ts_nop 0\n\tglobal_load_lds_dword %0, %1" ::"v"(voff), "s"(sbase), "s"(lds_addr) : "memory");
}
DI void gstore_sc1(const void* sbase, unsigned voff, u32x4 v) {
  asm volatile("global_store_dwordx4 %0, %1, %2 sc1\n\ts_nop 1" ::"v"(voff), "v"(v), "s"(sbase) : "memory");
}
DI void gstore8_sc1(const void* sbase, unsigned voff, u32x2 v) {
  asm volatile("global_store_dwordx2 %0, %1, %2 sc1\n\ts_nop 1" ::"v"(voff), "v"(v), "s"(sbase) : "memory");
}
template <int OFF> DI u32x4 gload16(const void* sbase, unsigned voff) {
  u32x4 v; asm volatile("global_load_dwordx4 %0, %1, %2 offset:%3" : "=v"(v) : "v"(voff), "s"(sbase), "n"(OFF) : "memory"); return v;
}
DI u32x4 gload16_sc1(const void* sbase, unsigned voff) {
  u32x4 v; asm volatile("global_load_dwordx4 %0, %1, %2 sc1" : "=v"(v) : "v"(voff), "s"(sbase) : "memory"); return v;
}
DI u32x2 gload8_sc1(const void* sbase, unsigned voff) {
  u32x2 v; asm volatile("global_load_dwordx2 %0, %1, %2 sc1" : "=v"(v) : "v"(voff), "s"(sbase) : "memory"); return v;
}
DI u32x2 gload8(const void* sbase, unsigned voff) {
  u32x2 v; asm volatile("global_load_dwordx2 %0, %1, %2" : "=v"(v) : "v"(voff), "s"(sbase) : "memory"); return v;
}
DI unsigned gload4_sc1(const void* sbase, unsigned voff) {
  unsigned v; asm volatile("global_load_dword %0, %1, %2 sc1" : "=v"(v) : "v"(voff), "s"(sbase) : "memory"); return v;
}
DI unsigned gload2(const void* sbase, unsigned voff) {
  unsigned v; asm volatile("global_load_ushort %0, %1, %2" : "=v"(v) : "v"(voff), "s"(sbase) : "memory"); return v;
}
template <int N> DI void wait1(u32x4& a) { asm volatile("s_waitcnt vmcnt(%1)" : "+v"(a) : "n"(N) : "memory"); }
template <int N> DI void wait2(u32x4& a, u32x4& b) { asm volatile("s_waitcnt vmcnt(%2)" : "+v"(a), "+v"(b) : "n"(N) : "memory"); }
template <int N> DI void wait4(u32x2& a, u32x2& b, u32x2& c, u32x2& d) { asm volatile("s_waitcnt vmcnt(%4)" : "+v"(a), "+v"(b), "+v"(c), "+v"(d) : "n"(N) : "memory"); }
template <int N> DI void waitn2(u32x2& a) { asm volatile("s_waitcnt vmcnt(%1)" : "+v"(a) : "n"(N) : "memory"); }
template <int N> DI void wait5(u32x2& a, u32x2& b, u32x2& c, u32x2& d, u32x2& e) { asm volatile("s_waitcnt vmcnt(%5)" : "+v"(a), "+v"(b), "+v"(c), "+v"(d), "+v"(e) : "n"(N) : "memory"); }
DI void reload2(u32x4& a, u32x4& b, const void* pa, const void* pb, unsigned voff) {
  asm volatile("global_load_dwordx4 %0, %2, %3 sc1\n\tglobal_load_dwordx4 %1, %2, %4 sc1\n\ts_waitcnt vmcnt(0)" : "+&v"(a), "+&v"(b) : "v"(voff), "s"(pa), "s"(pb) : "memory");
}
DI float msel(unsigned v, unsigned long long m) { float o; asm("v_cndmask_b32 %0, 0, %1, %2" : "=v"(o) : "v"(v), "s"(m)); return o; }
DI void pin2(u32x2& a) { asm volatile("" : "+v"(a)::"memory"); }
DI void wait0_2(u32x2& a) { asm volatile("s_waitcnt vmcnt(0)" : "+v"(a)::"memory"); }
DI void opaque(unsigned& a) { asm volatile("" : "+v"(a)); }
DI void opaquef(float& a) { asm volatile("" : "+v"(a)); }
DI int lane_now() { int l; asm volatile("v_mbcnt_lo_u32_b32 %0, -1, 0\n\tv_mbcnt_hi_u32_b32 %0, -1, %0" : "=v"(l)); return l; }
DI void opaque16(f32x16& a) { asm volatile("" : "+v"(a)); }
DI void opaquec(f32x2& a) { float p = a[0], q = a[1]; asm volatile("" : "+v"(p), "+v"(q)); a[0] = p; a[1] = q; }
DI bf16x8 tr_frag(unsigned a0, unsigned a1) {
  bf16x4 lo = __builtin_amdgcn_ds_read_tr16_b64_v4bf16((LAS bf16x4*)a0);
  bf16x4 hi = __builtin_amdgcn_ds_read_tr16_b64_v4bf16((LAS bf16x4*)a1);
  return __builtin_shufflevector(lo, hi, 0, 1, 2, 3, 4, 5, 6, 7);
}
DI u32x4 lds_rd(unsigned a) { return *(LAS u32x4*)a; }
DI void lds_wr(unsigned a, u32x4 v) { *(LAS u32x4*)a = v; }
DI f32x2 lds_rd8(unsigned a) { return *(LAS f32x2*)a; }
DI void lds_wr8(unsigned a, f32x2 v) { *(LAS f32x2*)a = v; }
DI u32x4 norm16(u32x4 v, float a, float b) {
  unsigned a0 = v[0], a1 = v[1], a2 = v[2], a3 = v[3], t0, t1, t2, t3;
  asm("v_lshlrev_b32 %4, 16, %0\n\tv_lshlrev_b32 %5, 16, %1\n\tv_lshlrev_b32 %6, 16, %2\n\tv_lshlrev_b32 %7, 16, %3\n\t"
      "v_and_b32 %0, 0xffff0000, %0\n\tv_and_b32 %1, 0xffff0000, %1\n\tv_and_b32 %2, 0xffff0000, %2\n\tv_and_b32 %3, 0xffff0000, %3\n\t"
      "v_fma_f32 %4, %4, %8, %9\n\tv_fma_f32 %5, %5, %8, %9\n\tv_fma_f32 %6, %6, %8, %9\n\tv_fma_f32 %7, %7, %8, %9\n\t"
      "v_fma_f32 %0, %0, %8, %9\n\tv_fma_f32 %1, %1, %8, %9\n\tv_fma_f32 %2, %2, %8, %9\n\tv_fma_f32 %3, %3, %8, %9\n\t"
      "v_cvt_pk_bf16_f32 %0, %4, %0\n\tv_cvt_pk_bf16_f32 %1, %5, %1\n\tv_cvt_pk_bf16_f32 %2, %6, %2\n\tv_cvt_pk_bf16_f32 %3, %7, %3"
      : "+v"(a0), "+v"(a1), "+v"(a2), "+v"(a3), "=&v"(t0), "=&v"(t1), "=&v"(t2), "=&v"(t3) : "v"(a), "v"(b));
  return u32x4{a0, a1, a2, a3};
}
DI unsigned norm4(unsigned a, float sc, float bi) {
  unsigned t;
  asm volatile("v_lshlrev_b32 %1, 16, %0\n\tv_and_b32 %0, 0xffff0000, %0\n\tv_fma_f32 %1, %1, %2, %3\n\tv_fma_f32 %0, %0, %2, %3\n\tv_cvt_pk_bf16_f32 %0, %1, %0"
               : "+v"(a), "=&v"(t) : "v"(sc), "v"(bi));
  return a;
}
template <int CTRL> DI float dpp_add(float a) {
  return a + __builtin_bit_cast(float, __builtin_amdgcn_update_dpp(0, __builtin_bit_cast(int, a), CTRL, 0xf, 0xf, true));
}
DI f32x2 cfma(f32x2 a, f32x2 b, f32x2 c) {
  f32x2 o;
  o[0] = __builtin_fmaf(a[0], b[0], __builtin_fmaf(-a[1], b[1], c[0]));
  o[1] = __builtin_fmaf(a[0], b[1], __builtin_fmaf(a[1], b[0], c[1]));
  return o;
}
DI f32x2 cfma_s(f32x2 a, f32x2 b, f32x2 c) {
  float o0, o1;
  asm("v_fma_f32 %0, -%3, %5, %6\n\tv_fma_f32 %1, %3, %4, %7\n\tv_fmac_f32 %0, %2, %4\n\tv_fmac_f32 %1, %2, %5"
      : "=&v"(o0), "=&v"(o1) : "v"(a[0]), "v"(a[1]), "v"(b[0]), "v"(b[1]), "v"(c[0]), "v"(c[1]));
  return f32x2{o0, o1};
}
DI f32x2 cmul_s(f32x2 a, f32x2 b) {
  float o0, o1;
  asm("v_mul_f32_e64 %0, -%3, %5\n\tv_mul_f32 %1, %3, %4\n\tv_fmac_f32 %0, %2, %4\n\tv_fmac_f32 %1, %2, %5"
      : "=&v"(o0), "=&v"(o1) : "v"(a[0]), "v"(a[1]), "v"(b[0]), "v"(b[1]));
  return f32x2{o0, o1};
}
DI float usel(float lo, float hi) { float o; asm("v_cndmask_b32 %0, %1, %2, %3" : "=v"(o) : "v"(lo), "v"(hi), "s"(0xffffffff00000000ull)); return o; }
DI f32x2 csq(f32x2 a) { f32x2 o; o[0] = __builtin_fmaf(a[0], a[0], -a[1] * a[1]); o[1] = 2.f * a[0] * a[1]; return o; }
DI float up32(float v) {
  auto t = __builtin_amdgcn_permlane32_swap(0u, __float_as_uint(v), false, false);
  return __uint_as_float(t[0]);
}

#define STAMP(i)
#define RSTAMP(i)
#define STAMP_OUT()

constexpr int p1_tile(int s) { return s < 6 ? (s & 1) : s / 3; }
constexpr int p1_kt(int s) { return s < 6 ? (s >> 1) : s % 3; }
constexpr int p2_cb(int n) { return n >= 420 ? (n - 420) / 4 : (n % 12) % 3; }
constexpr int p2_k4(int n) { return n >= 420 ? (n - 420) % 4 : (n % 12) / 3; }
constexpr bool has_wdma(int s) { return s + 3 <= 35; }
constexpr int n_pf(int s) { return (s >= 0 && s <= 6) ? 1 : 0; }
constexpr int EPI0 = 61;
constexpr int st_slot(int T, int o) { int r = EPI0 + 33 * T + (o ? 33 : 17); return (r % 12 <= 7) ? r / 12 : r / 12 + 1; }
constexpr int n_actst(int s) { int n = 0; for (int T = 0; T < 6; ++T) for (int o = 0; o < 2; ++o) if (st_slot(T, o) == s) ++n; return n; }
constexpr int st_id(int s) { for (int i = 0; i < 12; ++i) if (st_slot(i >> 1, i & 1) == s) return i; return 0; }
constexpr int mlp_nadma(int s) { return (s >= 15 && s + 3 <= 35) ? 2 : 0; }
constexpr int mlp_wait(int s) { return s + 2 > 35 ? 63 : (has_wdma(s) ? 3 : 0) + mlp_nadma(s) + n_actst(s - 1) + n_actst(s) + n_pf(s - 1) + n_pf(s); }
constexpr int mlp_flag_slot(int T) { return st_slot(T, 1) + 3; }
constexpr bool ssm_a0(int s) { return s >= 16 && s <= 33; }
constexpr bool ssm_a1(int s) { return s >= 15 && s <= 32; }
constexpr int ssm_late_st(int s) { return (s >= 11 && s <= 23 && (s - 11) % 6 == 0) ? 4 : 0; }
constexpr int ssm_ndma(int s) { return (has_wdma(s) ? 3 : 0) + (ssm_a0(s) ? 1 : 0) + (ssm_a1(s) ? 1 : 0); }
constexpr int ssm_wait(int s) { return ssm_ndma(s) + ssm_late_st(s - 1) + ssm_late_st(s) + n_pf(s - 1) + n_pf(s); }

struct Params {
  const u16 *x, *lre, *lim, *bre, *bim, *cre, *cim, *ldt, *dsk, *w1, *b1, *w2;
  char* ws;
  float* out;
};

template <bool SSM>
DI void body(const Params& P, const unsigned lds0, const int c, const int j) {
  char* const ws = P.ws;
  const int tid = threadIdx.x, lane = tid & 63;
  const int w = __builtin_amdgcn_readfirstlane(tid >> 6);
  const int rg = w & 3, set = w >> 2;
  const int r = lane & 31, h = lane >> 5;
  unsigned stampv = 0; (void)stampv;
  RSTAMP(60); STAMP(0);
  const int gq = SSM ? 4 : j / 3, mi = j - 3 * gq;

  u32x4 xr[2][9];
  unsigned aux[3], epoch, epoch2 = 0;
  const char* xb = (const char*)P.x + (size_t)(128 * c + 16 * w) * (D * 2);
  const unsigned xoff = (lane >> 3) * (D * 2) + (lane & 7) * 16;
  const unsigned xoff1 = xoff + 8 * (D * 2);
  __builtin_amdgcn_s_setprio(2);
  static_for<0, 9>([&](auto I) INL { xr[0][I.value] = gload16<I.value * 128>(xb, xoff); });
  constexpr int NAUX = SSM ? 5 : 2;
  if constexpr (SSM) {
    const unsigned o = (192 * mi + (tid < 192 ? tid : 0)) * 2;
    aux[0] = gload2(P.lre, o); aux[1] = gload2(P.lim, o); aux[2] = gload2(P.ldt, o);
    epoch2 = gload4_sc1(ws + WS_EPOCH + (16 + mi) * 128, 0u);
  } else {
    aux[0] = gload2(P.b1, (384 * j + (tid < 384 ? tid : 0)) * 2);
  }
  epoch = gload4_sc1(ws + WS_EPOCH + c * 128, 0u);

  unsigned pvo = lane * 16;
  unsigned voff1, voff2;
  const char *wa[2], *wb[3][2], *acm[3];
  if constexpr (SSM) {
    voff1 = voff2 = (lane >> 3) * (D * 2) + (((lane & 7) ^ ((4 * (w & 1) + (lane >> 4)) & 7)) * 16);
    wa[0] = (const char*)P.bre + (size_t)(192 * mi + 8 * w) * (D * 2);
    wa[1] = (const char*)P.bim + (size_t)(192 * mi + 8 * w) * (D * 2);
  } else {
    const unsigned swz = ((lane & 7) ^ (((lane >> 4) & 1) << 2)) * 16;
    voff1 = (lane >> 3) * (F * 2) + swz;
    voff2 = (lane >> 3) * (D * 2) + swz;
    wa[0] = wa[1] = (const char*)P.w1 + ((size_t)(8 * w) * F + 384 * j) * 2;
  }
#pragma unroll
  for (int k = 0; k < 3; ++k) {
    const int mm = (mi + k >= 3) ? mi + k - 3 : mi + k;
    if constexpr (SSM) {
      wb[k][0] = (const char*)P.cre + (size_t)(192 * mi + 8 * w) * (D * 2) + 384 * mm;
      wb[k][1] = (const char*)P.cim + (size_t)(192 * mi + 8 * w) * (D * 2) + 384 * mm;
    } else {
      wb[k][0] = wb[k][1] = (const char*)P.w2 + ((size_t)(384 * (3 * gq + mm) + 8 * w) * D + 192 * mi) * 2;
    }
    acm[k] = ws + WS_ACT + (size_t)((c * 15 + 3 * gq + mm) * 6) * 16384 + w * 1024;
  }
  auto dmaW = [&](int s, int i) INL {
    const unsigned dst = lds0 + (s & 3) * SLOT + w * 1024 + i * 8192;
    const int u = s - 18;
    if constexpr (SSM) {
      if (s < 18) gldsW(wa[p1_tile(s) & 1] + (64 * (p1_tile(s) >> 1)) * (D * 2) + (192 * p1_kt(s) + 64 * i) * 2, voff1, dst);
      else gldsW(wb[(u % 6) >> 1][u & 1] + (64 * i) * (D * 2) + 128 * (u / 6), voff2, dst);
    } else {
      if (s < 18) gldsW(wa[0] + ((size_t)(192 * p1_kt(s) + 64 * i) * F + 64 * p1_tile(s)) * 2, voff1, dst);
      else gldsW(wb[u % 3][0] + ((size_t)(64 * (u / 3)) * D + 64 * i) * 2, voff2, dst);
    }
  };
  float rs[2], nb[2];
  unsigned xw_ad[2];
  xw_ad[0] = lds0 + X_OFF + (16 * w + (lane >> 3)) * 128 + (((lane & 7) ^ (lane >> 4)) * 16);
  xw_ad[1] = (xw_ad[0] ^ 64) + 1024;
  auto xwrite1 = [&](int ps, int i) INL { lds_wr(xw_ad[ps] + (i % 3) * 16384, xr[ps][i]); };
  static_for<0, 2>([&](auto PI) INL {
    constexpr int ps = PI.value;
    f32x4 s1 = {}, s2 = {}, s3 = {};
    const unsigned selv = (((lane >> 4) & 3) == (lane & 3)) ? 0x3f803f80u : 0u;
    const u32x4 sel = {selv, selv, selv, selv};
    static_for<0, 9>([&](auto GI) INL {
      constexpr int i = GI.value;
      if constexpr (ps == 0) { xr[1][i] = gload16<i * 128>(xb, xoff1); if constexpr (i == 8) __builtin_amdgcn_s_setprio(0); wait1<9 + NAUX>(xr[0][i]); }
      else wait1<8 - i + 4>(xr[1][i]);
      const u32x2 t0 = {xr[ps][i][0], xr[ps][i][1]}, t1 = {xr[ps][i][2], xr[ps][i][3]};
      s2 = __builtin_amdgcn_mfma_f32_4x4x4bf16_1k(__builtin_bit_cast(s16x4, t0), __builtin_bit_cast(s16x4, t0), s2, 0, 0, 0);
      s3 = __builtin_amdgcn_mfma_f32_4x4x4bf16_1k(__builtin_bit_cast(s16x4, t1), __builtin_bit_cast(s16x4, t1), s3, 0, 0, 0);
      s1 = MFMA16(BF8(sel), BF8(xr[ps][i]), s1);
      SCHED();
    });
    if constexpr (ps == 0) {
      dmaW(0, 0); dmaW(0, 1); dmaW(0, 2);
      unsigned long long pc; unsigned len;
      if constexpr (SSM) asm volatile("s_getpc_b64 %0\nfk_ssm_beg:\n\ts_mov_b32 %1, fk_ssm_end-fk_ssm_beg" : "=s"(pc), "=s"(len));
      else asm volatile("s_getpc_b64 %0\nfk_mlp_beg:\n\ts_mov_b32 %1, fk_mlp_end-fk_mlp_beg" : "=s"(pc), "=s"(len));
      const unsigned co = (w * 32 + (lane >> 1)) * 128;
      glds4((const void*)pc, co < len ? co : 0u, lds0 + TAB_OFF + 14336);
    }
    s2 += s3;
    const float e0 = s2[0], e1 = s2[1], e2 = s2[2], e3 = s2[3];
    const float lo2 = (lane & 1) ? e1 : e0, hi2 = (lane & 1) ? e3 : e2;
    float a2 = (lane & 2) ? hi2 : lo2;
    const float f0 = s1[0], f1 = s1[1], f2 = s1[2], f3 = s1[3];
    const float lo1 = (lane & 16) ? f1 : f0, hi1 = (lane & 16) ? f3 : f2;
    float a1 = (lane & 32) ? hi1 : lo1;
    a1 = dpp_add<0xB1>(a1); a2 = dpp_add<0xB1>(a2);
    a1 = dpp_add<0x4E>(a1); a2 = dpp_add<0x4E>(a2);
    a1 = dpp_add<0x141>(a1); a2 = dpp_add<0x141>(a2);
    const float m = a1 * (1.f / D);
    const float var = fmaxf(a2 * (1.f / D) - m * m, 0.f);
    const float rstd = __builtin_amdgcn_rsqf(var + 1e-5f);
    rs[ps] = SSM ? rstd : rstd * LOG2E;
    nb[ps] = -m * rs[ps];
#pragma unroll
    for (int i = 0; i < 3; ++i) { xr[ps][i] = norm16(xr[ps][i], rs[ps], nb[ps]); xwrite1(ps, i); }
    if constexpr (ps == 0) { STAMP(1); }
  });
  STAMP(2);
  dmaW(1, 0); dmaW(1, 1); dmaW(1, 2);
  opaque(aux[0]); opaque(epoch);
  const unsigned tag = __builtin_amdgcn_readfirstlane(epoch) + 1u;
  unsigned tag2 = 0;
  if constexpr (SSM) {
    opaque(aux[1]); opaque(aux[2]); opaque(epoch2);
    tag2 = __builtin_amdgcn_readfirstlane(epoch2) + 1u;
    if (w < 3) {
      const float lr = bflo(aux[0]), li = bflo(aux[1]);
      const float dt = __builtin_amdgcn_exp2f(bflo(aux[2]) * LOG2E);
      const float mag = __builtin_amdgcn_exp2f(dt * lr * LOG2E);
      const float rev = __builtin_amdgcn_fractf(dt * li * 0.15915494309189535f);
      f32x2 L = {mag * __builtin_amdgcn_cosf(rev), mag * __builtin_amdgcn_sinf(rev)};
      const float inv = 2.f / (lr * lr + li * li);
      const float nr = L[0] - 1.f, ni = L[1];
      const f32x2 c2 = {(nr * lr + ni * li) * inv, (ni * lr - nr * li) * inv};
      const f32x2 L16 = csq(csq(csq(csq(L))));
      const f32x2 L128 = csq(csq(csq(L16)));
      lds_wr8(lds0 + LT_OFF + tid * 8, L); lds_wr8(lds0 + LT_OFF + 1536 + tid * 8, c2);
      lds_wr8(lds0 + LT_OFF + 2 * 1536 + tid * 8, L16); lds_wr8(lds0 + LT_OFF + 3 * 1536 + tid * 8, L128);
    }
  } else {
    if (tid < 384) *(LAS float*)(lds0 + TAB_OFF + tid * 4) = bflo(aux[0]) * LOG2E;
    if ((w >> 1) == gq) {
      const char* sb = ws + WS_STATS + (c * 16 + j) * 256 + (w & 1) * 128;
      gstore8_sc1(sb, (lane >> 3) * 8, u32x2{__float_as_uint(rs[0] * (1.f / LOG2E)), __float_as_uint(nb[0] * (1.f / LOG2E))});
      gstore8_sc1(sb, (lane >> 3) * 8 + 64, u32x2{__float_as_uint(rs[1] * (1.f / LOG2E)), __float_as_uint(nb[1] * (1.f / LOG2E))});
    }
  }
  const int rperm = 16 * ((r >> 2) & 1) + (r & 3) + 4 * (r >> 3);
  bf16x8 xn[36];
  unsigned xn_ad[4];
  { const int row = 32 * rg + (SSM ? rperm : r); xn_ad[0] = lds0 + X_OFF + row * 128 + ((h ^ ((row >> 1) & 7)) * 16); }
  xn_ad[1] = xn_ad[0] ^ 32; xn_ad[2] = xn_ad[0] ^ 64; xn_ad[3] = xn_ad[0] ^ 96;
  auto xn_read = [&](int rd, int ks) INL { xn[12 * rd + ks] = BF8(lds_rd(xn_ad[ks & 3] + (ks >> 2) * 16384)); };

  const int G1 = (lane >> 4) & 1, q = (lane >> 2) & 3, p4 = lane & 3;
  unsigned rb1[4], rb2[4][2], ab[2];
  if constexpr (SSM) {
    const int row = 32 * set + r;
    rb1[0] = lds0 + row * 128 + ((h ^ ((row >> 1) & 7)) * 16);
    rb1[1] = rb1[0] ^ 32; rb1[2] = rb1[0] ^ 64; rb1[3] = rb1[0] ^ 96;
    opaque(rb1[0]); opaque(rb1[1]); opaque(rb1[2]); opaque(rb1[3]);
  } else {
    rb1[0] = lds0 + (8 * h + q) * 128 + (((set * 4 + G1 * 2 + (p4 >> 1)) ^ (((q >> 1) & 1) << 2)) * 16) + (p4 & 1) * 8;
    rb1[1] = rb1[0] + 2 * SLOT;
    opaque(rb1[0]); opaque(rb1[1]);
  }
  unsigned tabb = SSM ? lds0 + TAB_OFF + (32 * set + r) * 8 : lds0 + TAB_OFF + (32 * set + 4 * h) * 4;
  opaque(tabb);
  unsigned tabh = tabb + h * 2048 + rg * 512;
  if constexpr (SSM) opaque(tabh);
  auto bias_init = [&](int T) INL {
    f32x16 a;
#pragma unroll
    for (int g = 0; g < 4; ++g) {
      f32x4 v = *(LAS f32x4*)(tabb + (64 * T + 8 * g) * 4);
      a[4 * g] = v[0]; a[4 * g + 1] = v[1]; a[4 * g + 2] = v[2]; a[4 * g + 3] = v[3];
    }
    return a;
  };
  auto wfrag = [&](int n) INL {
    const int s = n / 12, ks = n % 12;
    if constexpr (SSM) {
      if (s < 18) return BF8(lds_rd(rb1[ks & 3] + (s & 1) * SLOT + (ks >> 2) * 8192));
      else return BF8(lds_rd(rb2[p2_k4(n)][(s >> 1) & 1] + (s & 1) * SLOT + p2_cb(n) * 8192));
    } else {
      if (s < 18) { unsigned a = rb1[(s >> 1) & 1] + (s & 1) * SLOT + ks * 2048; return tr_frag(a, a + 512); }
      else { unsigned a = rb2[0][(s >> 1) & 1] + (s & 1) * SLOT + p2_cb(n) * 8192 + p2_k4(n) * 2048; return tr_frag(a, a + 1024); }
    }
  };
  auto afrag = [&](int kq) INL {
    if constexpr (SSM) {
      const int u = kq >> 2, k4 = kq & 3;
      const int v = (k4 < 2) ? u - 1 : u;
      const unsigned off = ((v + 1) % 3) * 16384 + ((k4 < 2) ? 8192 : 0) + (k4 & 1) * 256;
      return tr_frag(ab[0] + off, ab[1] + off);
    } else {
      return BF8(lds_rd(ab[0] + ((kq >> 2) & 3) * 16384 + (kq & 3) * 4096));
    }
  };
  WAIT_L0();
  STAMP(3);
  WAIT_V(4);
  BAR();
  STAMP(4);

  f32x16 acc[SSM ? 4 : 3];
  f32x16 oacc[3];
  u32x4 actp[2], gr[2];
  float tn, tr, tprev, tprev2;
  f32x2 Lam, Lx, ee, cc, cl, cq, g0, g1, g2;
  const unsigned long long gm0 = (c - 1 - 4 * rg >= 0 ? 0xffffffffull : 0ull) | (c - 3 - 4 * rg >= 0 ? 0xffffffff00000000ull : 0ull);
  const unsigned long long gm1 = (c - 2 - 4 * rg >= 0 ? 0xffffffffull : 0ull) | (c - 4 - 4 * rg >= 0 ? 0xffffffff00000000ull : 0ull);
  bf16x8 fr[4], af[4];
  u32x2 fx[3], fdk[3], fst;
  u32x2 flg;
  unsigned sto;
  const f32x16 zero = {};
  const char* amine = ws + WS_ACT + (size_t)((c * 15 + j) * 6) * 16384 + set * 8192 + rg * (SSM ? 2048 : 1024);
  const char* flagb = ws + WS_FLAG + (c * 16 + 3 * gq) * 128;
  if constexpr (!SSM) acc[0] = bias_init(0);
  fr[0] = wfrag(0); fr[1] = wfrag(1);
  if constexpr (!SSM) fr[2] = wfrag(2);

  const char* pbase = ws + WS_PART + (size_t)((c * 4 + rg) * 9 + 3 * mi) * 20480 + gq * 4096 + set * 2048;
  auto pstore = [&](int cb, int hh) INL {
    u32x4 v;
#pragma unroll
    for (int e = 0; e < 4; ++e) v[e] = pk2(oacc[cb][8 * hh + 2 * e], oacc[cb][8 * hh + 2 * e + 1]);
    gstore_sc1(pbase + cb * 20480 + hh * 1024, pvo, v);
  };
  auto step = [&](auto NI) INL {
    constexpr int n = NI.value;
    constexpr int s = n / 12, ks = n % 12;
    if constexpr (n == 216 - 4) {
      const int lane = lane_now(), r = lane & 31, h = lane >> 5, G1 = (lane >> 4) & 1, q = (lane >> 2) & 3, p4 = lane & 3;
      if constexpr (SSM) {
        const int rperm = 16 * ((r >> 2) & 1) + (r & 3) + 4 * (r >> 3);
        const int row = 32 * set + rperm;
        rb2[0][0] = lds0 + row * 128 + ((h ^ ((row >> 1) & 7)) * 16);
        rb2[1][0] = rb2[0][0] ^ 32; rb2[2][0] = rb2[0][0] ^ 64; rb2[3][0] = rb2[0][0] ^ 96;
#pragma unroll
        for (int k = 0; k < 4; ++k) { rb2[k][1] = rb2[k][0] + 2 * SLOT; opaque(rb2[k][0]); opaque(rb2[k][1]); }
        const int o = p4 >> 1;
        ab[0] = lds0 + ARING + rg * 2048 + o * 1024 + G1 * 512 + (((2 * h) ^ (2 * G1 + o)) * 64) + q * 16 + (p4 & 1) * 8;
        ab[1] = ab[0] ^ 64;
        opaque(ab[0]); opaque(ab[1]);
      } else {
        const int dch = 4 * (p4 & 1) + 2 * G1 + (p4 >> 1);
        rb2[0][0] = lds0 + (4 * h + q) * 128 + (((set * 4 + (dch >> 1)) ^ (((q >> 1) & 1) << 2)) * 16) + (dch & 1) * 8;
        rb2[0][1] = rb2[0][0] + 2 * SLOT;
        ab[0] = lds0 + ARING + rg * 1024 + lane * 16;
        opaque(rb2[0][0]); opaque(rb2[0][1]); opaque(ab[0]);
      }
    }
    if constexpr (SSM && n == 15 * 12) pvo = lane_now() * 16;
    if constexpr (SSM && ks == 10 && (s & 1) == 1 && s < 17) {
#pragma unroll
      for (int k = 0; k < 4; ++k) { rb1[k] += (s & 2) ? -2 * SLOT : 2 * SLOT; opaque(rb1[k]); }
    }
    constexpr int pb = (s >= 6) ? (s - 6) / 6 : 0, ph = (s >= 6 && s < 24) ? (s - 6) % 6 : -1;
    if constexpr (ks == 0) {
      constexpr int pub = SSM ? ((s >= 14 && s <= 26 && (s - 14) % 6 == 0) ? (s - 14) / 6 + 1 : 0)
                              : (s == mlp_flag_slot(0) ? 1 : s == mlp_flag_slot(1) ? 2 : s == mlp_flag_slot(2) ? 3 : s == mlp_flag_slot(3) ? 4 : s == mlp_flag_slot(4) ? 5 : s == mlp_flag_slot(5) ? 6 : 0);
      constexpr bool poll = SSM ? (s >= 16 && s <= 28 && (s - 16) % 6 == 0) : (s >= 14 && s <= 29 && (s - 14) % 3 == 0);
      if constexpr (poll) { if (w == 0) { const int l = lane_now(); flg = gload8_sc1(flagb, (l < 3 ? l : 0) * 128); } }
      if constexpr (pub > 0) { if (w == 1) gstore8_sc1(flagb + mi * 128, 0u, u32x2{(unsigned)pub, tag}); }
      if constexpr (SSM && ph == 3) {
        const unsigned go = 3 * tabb - tabh;
        const char* gb = ws + WS_GRAN + ((mi * 3 + pb) * 16 + c - 1 - 4 * rg) * 1024 + rg * 512 - 2 * (lds0 + TAB_OFF);
        gr[0] = gload16_sc1(gb, go);
        gr[1] = gload16_sc1(gb - 1024, go);
      }
    }
    if constexpr (s < 6) {
      constexpr int kt = s >> 1;
      if constexpr ((s & 1) == 0) {
        if constexpr (ks == 0) { xn_read(kt, 0); xn_read(kt, 1); xn_read(kt, 2); xn_read(kt, 3); }
        else if constexpr (ks + 3 < 12) xn_read(kt, ks + 3);
      } else if constexpr (kt < 2) {
        if constexpr (ks % 2 == 0 && ks <= 8) { xwrite1((ks / 2) / 3, 3 * (kt + 1) + (ks / 2) % 3); SCHED(); }
        if constexpr (ks == 9) { xwrite1(1, 3 * (kt + 1) + 2); SCHED(); }
      }
    }
    if constexpr (SSM && ks == 4 && (s == 8 || s == 11 || s == 14 || s == 16)) {
      const int l = lane_now(), r = l & 31;
      const int row = 32 * rg + 16 * ((r >> 2) & 1) + (r & 3) + 4 * (r >> 3);
      const unsigned a = lds0 + X_OFF + 32768 + row * 128 + (((l >> 5) ^ ((row >> 1) & 7)) * 16);
      xn[32] = BF8(lds_rd(a)); xn[33] = BF8(lds_rd(a ^ 32)); xn[34] = BF8(lds_rd(a ^ 64)); xn[35] = BF8(lds_rd(a ^ 96));
    }
    if constexpr (s == 0 && ks < 3) dmaW(2, ks);
    if constexpr (s == 0 && ks >= 3 && ks < 6) dmaW(3, ks - 3);
    if constexpr (s > 0 && ks % 3 == 0 && ks <= 6 && has_wdma(s)) dmaW(s + 3, ks / 3);
    if constexpr (SSM) {
      constexpr int v = s - 16;
      if constexpr (ks == 2 && ssm_a0(s)) gldsA(acm[(v % 6) >> 1] + (2 * (v / 6) + (v & 1)) * 16384 + 8192, pvo, lds0 + ARING + ((v + 1) % 3) * 16384 + w * 1024);
      if constexpr (ks == 5 && ssm_a1(s)) gldsA(acm[((v + 1) % 6) >> 1] + (2 * ((v + 1) / 6) + ((v + 1) & 1)) * 16384, pvo, lds0 + ARING + ((v + 1) % 3) * 16384 + 8192 + w * 1024);
    } else {
      constexpr int u = s + 3 - 18;
      if constexpr ((ks == 2 || ks == 5) && mlp_nadma(s) > 0) gldsA(acm[u % 3] + (u / 3) * 16384 + (ks / 3) * 8192, pvo, lds0 + ARING + (u & 3) * 16384 + w * 1024 + (ks / 3) * 8192);
    }
    if constexpr (s <= 6 && ks == 7) {
      const unsigned l = lane_now();
      constexpr int sl = s < 3 ? s : s - 3;
      if constexpr (SSM) {
        constexpr unsigned lim = s < 3 ? 72 : 108;
        unsigned id = sl * 32 + rg * 8 + (l >> 3); id = id < lim ? id : lim - 1;
        const u16* mat = s < 3 ? (set ? P.bim : P.bre) : (set ? P.cim : P.cre);
        glds4((const char*)mat + (size_t)(192 * mi + (s < 3 ? 64 : 0)) * (D * 2) + c * lim * 128, id * 128 + (l & 7) * 4, lds0 + TAB_OFF + 14336);
      } else if constexpr (s < 3) {
        unsigned id = sl * 64 + w * 8 + (l >> 3); id = id < 144 ? id : 143;
        glds4((const char*)P.w1 + (size_t)(36 * c) * (F * 2) + 768 * j + 256, (id >> 2) * (F * 2) + (id & 3) * 128 + (l & 7) * 4, lds0 + TAB_OFF + 14336);
      } else {
        unsigned id = sl * 64 + w * 8 + (l >> 3); id = id < 216 ? id : 215;
        glds4((const char*)P.w2 + (size_t)(384 * j) * (D * 2) + c * 216 * 128, id * 128 + (l & 7) * 4, lds0 + TAB_OFF + 14336);
      }
    }
    if constexpr (SSM && n <= 213) fr[(n + 2) & 3] = wfrag(n + 2);
    if constexpr ((!SSM || n >= 213) && n + 3 < 432) fr[(n + 3) & 3] = wfrag(n + 3);
    if constexpr (SSM) {
      constexpr int u = s - 18;
      if constexpr (s >= 18 && s < 35 && (ks == 3 || ks == 6)) af[ks / 3 + 1] = afrag(4 * u + ks / 3 + 1);
      if constexpr (s == 35 && ks == 0) { af[2] = afrag(4 * u + 2); af[3] = afrag(4 * u + 3); }
      if constexpr (s >= 17 && s < 35 && (ks == 9 || ks == 10)) af[ks - 9] = afrag(4 * (u + 1) + ks - 9);
    } else {
      if constexpr (n + 3 >= 216 && n + 3 <= 420 && (n + 3) % 3 == 0) af[((n + 3 - 216) / 3) & 3] = afrag((n + 3 - 216) / 3);
      if constexpr (n >= 418 && n <= 420) af[n - 417] = afrag(68 + n - 417);
      if constexpr (n == 408) {
        const int l = lane_now();
        const int dcol = 192 * mi + 32 * set + 16 * (w & 1) + 8 * ((w >> 1) & 1);
        const char* xb2 = (const char*)P.x + (size_t)(128 * c + 32 * gq) * (D * 2) + dcol * 2;
        const unsigned xo = (l >> 1) * (D * 2) + (l & 1) * 8;
#pragma unroll
        for (int cb = 0; cb < 3; ++cb) { fx[cb] = gload8(xb2 + cb * 128, xo); fdk[cb] = gload8((const char*)P.dsk + dcol * 2 + cb * 128, (l & 1) * 8); }
        fst = gload8_sc1(ws + WS_STATS + (c * 16 + j) * 256, (l >> 1) * 8);
      }
    }
    if constexpr (s < 18) {
      constexpr int T = p1_tile(s), kt = p1_kt(s);
      if constexpr (SSM) { acc[T & 3] = MFMA32(xn[12 * kt + ks], fr[n & 3], (kt == 0 && ks == 0) ? zero : acc[T & 3]); opaque16(acc[T & 3]); }
      else { acc[T % 3] = MFMA32(fr[n & 3], xn[12 * kt + ks], acc[T % 3]); opaque16(acc[T % 3]); }
      if constexpr (s < 4) {
        constexpr int v = n - 24 * kt;
        constexpr int d0 = (6 * v + 4) / 5, d1 = (6 * (v + 1) + 4) / 5;
        if constexpr (d0 < 24) { constexpr int ch = d0 >> 2; xr[ch / 3][3 * (kt + 1) + ch % 3][d0 & 3] = norm4(xr[ch / 3][3 * (kt + 1) + ch % 3][d0 & 3], rs[ch / 3], nb[ch / 3]); }
        if constexpr (d0 + 1 < d1 && d0 + 1 < 24) { constexpr int d = d0 + 1, ch = d >> 2; xr[ch / 3][3 * (kt + 1) + ch % 3][d & 3] = norm4(xr[ch / 3][3 * (kt + 1) + ch % 3][d & 3], rs[ch / 3], nb[ch / 3]); }
      }
      if constexpr (!SSM && ks == 9 && s < 17 && p1_kt(s + 1) == 0) acc[p1_tile(s + 1) % 3] = bias_init(p1_tile(s + 1));
    } else {
      oacc[p2_cb(n)] = MFMA32(fr[n & 3], af[p2_k4(n)], n < 219 ? zero : oacc[p2_cb(n)]);
      opaque16(oacc[p2_cb(n)]);
      if constexpr (n == 425 || n == 426 || n == 429 || n == 430) pstore((n - 425) / 4, (n - 425) & 1);
    }
    if constexpr (!SSM) {
      if constexpr (n >= EPI0 && n < EPI0 + 198) {
        constexpr int T = (n - EPI0) / 33, u = (n - EPI0) % 33, e = u >> 1;
        const f32x16& pa = acc[T % 3];
        if constexpr ((u & 1) == 0) {
          if constexpr (e < 16) { tn = __builtin_amdgcn_exp2f(pa[e]); opaquef(tn); }
          if constexpr (e >= 1) {
            const float a = __builtin_fmaf(-pa[e - 1], tr, pa[e - 1]);
            if constexpr (((e - 1) & 1) == 0) { tprev = a; opaquef(tprev); }
            else { unsigned pk = pk2(tprev, a); opaque(pk); actp[(e - 1) >> 3][((e - 1) >> 1) & 3] = pk; }
          }
        } else { tr = __builtin_amdgcn_rcpf(__builtin_fmaf(tn, __builtin_fmaf(tn, 0.5f, 1.f), 1.f)); opaquef(tr); }
      }
      if constexpr (ks == 7 && n_actst(s) > 0) { constexpr int id = st_id(s); gstore_sc1(amine + (id >> 1) * 16384 + (id & 1) * 4096, pvo, actp[id & 1]); }
    } else if constexpr (ph >= 0) {
      f32x16& ur = acc[(2 * pb) & 3];
      f32x16& ui = acc[(2 * pb + 1) & 3];
      const unsigned lt = tabb + pb * 512;
      const unsigned wt = tabb + (WT_OFF - TAB_OFF) + 2048, gt = tabb + (GT_OFF - TAB_OFF);
      if constexpr (ph == 0) {
        if constexpr (ks == 0) { ee = cfma(Lam, f32x2{ur[0], ui[0]}, f32x2{ur[1], ui[1]}); opaquec(ee); }
        if constexpr (ks >= 1 && ks <= 9) {
          constexpr int t0 = 2 + ((ks - 1) * 14) / 9, t1 = 2 + (ks * 14) / 9;
          ee = cfma(Lam, ee, f32x2{ur[t0], ui[t0]});
          if constexpr (t0 + 1 < t1) ee = cfma(Lam, ee, f32x2{ur[t0 + 1], ui[t0 + 1]});
          opaquec(ee);
        }
        if constexpr (ks == 7) Lx = lds_rd8(lt + 2 * 1536);
        if constexpr (ks == 10) {
          const f32x2 elo = {up32(ee[0]), up32(ee[1])};
          lds_wr8(tabh + (WT_OFF - TAB_OFF), cfma_s(Lx, elo, ee));
        }
        if constexpr (ks == 11) { Lam = csq(Lx); opaquec(Lam); }
      }
      if constexpr (ph == 1) {
        if constexpr (ks == 0) { g0 = lds_rd8(wt); g1 = lds_rd8(wt + 512); g2 = lds_rd8(wt + 1024); ee = lds_rd8(wt + 1536); }
        if constexpr (ks == 2) {
          cl = f32x2{0.f, 0.f}; cq = f32x2{1.f, 0.f};
          if (rg > 0) { cl = g0; cq = Lam; }
          if (rg > 1) { cl = cfma_s(Lam, cl, g1); cq = cmul_s(Lam, Lam); }
          if (rg > 2) {
            cl = cfma_s(Lam, cl, g2); cq = cmul_s(Lam, cq);
            const f32x2 E = cfma_s(Lam, cl, ee);
            gstore_sc1(ws + WS_GRAN + ((mi * 3 + pb) * 16 + c) * 1024 - 2 * (lds0 + TAB_OFF), 2 * tabb,
                       u32x4{__float_as_uint(E[0]), tag2, __float_as_uint(E[1]), tag2});
          }
          opaquec(cl); opaquec(cq);
          g0 = lds_rd8(wt - 2048 + rg * 512); cc = lds_rd8(lt + 1536);
        }
        if constexpr (ks == 4) {
          const f32x2 a = cfma_s(Lx, cl, g0);
          cl = f32x2{usel(cl[0], a[0]), usel(cl[1], a[1])};
          opaquec(cl);
        }
        if constexpr (ks == 5) { const f32x2 a = cmul_s(Lx, cq); cq = f32x2{usel(cq[0], a[0]), usel(cq[1], a[1])}; opaquec(cq); }
        if constexpr (ks == 6) { cl = cmul_s(cc, cl); opaquec(cl); }
        if constexpr (ks == 7) { cq = cmul_s(cc, cq); opaquec(cq); }
      }
      if constexpr ((ph == 1 && ks >= 8) || ph == 2) {
        constexpr int t = (ph == 1) ? ks - 8 : ks + 4;
        const float t1 = cc[1] * ui[t], t2 = cc[1] * ur[t];
        float a = __builtin_fmaf(cc[0], ur[t], -t1), b = __builtin_fmaf(-cc[0], ui[t], -t2);
        opaquef(a); opaquef(b);
        ur[t] = a; ui[t] = b;
      }
      if constexpr (ph == 3) {
        if constexpr (ks == 4) cc = lds_rd8(lt + 3 * 1536);
        if constexpr (ks == 7) { Lx = csq(cc); opaquec(Lx); }
        if constexpr (ks == 9) {
          SCHED();
          wait2<ssm_ndma(s)>(gr[0], gr[1]);
          auto stale = [&]() INL {
            const unsigned b0 = (gr[0][1] ^ tag2) | (gr[0][3] ^ tag2), b1 = (gr[1][1] ^ tag2) | (gr[1][3] ^ tag2);
            return ((__builtin_amdgcn_ballot_w64(b0 != 0u) & gm0) | (__builtin_amdgcn_ballot_w64(b1 != 0u) & gm1)) != 0ull;
          };
          if (__builtin_expect(stale(), 0)) {
            const unsigned go = 3 * tabb - tabh;
            const char* gb = ws + WS_GRAN + ((mi * 3 + pb) * 16 + c - 1 - 4 * rg) * 1024 + rg * 512 - 2 * (lds0 + TAB_OFF);
            int spins = 200000;
            do { reload2(gr[0], gr[1], gb, gb - 1024, go); } while (stale() && --spins > 0);
          }
          const f32x2 a0 = {msel(gr[0][0], gm0), msel(gr[0][2], gm0)}, a1 = {msel(gr[1][0], gm1), msel(gr[1][2], gm1)};
          ee = cfma_s(cc, a1, a0); opaquec(ee);
        }
        if constexpr (ks == 10) {
          const f32x2 lo = {up32(ee[0]), up32(ee[1])};
          lds_wr8(tabh + (GT_OFF - TAB_OFF), cfma_s(Lx, ee, lo));
        }
        if constexpr (ks == 11) { cc = csq(Lx); opaquec(cc); }
      }
      if constexpr (ph == 4 && ks <= 3) {
        if constexpr (ks == 0) { ee = lds_rd8(gt + 2048 + 1536); g0 = lds_rd8(gt + 2048 + 1024); g1 = lds_rd8(gt + 2048 + 512); g2 = lds_rd8(gt + 2048); Lam = lds_rd8(lt); }
        if constexpr (ks == 2) { ee = cfma(cc, cfma(cc, ee, g0), g1); opaquec(ee); }
        if constexpr (ks == 3) { ee = cfma(cq, cfma(cc, ee, g2), cl); ee[1] = -ee[1]; opaquec(ee); }
      }
      if constexpr ((ph == 4 && ks >= 4) || (ph == 5 && ks <= 7)) {
        constexpr int tt = (ph == 4) ? ks - 4 : ks + 8;
        ee = cfma(f32x2{Lam[0], -Lam[1]}, ee, f32x2{ur[tt], ui[tt]});
        opaquec(ee);
        if constexpr ((tt & 1) == 0) { tprev = ee[0]; tprev2 = ee[1]; }
        else {
          unsigned p0 = pk2(tprev, ee[0]), p1 = pk2(tprev2, ee[1]); opaque(p0); opaque(p1);
          if constexpr (tt < 8) { gr[0][tt >> 1] = p0; gr[1][tt >> 1] = p1; }
          else { actp[0][(tt >> 1) & 3] = p0; actp[1][(tt >> 1) & 3] = p1; }
        }
      }
      if constexpr (ph == 5 && ks >= 7 && ks <= 10) {
        constexpr int o = (ks - 7) >> 1, part = (ks - 7) & 1;
        if constexpr (ks == 7) { const int l = lane_now(); sto = (l ^ (8 * (l >> 5))) * 16; }
        if constexpr (ks == 9) sto ^= 64;
        gstore_sc1(amine + (2 * pb + part) * 16384 + o * 1024, sto, o ? actp[part] : gr[part]);
      }
    }
    if constexpr (SSM && ks == 9 && (s == 5 || s == 11 || s == 17)) Lam = lds_rd8(tabb + ((s - 5) / 6) * 512);
    if constexpr (n == 8) { SCHED(); WAIT_V(7); BAR(); }
    if constexpr (ks == 11 && s < 35) {
      SCHED();
      if constexpr (s == 1 || s == 3 || (SSM && (ph == 0 || ph == 3 || s >= 17))) WAIT_L0();
      if constexpr (s == 0) WAIT_V(4); else WAIT_V(SSM ? ssm_wait(s) : mlp_wait(s));
      constexpr bool chk = SSM ? (s >= 16 && s <= 28 && (s - 16) % 6 == 0) : (s >= 15 && s <= 30 && (s - 15) % 3 == 0);
      if constexpr (chk) {
        if (w == 0) {
          constexpr unsigned need = SSM ? (s - 16) / 6 + 1 : (s - 15) / 3 + 1;
          pin2(flg);
          auto late = [&]() INL { return __builtin_amdgcn_ballot_w64(flg[1] != tag || flg[0] < need) != 0ull; };
          if (__builtin_expect(late(), 0)) {
            const int l = lane_now();
            int spins = 200000;
            do { flg = gload8_sc1(flagb, (l < 3 ? l : 0) * 128); wait0_2(flg); } while (late() && --spins > 0);
          }
        }
      }
      BAR();
      STAMP(5 + s);
    }
    SCHED();
  };
  run_seq<0>(step, __make_integer_seq<ISeq, int, 216>{});
  run_seq<216>(step, __make_integer_seq<ISeq, int, 216>{});
  pstore(2, 0); pstore(2, 1);
  STAMP(40);
  WAIT_V(0);
  BAR();
  STAMP(41);
  if (w == 1) gstore8_sc1(flagb + mi * 128, 0u, u32x2{7u, tag});
  if constexpr (SSM) {
    if (c == 15 && w == 0 && pvo == 0) *(unsigned*)(ws + WS_EPOCH + (16 + mi) * 128) = tag2;
  } else {
    const int l = lane_now();
    float xf[12], sk[12];
    pin2(fx[0]); pin2(fx[1]); pin2(fx[2]); pin2(fdk[0]); pin2(fdk[1]); pin2(fdk[2]); pin2(fst);
#pragma unroll
    for (int e = 0; e < 12; ++e) {
      const unsigned xv = fx[e >> 2][(e >> 1) & 1], dv = fdk[e >> 2][(e >> 1) & 1];
      xf[e] = (e & 1) ? bfhi(xv) : bflo(xv);
      sk[e] = __builtin_fmaf(xf[e], __uint_as_float(fst[0]), __uint_as_float(fst[1])) * ((e & 1) ? bfhi(dv) : bflo(dv));
      opaquef(xf[e]); opaquef(sk[e]);
    }
    const char* fb = ws + WS_FLAG + (c * 16 + mi) * 128;
    if (w == 0) {
      const unsigned fo = (l < 4 && l != gq ? l : (gq ^ 1)) * 384;
      int spins = 400000;
      do { flg = gload8_sc1(fb, fo); wait0_2(flg); } while (__builtin_amdgcn_ballot_w64(flg[1] != tag || flg[0] < 7u) != 0ull && --spins > 0);
    }
    BAR();
    STAMP(42);
    const char* pb = ws + WS_PART + (size_t)((c * 4 + gq) * 9 + 3 * mi) * 20480 + w * 512;
    u32x2 pv[3][4];
    float g[12];
#pragma unroll
    for (int cb = 0; cb < 3; ++cb)
#pragma unroll
      for (int k = 0; k < 4; ++k) pv[cb][k] = gload8_sc1(pb + cb * 20480 + k * 4096, l * 8);
    static_for<0, 3>([&](auto CI) INL {
      constexpr int cb = CI.value;
      wait4<8 - 4 * cb>(pv[cb][0], pv[cb][1], pv[cb][2], pv[cb][3]);
#pragma unroll
      for (int e = 0; e < 4; ++e) {
        float z;
        if (e & 1) z = (bfhi(pv[cb][0][e >> 1]) + bfhi(pv[cb][1][e >> 1])) + (bfhi(pv[cb][2][e >> 1]) + bfhi(pv[cb][3][e >> 1]));
        else z = (bflo(pv[cb][0][e >> 1]) + bflo(pv[cb][1][e >> 1])) + (bflo(pv[cb][2][e >> 1]) + bflo(pv[cb][3][e >> 1]));
        g[4 * cb + e] = z * 0.693147180559945f * __builtin_amdgcn_rcpf(1.f + __builtin_amdgcn_exp2f(-z));
        xf[4 * cb + e] = __builtin_fmaf(sk[4 * cb + e], g[4 * cb + e], xf[4 * cb + e]);
        opaquef(g[4 * cb + e]); opaquef(xf[4 * cb + e]);
      }
      SCHED();
    });
    STAMP(43);
    if (w == 0) {
      int spins = 400000;
      do { flg = gload8_sc1(fb + 4 * 384, 0u); wait0_2(flg); } while (__builtin_amdgcn_ballot_w64(flg[1] != tag || flg[0] < 7u) != 0ull && --spins > 0);
    }
    BAR();
    STAMP(44);
    u32x2 yv[3];
#pragma unroll
    for (int cb = 0; cb < 3; ++cb) yv[cb] = gload8_sc1(pb + cb * 20480 + 4 * 4096, l * 8);
    float* op = P.out + (size_t)(128 * c + 32 * gq + (l >> 1)) * D + 192 * mi + 32 * set + 16 * (w & 1) + 8 * ((w >> 1) & 1) + (l & 1) * 4;
    static_for<0, 3>([&](auto CI) INL {
      constexpr int cb = CI.value;
      waitn2<2>(yv[cb]);
      f32x4 o;
      o[0] = __builtin_fmaf(bflo(yv[cb][0]), g[4 * cb], xf[4 * cb]); o[1] = __builtin_fmaf(bfhi(yv[cb][0]), g[4 * cb + 1], xf[4 * cb + 1]);
      o[2] = __builtin_fmaf(bflo(yv[cb][1]), g[4 * cb + 2], xf[4 * cb + 2]); o[3] = __builtin_fmaf(bfhi(yv[cb][1]), g[4 * cb + 3], xf[4 * cb + 3]);
      *(f32x4*)(op + 64 * cb) = o;
      SCHED();
    });
    if (j == 0 && w == 0 && l == 0) *(unsigned*)(ws + WS_EPOCH + c * 128) = tag;
  }
  if constexpr (SSM) asm volatile("fk_ssm_end:"); else asm volatile("fk_mlp_end:");
  if constexpr (SSM) { STAMP(42); STAMP(43); STAMP(44); }
  STAMP(45);
  STAMP(46);
  RSTAMP(61);
  STAMP_OUT();
}

__global__ __launch_bounds__(512, 2) void main_kernel(const Params P) {
  __shared__ __attribute__((aligned(16))) char smem[163840];
  const unsigned lds0 = (unsigned)(size_t)(LAS char*)smem;
  const int b = blockIdx.x, xcd = b & 7, idx = b >> 3;
  const int j = idx >> 1, c = 2 * xcd + (idx & 1);
#if defined(__HIP_DEVICE_COMPILE__)
  if (j < 12) body<false>(P, lds0, c, j);
  else body<true>(P, lds0, c, j);
#endif
}

extern "C" void kernel_launch(void* const* d_in, const int* in_sizes, int n_in, void* d_out, int out_size, void* d_ws, size_t ws_size,
                              hipStream_t stream) {
  Params P;
  P.x = (const u16*)d_in[0]; P.lre = (const u16*)d_in[1]; P.lim = (const u16*)d_in[2];
  P.bre = (const u16*)d_in[3]; P.bim = (const u16*)d_in[4]; P.cre = (const u16*)d_in[5]; P.cim = (const u16*)d_in[6];
  P.ldt = (const u16*)d_in[7]; P.w1 = (const u16*)d_in[10]; P.b1 = (const u16*)d_in[11]; P.w2 = (const u16*)d_in[12];
  P.dsk = (const u16*)d_in[8]; P.ws = (char*)d_ws; P.out = (float*)d_out;
  main_kernel<<<240, 512, 0, stream>>>(P);
}
```

```cpp
#include <hip/hip_runtime.h>
#include <hip/hip_bf16.h>
#pragma clang diagnostic ignored "-Wint-to-pointer-cast"

typedef unsigned short u16;
typedef __attribute__((ext_vector_type(2))) __bf16 bf16x2;
typedef __attribute__((ext_vector_type(4))) __bf16 bf16x4;
typedef __attribute__((ext_vector_type(8))) __bf16 bf16x8;
typedef __attribute__((ext_vector_type(4))) short s16x4;
typedef __attribute__((ext_vector_type(2))) float f32x2;
typedef __attribute__((ext_vector_type(4))) float f32x4;
typedef __attribute__((ext_vector_type(16))) float f32x16;
typedef __attribute__((ext_vector_type(2))) unsigned u32x2;
typedef __attribute__((ext_vector_type(4))) unsigned u32x4;
#define LAS __attribute__((address_space(3)))
#define GAS __attribute__((address_space(1)))
#define DI __device__ __forceinline__
#define INL __attribute__((always_inline))

constexpr int S = 2048, D = 576, F = 4608;
constexpr float LOG2E = 1.44269504088896f;
constexpr int SLOT = 24576;
constexpr int X_OFF = 4 * SLOT;
constexpr int ARING = X_OFF;
constexpr int TAB_OFF = ARING + 49152;
constexpr int LT_OFF = TAB_OFF;
constexpr int WT_OFF = TAB_OFF + 6144;
constexpr int JUNK_OFF = TAB_OFF + 14336;
constexpr int GT_OFF = TAB_OFF + 10240;
constexpr size_t WS_EP2 = 0;
constexpr size_t WS_GRAN = 32768;
#define LAY 0
constexpr size_t WS_C0 = LAY == 2 ? (2u << 20) : 262144, WS_CS = LAY == 0 ? 2318336 : LAY == 1 ? 2359296 : (4u << 20);
constexpr int C_FLAG = 0;
constexpr int C_STAT = 4096;
constexpr int C_ACT = LAY ? 32768 : 8192;
constexpr int C_PART = C_ACT + 1572864;

DI float bflo(unsigned v) { return __uint_as_float(v << 16); }
DI float bfhi(unsigned v) { return __uint_as_float(v & 0xffff0000u); }
DI unsigned pk2(float a, float b) { f32x2 v = {a, b}; return __builtin_bit_cast(unsigned, __builtin_convertvector(v, bf16x2)); }
#define WAIT_V(n) asm volatile("s_waitcnt vmcnt(%0)" ::"n"(n) : "memory")
#define WAIT_L0() asm volatile("s_waitcnt lgkmcnt(0)" ::: "memory")
#define BAR() asm volatile("s_barrier" ::: "memory")
#define SCHED() __builtin_amdgcn_sched_barrier(0)
#define BF8(x) __builtin_bit_cast(bf16x8, (x))
#define MFMA16(a, b, c) __builtin_amdgcn_mfma_f32_16x16x32_bf16((a), (b), (c), 0, 0, 0)
#define MFMA32(a, b, c) __builtin_amdgcn_mfma_f32_32x32x16_bf16((a), (b), (c), 0, 0, 0)
template <int I> struct IC { static constexpr int value = I; };
template <int B, int E, class Fn> DI void static_for(Fn&& f) { if constexpr (B < E) { f(IC<B>{}); static_for<B + 1, E>(f); } }

template <class T, T... I> struct ISeq {};
template <int B, class Fn, int... I> DI void run_seq(Fn&& f, ISeq<int, I...>) { (f(IC<B + I>{}), ...); }

DI void gldsW(const void* sbase, unsigned voff, unsigned lds_addr) {
  asm volatile("s_mov_b32 m0, %2\n\ts_nop 0\n\tglobal_load_lds_dwordx4 %0, %1" ::"v"(voff), "s"(sbase), "s"(lds_addr) : "memory");
}
DI void gldsA(const void* sbase, unsigned voff, unsigned lds_addr) {
  asm volatile("s_mov_b32 m0, %2\n\ts_nop 0\n\tglobal_load_lds_dwordx4 %0, %1 sc1" ::"v"(voff), "s"(sbase), "s"(lds_addr) : "memory");
}
DI void glds4(const void* sbase, unsigned voff, unsigned lds_addr) {
  asm volatile("s_mov_b32 m0, %2\n\ts_nop 0\n\tglobal_load_lds_dword %0, %1" ::"v"(voff), "s"(sbase), "s"(lds_addr) : "memory");
}
DI void gstore_sc1(const void* sbase, unsigned voff, u32x4 v) {
  asm volatile("global_store_dwordx4 %0, %1, %2 sc1\n\ts_nop 1" ::"v"(voff), "v"(v), "s"(sbase) : "memory");
}
DI void gstore8_sc1(const void* sbase, unsigned voff, u32x2 v) {
  asm volatile("global_store_dwordx2 %0, %1, %2 sc1\n\ts_nop 1" ::"v"(voff), "v"(v), "s"(sbase) : "memory");
}
template <int OFF> DI u32x4 gload16(const void* sbase, unsigned voff) {
  u32x4 v; asm volatile("global_load_dwordx4 %0, %1, %2 offset:%3" : "=v"(v) : "v"(voff), "s"(sbase), "n"(OFF) : "memory"); return v;
}
DI u32x4 gload16_sc1(const void* sbase, unsigned voff) {
  u32x4 v; asm volatile("global_load_dwordx4 %0, %1, %2 sc1" : "=v"(v) : "v"(voff), "s"(sbase) : "memory"); return v;
}
DI u32x2 gload8_sc1(const void* sbase, unsigned voff) {
  u32x2 v; asm volatile("global_load_dwordx2 %0, %1, %2 sc1" : "=v"(v) : "v"(voff), "s"(sbase) : "memory"); return v;
}
DI u32x2 gload8(const void* sbase, unsigned voff) {
  u32x2 v; asm volatile("global_load_dwordx2 %0, %1, %2" : "=v"(v) : "v"(voff), "s"(sbase) : "memory"); return v;
}
DI unsigned gload4_sc1(const void* sbase, unsigned voff) {
  unsigned v; asm volatile("global_load_dword %0, %1, %2 sc1" : "=v"(v) : "v"(voff), "s"(sbase) : "memory"); return v;
}
DI unsigned gload2(const void* sbase, unsigned voff) {
  unsigned v; asm volatile("global_load_ushort %0, %1, %2" : "=v"(v) : "v"(voff), "s"(sbase) : "memory"); return v;
}
template <int N> DI void wait1(u32x4& a) { asm volatile("s_waitcnt vmcnt(%1)" : "+v"(a) : "n"(N) : "memory"); }
template <int N, int X> DI void wait1x(u32x4& a, unsigned more) {
  asm volatile("s_waitcnt vmcnt(%2)\n\ts_cmp_eq_u32 %1, 0\n\ts_cbranch_scc1 1f\n\ts_waitcnt vmcnt(%3)\n1:" : "+v"(a) : "s"(more), "n"(N + X), "n"(N) : "memory", "scc");
}
template <int N> DI void wait3(u32x4& a, u32x4& b, u32x4& c) { asm volatile("s_waitcnt vmcnt(%3)" : "+v"(a), "+v"(b), "+v"(c) : "n"(N) : "memory"); }
template <int N> DI void wait2(u32x4& a, u32x4& b) { asm volatile("s_waitcnt vmcnt(%2)" : "+v"(a), "+v"(b) : "n"(N) : "memory"); }
template <int N> DI void wait4(u32x2& a, u32x2& b, u32x2& c, u32x2& d) { asm volatile("s_waitcnt vmcnt(%4)" : "+v"(a), "+v"(b), "+v"(c), "+v"(d) : "n"(N) : "memory"); }
template <int N> DI void waitn2(u32x2& a) { asm volatile("s_waitcnt vmcnt(%1)" : "+v"(a) : "n"(N) : "memory"); }
template <int N> DI void wait5(u32x2& a, u32x2& b, u32x2& c, u32x2& d, u32x2& e) { asm volatile("s_waitcnt vmcnt(%5)" : "+v"(a), "+v"(b), "+v"(c), "+v"(d), "+v"(e) : "n"(N) : "memory"); }
DI void reload2(u32x4& a, u32x4& b, const void* pa, const void* pb, unsigned voff) {
  asm volatile("global_load_dwordx4 %0, %2, %3 sc1\n\tglobal_load_dwordx4 %1, %2, %4 sc1\n\ts_waitcnt vmcnt(0)" : "+&v"(a), "+&v"(b) : "v"(voff), "s"(pa), "s"(pb) : "memory");
}
DI float msel(unsigned v, unsigned long long m) { float o; asm("v_cndmask_b32 %0, 0, %1, %2" : "=v"(o) : "v"(v), "s"(m)); return o; }
DI void pin2(u32x2& a) { asm volatile("" : "+v"(a)::"memory"); }
DI void wait0_2(u32x2& a) { asm volatile("s_waitcnt vmcnt(0)" : "+v"(a)::"memory"); }
DI void opaque(unsigned& a) { asm volatile("" : "+v"(a)); }
DI void opaquef(float& a) { asm volatile("" : "+v"(a)); }
DI int lane_now() { int l; asm volatile("v_mbcnt_lo_u32_b32 %0, -1, 0\n\tv_mbcnt_hi_u32_b32 %0, -1, %0" : "=v"(l)); return l; }
DI void opaque16(f32x16& a) { asm volatile("" : "+v"(a)); }
DI void opaquec(f32x2& a) { float p = a[0], q = a[1]; asm volatile("" : "+v"(p), "+v"(q)); a[0] = p; a[1] = q; }
DI bf16x8 tr_frag(unsigned a0, unsigned a1) {
  bf16x4 lo = __builtin_amdgcn_ds_read_tr16_b64_v4bf16((LAS bf16x4*)a0);
  bf16x4 hi = __builtin_amdgcn_ds_read_tr16_b64_v4bf16((LAS bf16x4*)a1);
  return __builtin_shufflevector(lo, hi, 0, 1, 2, 3, 4, 5, 6, 7);
}
DI u32x4 lds_rd(unsigned a) { return *(LAS u32x4*)a; }
DI void lds_wr(unsigned a, u32x4 v) { *(LAS u32x4*)a = v; }
DI f32x2 lds_rd8(unsigned a) { return *(LAS f32x2*)a; }
DI void lds_wr8(unsigned a, f32x2 v) { *(LAS f32x2*)a = v; }
DI u32x4 norm16(u32x4 v, float a, float b) {
  unsigned a0 = v[0], a1 = v[1], a2 = v[2], a3 = v[3], t0, t1, t2, t3;
  asm("v_lshlrev_b32 %4, 16, %0\n\tv_lshlrev_b32 %5, 16, %1\n\tv_lshlrev_b32 %6, 16, %2\n\tv_lshlrev_b32 %7, 16, %3\n\t"
      "v_and_b32 %0, 0xffff0000, %0\n\tv_and_b32 %1, 0xffff0000, %1\n\tv_and_b32 %2, 0xffff0000, %2\n\tv_and_b32 %3, 0xffff0000, %3\n\t"
      "v_fma_f32 %4, %4, %8, %9\n\tv_fma_f32 %5, %5, %8, %9\n\tv_fma_f32 %6, %6, %8, %9\n\tv_fma_f32 %7, %7, %8, %9\n\t"
      "v_fma_f32 %0, %0, %8, %9\n\tv_fma_f32 %1, %1, %8, %9\n\tv_fma_f32 %2, %2, %8, %9\n\tv_fma_f32 %3, %3, %8, %9\n\t"
      "v_cvt_pk_bf16_f32 %0, %4, %0\n\tv_cvt_pk_bf16_f32 %1, %5, %1\n\tv_cvt_pk_bf16_f32 %2, %6, %2\n\tv_cvt_pk_bf16_f32 %3, %7, %3"
      : "+v"(a0), "+v"(a1), "+v"(a2), "+v"(a3), "=&v"(t0), "=&v"(t1), "=&v"(t2), "=&v"(t3) : "v"(a), "v"(b));
  return u32x4{a0, a1, a2, a3};
}
DI unsigned norm4(unsigned a, float sc, float bi) {
  unsigned t;
  asm volatile("v_lshlrev_b32 %1, 16, %0\n\tv_and_b32 %0, 0xffff0000, %0\n\tv_fma_f32 %1, %1, %2, %3\n\tv_fma_f32 %0, %0, %2, %3\n\tv_cvt_pk_bf16_f32 %0, %1, %0"
               : "+v"(a), "=&v"(t) : "v"(sc), "v"(bi));
  return a;
}
template <int CTRL> DI float dpp_add(float a) {
  return a + __builtin_bit_cast(float, __builtin_amdgcn_update_dpp(0, __builtin_bit_cast(int, a), CTRL, 0xf, 0xf, true));
}
DI f32x2 cfma(f32x2 a, f32x2 b, f32x2 c) {
  f32x2 o;
  o[0] = __builtin_fmaf(a[0], b[0], __builtin_fmaf(-a[1], b[1], c[0]));
  o[1] = __builtin_fmaf(a[0], b[1], __builtin_fmaf(a[1], b[0], c[1]));
  return o;
}
DI f32x2 cfma_s(f32x2 a, f32x2 b, f32x2 c) {
  float o0, o1;
  asm("v_fma_f32 %0, -%3, %5, %6\n\tv_fma_f32 %1, %3, %4, %7\n\tv_fmac_f32 %0, %2, %4\n\tv_fmac_f32 %1, %2, %5"
      : "=&v"(o0), "=&v"(o1) : "v"(a[0]), "v"(a[1]), "v"(b[0]), "v"(b[1]), "v"(c[0]), "v"(c[1]));
  return f32x2{o0, o1};
}
DI f32x2 cmul_s(f32x2 a, f32x2 b) {
  float o0, o1;
  asm("v_mul_f32_e64 %0, -%3, %5\n\tv_mul_f32 %1, %3, %4\n\tv_fmac_f32 %0, %2, %4\n\tv_fmac_f32 %1, %2, %5"
      : "=&v"(o0), "=&v"(o1) : "v"(a[0]), "v"(a[1]), "v"(b[0]), "v"(b[1]));
  return f32x2{o0, o1};
}
DI float usel(float lo, float hi) { float o; asm("v_cndmask_b32 %0, %1, %2, %3" : "=v"(o) : "v"(lo), "v"(hi), "s"(0xffffffff00000000ull)); return o; }
DI f32x2 csq(f32x2 a) { f32x2 o; o[0] = __builtin_fmaf(a[0], a[0], -a[1] * a[1]); o[1] = 2.f * a[0] * a[1]; return o; }
DI float sum16(float v) {
  auto t = __builtin_amdgcn_permlane16_swap(__float_as_uint(v), __float_as_uint(v), false, false);
  return __uint_as_float(t[0]) + __uint_as_float(t[1]);
}
DI float up32(float v) {
  auto t = __builtin_amdgcn_permlane32_swap(0u, __float_as_uint(v), false, false);
  return __uint_as_float(t[0]);
}

#define STAMP(i)
#define RSTAMP(i)
#define STAMP_OUT()

constexpr int p1_tile(int s) { return s < 6 ? (s & 1) : s / 3; }
constexpr int p1_kt(int s) { return s < 6 ? (s >> 1) : s % 3; }
constexpr int p2_cb(int n) { return n >= 420 ? (n - 420) / 4 : (n % 12) % 3; }
constexpr int p2_k4(int n) { return n >= 420 ? (n - 420) % 4 : (n % 12) / 3; }
constexpr int EPI0 = 61;
constexpr int st_slot(int T, int o) { int r = EPI0 + 33 * T + (o ? 33 : 17); return (r % 12 <= 7) ? r / 12 : r / 12 + 1; }
constexpr int st_id(int s) { for (int i = 0; i < 12; ++i) if (st_slot(i >> 1, i & 1) == s) return i; return -1; }
constexpr bool op_w(int R, int n) {
  const int s = n / 12, k = n % 12;
  if ((R == 1 && s >= 9 && s <= 14) || (R == 2 && s >= 15)) return false;
  return s == 0 ? k < 6 : (s <= 32 && k % 3 == 0 && k <= 6);
}
constexpr bool op_a(int R, int n) {
  const int s = n / 12, k = n % 12;
  if (R == 2) return false;
  if (R == 1) return (k == 2 && s >= 16 && s <= 32) || (k == 5 && s >= 15 && s <= 32) || n == 12 * 32 + 4;
  return (k == 2 || k == 5) && s >= 15 && s <= 32;
}
constexpr bool op_pf(int R, int n) { return R != 2 && n / 12 <= 6 && n % 12 == 8; }
constexpr bool op_cp(int R, int n) { const int s = n / 12; return n % 12 == 5 && s >= 7 && s <= 28 && s % 7 == 0; }
constexpr bool op_st(int R, int n) {
  const int s = n / 12, k = n % 12;
  if (R) return s >= 11 && s <= (R == 1 ? 17 : 23) && (s - 11) % 6 == 0 && k >= 7 && k <= 10;
  return k == 7 && st_id(s) >= 0;
}
constexpr bool drain(int R, int s) { return (R == 1 && s == 12) || (R == 2 && s == 18); }
constexpr int n_fill(int R, int n) { return op_w(R, n) + op_a(R, n); }
constexpr int n_lazy(int R, int n) { return op_pf(R, n) + op_cp(R, n) + op_st(R, n); }
constexpr int last_fill(int R, int L) { while (L >= 0 && n_fill(R, L) == 0) --L; return L; }
constexpr int ops_between(int R, int a, int e) { int n = 0; for (int i = a; i <= e; ++i) n += n_fill(R, i) + n_lazy(R, i); return n; }
constexpr int vm_n(int R, int L, int e) { const int l = last_fill(R, L); return (l >= 0 ? n_lazy(R, l) : 0) + ops_between(R, l + 1, e); }
constexpr int done_slot(int R, int m) { int s = m / 12; while (!drain(R, s) && last_fill(R, 12 * s - 1) <= m && s < 40) ++s; return s + 1; }
constexpr int pub_at(int R, int s) {
  if (R == 0) { for (int T = 0; T < 6; ++T) if (done_slot(0, 12 * st_slot(T, 1) + 7) == s) return T + 2; }
  else { for (int b = 0; b < 2; ++b) if (done_slot(R, 12 * (11 + 6 * b) + 10) == s) return b + 2; }
  return 0;
}
static_assert(pub_at(1, 13) == 2 && pub_at(1, 20) == 3 && pub_at(2, 14) == 2 && pub_at(2, 19) == 3, "schedule");
static_assert(done_slot(0, 12 * st_slot(0, 1) + 7) <= 12 && done_slot(0, 12 * st_slot(5, 1) + 7) <= 27, "schedule");
constexpr unsigned chk_need(int R, int s) {
  if (R == 0) return (s >= 15 && s <= 30 && (s - 15) % 3 == 0) ? (s - 15) / 3 + 2 : 0;
  if (R == 1) return s == 16 ? 2 : s == 22 ? 3 : s == 26 ? 2 : s == 28 ? 3 : s == 30 ? 4 : 0;
  return 0;
}
constexpr unsigned F_FIN = 8;

struct Params {
  const u16 *x, *lre, *lim, *bre, *bim, *cre, *cim, *ldt, *dsk, *w1, *b1, *w2;
  char* ws;
  float* out;
};

template <int R>
DI void body(const Params& P, const unsigned lds0, const int c, const int j) {
  constexpr bool SSM = R != 0;
  constexpr int PSTR = R == 2 ? 192 : 64;
  char* const ws = P.ws;
  const int tid = threadIdx.x, lane = tid & 63;
  const int w = __builtin_amdgcn_readfirstlane(tid >> 6);
  const int rg = w & 3, set = w >> 2;
  const int r = lane & 31, h = lane >> 5;
  unsigned stampv = 0; (void)stampv;
  RSTAMP(60); STAMP(0);
  const int gq = SSM ? 4 : j / 3, mi = j - 3 * gq;

  char* const wc = ws + WS_C0 + (size_t)c * WS_CS;
  u32x4 xr[2][9];
  unsigned aux[3], epoch, epoch2 = 0;
  const int rb0 = 8 * w, rb1_ = rb0 + 64;
  const char* xb0 = (const char*)P.x + (size_t)(128 * c + rb0) * (D * 2);
  const char* xb1 = (const char*)P.x + (size_t)(128 * c + rb1_) * (D * 2);
  const unsigned xoff = (lane >> 3) * (D * 2) + (lane & 7) * 16;
  __builtin_amdgcn_s_setprio(2);
  static_for<0, 9>([&](auto I) INL { xr[0][I.value] = gload16<I.value * 128>(xb0, xoff); });
  constexpr int NAUX = SSM ? 5 : 2;
  epoch = gload4_sc1(wc + C_FLAG + 16 * 128, 0u);
  if constexpr (SSM) {
    const unsigned o = (R == 2 ? 128 + 192 * (w < 3 ? w : 0) + lane : 192 * mi + (tid < 192 ? tid : 0)) * 2;
    aux[0] = gload2(P.lre, o); aux[1] = gload2(P.lim, o); aux[2] = gload2(P.ldt, o);
    epoch2 = gload4_sc1(ws + WS_EP2 + mi * 128, 0u);
  } else {
    aux[0] = gload2(P.b1, (384 * j + (tid < 384 ? tid : 0)) * 2);
  }

  unsigned pvo = lane * 16;
  unsigned voff1, voff2;
  const char *wa[2], *wb[3][2], *acm[3];
  if constexpr (SSM) {
    voff1 = voff2 = (lane >> 3) * (D * 2) + (((lane & 7) ^ ((4 * (w & 1) + (lane >> 4)) & 7)) * 16);
    wa[0] = (const char*)P.bre + (size_t)((R == 2 ? 128 : 192 * mi) + 8 * w) * (D * 2);
    wa[1] = (const char*)P.bim + (size_t)((R == 2 ? 128 : 192 * mi) + 8 * w) * (D * 2);
  } else {
    const unsigned swz = ((lane & 7) ^ (((lane >> 4) & 1) << 2)) * 16;
    voff1 = (lane >> 3) * (F * 2) + swz;
    voff2 = (lane >> 3) * (D * 2) + swz;
    wa[0] = wa[1] = (const char*)P.w1 + ((size_t)(8 * w) * F + 384 * j) * 2;
  }
#pragma unroll
  for (int k = 0; k < 3; ++k) {
    const int mm = (mi + k >= 3) ? mi + k - 3 : mi + k;
    if constexpr (SSM) {
      wb[k][0] = (const char*)P.cre + (size_t)(192 * mi + 8 * w) * (D * 2) + 384 * mm;
      wb[k][1] = (const char*)P.cim + (size_t)(192 * mi + 8 * w) * (D * 2) + 384 * mm;
    } else {
      wb[k][0] = wb[k][1] = (const char*)P.w2 + ((size_t)(384 * (3 * gq + mm) + 8 * w) * D + 192 * mi) * 2;
    }
    acm[k] = wc + C_ACT + ((3 * gq + mm) * 6) * 16384 + w * 1024;
  }
  auto dmaW = [&](int s, int i) INL {
    const unsigned dst = lds0 + (s & 3) * SLOT + w * 1024 + i * 8192;
    const int u = s - 18;
    if constexpr (SSM) {
      if (s < 18) gldsW(wa[p1_tile(s) & 1] + (PSTR * (p1_tile(s) >> 1)) * (D * 2) + (192 * p1_kt(s) + 64 * i) * 2, voff1, dst);
      else if (u < 12) gldsW(wb[(u % 6) >> 1][u & 1] + (64 * i) * (D * 2) + 128 * (u / 6), voff2, dst);
      else gldsW(wb[0][u & 1] - 384 * mi + (64 * i) * (D * 2) + 384 * ((u - 12) >> 1) + 256, voff2, dst);
    } else {
      if (s < 18) gldsW(wa[0] + ((size_t)(192 * p1_kt(s) + 64 * i) * F + 64 * p1_tile(s)) * 2, voff1, dst);
      else gldsW(wb[u % 3][0] + ((size_t)(64 * (u / 3)) * D + 64 * i) * 2, voff2, dst);
    }
  };
  auto code_touch = [&](int lines_per_wave_log2, unsigned skip, unsigned dst) INL {
    unsigned long long pc; unsigned len;
    if constexpr (R == 2) asm volatile("s_getpc_b64 %0\n\ts_mov_b32 %1, fk_hlp_end-." : "=s"(pc), "=s"(len));
    else if constexpr (R == 1) asm volatile("s_getpc_b64 %0\n\ts_mov_b32 %1, fk_ssm_end-." : "=s"(pc), "=s"(len));
    else asm volatile("s_getpc_b64 %0\n\ts_mov_b32 %1, fk_mlp_end-." : "=s"(pc), "=s"(len));
    const unsigned co = skip + (((unsigned)w << lines_per_wave_log2) + ((unsigned)lane_now() >> (6 - lines_per_wave_log2))) * 128;
    glds4((const void*)pc, co < len ? co : 0u, dst);
  };
  const unsigned ximg = (lane >> 3) * 128 + (((lane & 7) ^ ((4 * (w & 1) + (lane >> 4)) & 7)) * 16);
  float rs[2], nb[2];
  unsigned xw = lds0 + X_OFF + rb0 * 128 + ximg;
  auto xwrite1 = [&](int ps, int i) INL { lds_wr(xw + ps * 8192 + (i % 3) * 16384, xr[ps][i]); };
  auto stats = [&](auto PI) INL {
    constexpr int ps = PI.value;
    f32x4 s1 = {}, s2 = {}, s3 = {};
    const unsigned selv = (((lane >> 4) & 3) == (lane & 3)) ? 0x3f803f80u : 0u;
    const u32x4 sel = {selv, selv, selv, selv};
    static_for<0, 9>([&](auto GI) INL {
      constexpr int i = GI.value;
      if constexpr (ps == 0) { xr[1][i] = gload16<i * 128>(xb1, xoff); if constexpr (i == 8) __builtin_amdgcn_s_setprio(0); wait1<9 + NAUX>(xr[0][i]); }
      else wait1<8 - i + 4>(xr[1][i]);
      const u32x2 t0 = {xr[ps][i][0], xr[ps][i][1]}, t1 = {xr[ps][i][2], xr[ps][i][3]};
      s2 = __builtin_amdgcn_mfma_f32_4x4x4bf16_1k(__builtin_bit_cast(s16x4, t0), __builtin_bit_cast(s16x4, t0), s2, 0, 0, 0);
      s3 = __builtin_amdgcn_mfma_f32_4x4x4bf16_1k(__builtin_bit_cast(s16x4, t1), __builtin_bit_cast(s16x4, t1), s3, 0, 0, 0);
      s1 = MFMA16(BF8(sel), BF8(xr[ps][i]), s1);
      SCHED();
    });
    if constexpr (ps == 0) { dmaW(0, 0); dmaW(0, 1); dmaW(0, 2); code_touch(5, 0, lds0 + JUNK_OFF); }
    s2 += s3;
    const float e0 = s2[0], e1 = s2[1], e2 = s2[2], e3 = s2[3];
    const float lo2 = (lane & 1) ? e1 : e0, hi2 = (lane & 1) ? e3 : e2;
    float a2 = (lane & 2) ? hi2 : lo2;
    const float f0 = s1[0], f1 = s1[1], f2 = s1[2], f3 = s1[3];
    const float lo1 = (lane & 16) ? f1 : f0, hi1 = (lane & 16) ? f3 : f2;
    float a1 = (lane & 32) ? hi1 : lo1;
    a1 = dpp_add<0xB1>(a1); a2 = dpp_add<0xB1>(a2);
    a1 = dpp_add<0x4E>(a1); a2 = dpp_add<0x4E>(a2);
    a1 = dpp_add<0x141>(a1); a2 = dpp_add<0x141>(a2);
    const float m = a1 * (1.f / D);
    const float var = fmaxf(a2 * (1.f / D) - m * m, 0.f);
    rs[ps] = __builtin_amdgcn_rsqf(var + 1e-5f) * (SSM ? 1.f : LOG2E);
    nb[ps] = -m * rs[ps];
#pragma unroll
    for (int i = 0; i < 3; ++i) { xr[ps][i] = norm16(xr[ps][i], rs[ps], nb[ps]); xwrite1(ps, i); }
  };
  stats(IC<0>{});
  STAMP(1);
  stats(IC<1>{});
  opaque(epoch);
  const unsigned tag = __builtin_amdgcn_readfirstlane(epoch) + 1u;
  STAMP(2);
  dmaW(1, 0); dmaW(1, 1); dmaW(1, 2);
  opaque(aux[0]);
  unsigned tag2 = 0;
  if constexpr (SSM) {
    opaque(aux[1]); opaque(aux[2]); opaque(epoch2);
    tag2 = __builtin_amdgcn_readfirstlane(epoch2) + 1u;
    if (w < 3) {
      const float lr = bflo(aux[0]), li = bflo(aux[1]);
      const float dt = __builtin_amdgcn_exp2f(bflo(aux[2]) * LOG2E);
      const float mag = __builtin_amdgcn_exp2f(dt * lr * LOG2E);
      const float rev = __builtin_amdgcn_fractf(dt * li * 0.15915494309189535f);
      f32x2 L = {mag * __builtin_amdgcn_cosf(rev), mag * __builtin_amdgcn_sinf(rev)};
      const float inv = 2.f / (lr * lr + li * li);
      const float nr = L[0] - 1.f, ni = L[1];
      const f32x2 c2 = {(nr * lr + ni * li) * inv, (ni * lr - nr * li) * inv};
      const f32x2 L16 = csq(csq(csq(csq(L))));
      const f32x2 L128 = csq(csq(csq(L16)));
      lds_wr8(lds0 + LT_OFF + tid * 8, L); lds_wr8(lds0 + LT_OFF + 1536 + tid * 8, c2);
      lds_wr8(lds0 + LT_OFF + 2 * 1536 + tid * 8, L16); lds_wr8(lds0 + LT_OFF + 3 * 1536 + tid * 8, L128);
    }
  } else {
    if (tid < 384) *(LAS float*)(lds0 + TAB_OFF + tid * 4) = bflo(aux[0]) * LOG2E;
    if (set == (gq & 1)) {
      const float a = (gq & 2) ? rs[1] : rs[0], b = (gq & 2) ? nb[1] : nb[0];
      gstore8_sc1(wc + C_STAT + j * 256 + rg * 64, (lane >> 3) * 8, u32x2{__float_as_uint(a * (1.f / LOG2E)), __float_as_uint(b * (1.f / LOG2E))});
    }
  }
  u32x2 flg;
  const int rperm = 16 * ((r >> 2) & 1) + (r & 3) + 4 * (r >> 3);
  bf16x8 xn[36];
  unsigned xn_ad[4];
  { const int row = 32 * rg + (SSM ? rperm : r); xn_ad[0] = lds0 + X_OFF + row * 128 + ((h ^ ((row >> 1) & 7)) * 16); }
  xn_ad[1] = xn_ad[0] ^ 32; xn_ad[2] = xn_ad[0] ^ 64; xn_ad[3] = xn_ad[0] ^ 96;
  auto xn_read = [&](int rd, int ks) INL { xn[12 * rd + ks] = BF8(lds_rd(xn_ad[ks & 3] + (ks >> 2) * 16384)); };

  const int G1 = (lane >> 4) & 1, q = (lane >> 2) & 3, p4 = lane & 3;
  unsigned rb1[4], rb2[4][2], ab[2];
  if constexpr (SSM) {
    const int row = 32 * set + r;
    rb1[0] = lds0 + row * 128 + ((h ^ ((row >> 1) & 7)) * 16);
    rb1[1] = rb1[0] ^ 32; rb1[2] = rb1[0] ^ 64; rb1[3] = rb1[0] ^ 96;
    opaque(rb1[0]); opaque(rb1[1]); opaque(rb1[2]); opaque(rb1[3]);
  } else {
    rb1[0] = lds0 + (8 * h + q) * 128 + (((set * 4 + G1 * 2 + (p4 >> 1)) ^ (((q >> 1) & 1) << 2)) * 16) + (p4 & 1) * 8;
    rb1[1] = rb1[0] + 2 * SLOT;
    opaque(rb1[0]); opaque(rb1[1]);
  }
  unsigned tabb = SSM ? lds0 + TAB_OFF + (32 * set + r) * 8 : lds0 + TAB_OFF + (32 * set + 4 * h) * 4;
  opaque(tabb);
  unsigned tabh = tabb + h * 2048 + rg * 512;
  if constexpr (SSM) opaque(tabh);
  auto bias_init = [&](int T) INL {
    f32x16 a;
#pragma unroll
    for (int g = 0; g < 4; ++g) {
      f32x4 v = *(LAS f32x4*)(tabb + (64 * T + 8 * g) * 4);
      a[4 * g] = v[0]; a[4 * g + 1] = v[1]; a[4 * g + 2] = v[2]; a[4 * g + 3] = v[3];
    }
    return a;
  };
  auto wfrag = [&](int n) INL {
    const int s = n / 12, ks = n % 12;
    if constexpr (SSM) {
      if (s < 18) return BF8(lds_rd(rb1[ks & 3] + (s & 1) * SLOT + (ks >> 2) * 8192));
      else return BF8(lds_rd(rb2[p2_k4(n)][(s >> 1) & 1] + (s & 1) * SLOT + p2_cb(n) * 8192));
    } else {
      if (s < 18) { unsigned a = rb1[(s >> 1) & 1] + (s & 1) * SLOT + ks * 2048; return tr_frag(a, a + 512); }
      else { unsigned a = rb2[0][(s >> 1) & 1] + (s & 1) * SLOT + p2_cb(n) * 8192 + p2_k4(n) * 2048; return tr_frag(a, a + 1024); }
    }
  };
  auto afrag = [&](int kq) INL {
    if constexpr (SSM) {
      const int u = kq >> 2, k4 = kq & 3;
      const int v = (k4 < 2) ? u - 1 : u;
      const unsigned off = (v == 17 ? 3 : (v + 1) % 3) * 16384 + ((k4 < 2) ? 8192 : 0) + (k4 & 1) * 256;
      return tr_frag(ab[0] + off, ab[1] + off);
    } else {
      return BF8(lds_rd(ab[0] + ((kq >> 2) & 3) * 16384 + (kq & 3) * 4096));
    }
  };
  WAIT_L0();
  STAMP(3);
  WAIT_V(4);
  BAR();
  STAMP(4);

  f32x16 acc[SSM ? 4 : 3];
  f32x16 oacc[3];
  u32x4 actp[2], gr[2];
  float tn, tr, tprev, tprev2;
  f32x2 Lam, Lx, ee, cc, cl, cq, g0, g1, g2;
  const unsigned long long gm0 = (c - 1 - 4 * rg >= 0 ? 0xffffffffull : 0ull) | (c - 3 - 4 * rg >= 0 ? 0xffffffff00000000ull : 0ull);
  const unsigned long long gm1 = (c - 2 - 4 * rg >= 0 ? 0xffffffffull : 0ull) | (c - 4 - 4 * rg >= 0 ? 0xffffffff00000000ull : 0ull);
  bf16x8 fr[4], af[4];
  u32x2 fx[3], fdk[3], fst;
  unsigned sto;
  const f32x16 zero = {};
  const char* amine = wc + C_ACT + (j * 6) * 16384 + set * 8192 + rg * (SSM ? 2048 : 1024);
  const char* flagb = wc + C_FLAG + (3 * gq) * 128;
  if constexpr (!SSM) acc[0] = bias_init(0);
  fr[0] = wfrag(0); fr[1] = wfrag(1);
  if constexpr (!SSM) fr[2] = wfrag(2);

  const char* pbase = wc + C_PART + (rg * 9 + 3 * mi) * 20480 + gq * 4096 + set * 2048;
  auto pstore = [&](int cb, int hh) INL {
    u32x4 v;
#pragma unroll
    for (int e = 0; e < 4; ++e) v[e] = pk2(oacc[cb][8 * hh + 2 * e], oacc[cb][8 * hh + 2 * e + 1]);
    const unsigned o = pvo ^ (set * 128 + ((pvo >> 3) & 64) + hh * 32);
    if (!SSM && rg == gq) lds_wr(lds0 + ARING + 32768 + cb * 4096 + set * 2048 + hh * 1024 + o, v);
    else gstore_sc1(pbase + cb * 20480 + hh * 1024, o, v);
  };
  const int fpk = (w >> 1) < 3 ? (gq + 1 + (w >> 1)) & 3 : 4;
  const char* ffl = wc + C_FLAG + (3 * fpk + mi) * 128;
  bool got = false;
  auto fin_ready = [&]() INL { return __builtin_amdgcn_readfirstlane(flg[1]) == tag && __builtin_amdgcn_readfirstlane(flg[0]) >= F_FIN; };
  auto fin_fetch = [&]() INL {
    const char* src = wc + C_PART + (gq * 9 + 3 * mi) * 20480 + fpk * 4096 + (w & 1) * 2048;
#pragma unroll
    for (int i = 0; i < 6; ++i) gldsA(src + (i >> 1) * 20480 + (i & 1) * 1024, pvo, lds0 + (w >> 1) * 12288 + (i >> 1) * 4096 + (w & 1) * 2048 + (i & 1) * 1024);
  };
  auto step = [&](auto NI) INL {
    constexpr int n = NI.value;
    constexpr int s = n / 12, ks = n % 12;
    if constexpr (R != 2 && n == 216 - 4) {
      const int lane = lane_now(), r = lane & 31, h = lane >> 5, G1 = (lane >> 4) & 1, q = (lane >> 2) & 3, p4 = lane & 3;
      if constexpr (SSM) {
        const int rperm = 16 * ((r >> 2) & 1) + (r & 3) + 4 * (r >> 3);
        const int row = 32 * set + rperm;
        rb2[0][0] = lds0 + row * 128 + ((h ^ ((row >> 1) & 7)) * 16);
        rb2[1][0] = rb2[0][0] ^ 32; rb2[2][0] = rb2[0][0] ^ 64; rb2[3][0] = rb2[0][0] ^ 96;
#pragma unroll
        for (int k = 0; k < 4; ++k) { rb2[k][1] = rb2[k][0] + 2 * SLOT; opaque(rb2[k][0]); opaque(rb2[k][1]); }
        const int o = p4 >> 1;
        ab[0] = lds0 + ARING + rg * 2048 + o * 1024 + G1 * 512 + (((2 * h) ^ (2 * G1 + o)) * 64) + q * 16 + (p4 & 1) * 8;
        ab[1] = ab[0] ^ 64;
        opaque(ab[0]); opaque(ab[1]);
      } else {
        const int dch = 4 * (p4 & 1) + 2 * G1 + (p4 >> 1);
        rb2[0][0] = lds0 + (4 * h + q) * 128 + (((set * 4 + (dch >> 1)) ^ (((q >> 1) & 1) << 2)) * 16) + (dch & 1) * 8;
        rb2[0][1] = rb2[0][0] + 2 * SLOT;
        ab[0] = lds0 + ARING + rg * 1024 + lane * 16;
        opaque(rb2[0][0]); opaque(rb2[0][1]); opaque(ab[0]);
      }
    }
    if constexpr (SSM && n == 15 * 12) pvo = lane_now() * 16;
    if constexpr (SSM && ks == 10 && (s & 1) == 1 && s < (R == 1 ? 11 : 17)) {
#pragma unroll
      for (int k = 0; k < 4; ++k) { rb1[k] += (s & 2) ? -2 * SLOT : 2 * SLOT; opaque(rb1[k]); }
    }
    constexpr int pb = (s >= 6) ? (s - 6) / 6 : 0, ph = (s >= 6 && s < (R == 1 ? 18 : 24)) ? (s - 6) % 6 : -1;
    constexpr bool mm_on = R == 0 || (R == 1 ? (n < 144 || n >= 216) : n < 216);
    if constexpr (ks == 0) {
      constexpr int pub = pub_at(R, s);
      constexpr bool poll = chk_need(R, SSM ? s : s + 1) != 0;
      if constexpr (poll) { if (w == 0) { const int l = lane_now(); flg = gload8_sc1(flagb, (l & 3) * 128); } }
      if constexpr (pub > 0) { if (w == 1) gstore8_sc1(flagb + mi * 128, 0u, u32x2{(unsigned)pub, tag}); }
      if constexpr (SSM && ph == 3) {
        const unsigned go = 3 * tabb - tabh;
        const char* gb = ws + WS_GRAN + ((R == 2 ? pb * 3 + 2 : mi * 3 + pb) * 16 + c - 1 - 4 * rg) * 1024 + rg * 512 - 2 * (lds0 + TAB_OFF);
        gr[0] = gload16_sc1(gb, go);
        gr[1] = gload16_sc1(gb - 1024, go);
      }
    }
    if constexpr (s < 6) {
      constexpr int kt = s >> 1;
      if constexpr ((s & 1) == 0) {
        if constexpr (ks == 0) { xn_read(kt, 0); xn_read(kt, 1); xn_read(kt, 2); xn_read(kt, 3); }
        else if constexpr (ks + 3 < 12) xn_read(kt, ks + 3);
      } else if constexpr (kt < 2) {
        if constexpr (ks % 2 == 0 && ks <= 8) { xwrite1((ks / 2) / 3, 3 * (kt + 1) + (ks / 2) % 3); SCHED(); }
        if constexpr (ks == 9) { xwrite1(1, 3 * (kt + 1) + 2); SCHED(); }
      }
    }
    if constexpr (SSM && ks == 4 && (s == 8 || s == 11 || (R == 2 && (s == 14 || s == 17)))) {
      const int l = lane_now(), r = l & 31;
      const int row = 32 * rg + 16 * ((r >> 2) & 1) + (r & 3) + 4 * (r >> 3);
      const unsigned a = lds0 + X_OFF + 32768 + row * 128 + (((l >> 5) ^ ((row >> 1) & 7)) * 16);
      xn[32] = BF8(lds_rd(a)); xn[33] = BF8(lds_rd(a ^ 32)); xn[34] = BF8(lds_rd(a ^ 64)); xn[35] = BF8(lds_rd(a ^ 96));
    }
    if constexpr (op_w(R, n)) { if constexpr (s == 0) dmaW(2 + ks / 3, ks % 3); else dmaW(s + 3, ks / 3); }
    if constexpr (op_a(R, n)) {
      if constexpr (SSM) {
        constexpr int v = s - 16, u = (ks == 2) ? v : v + 1;
        gldsA((u < 12 ? acm[(u % 6) >> 1] + (2 * (u / 6) + (u & 1)) * 16384 : wc + C_ACT + (15 * 6 + u - 12) * 16384 + w * 1024) + (ks == 5 ? 0 : 8192), pvo, lds0 + ARING + (ks == 4 ? 3 : (v + 1) % 3) * 16384 + (ks == 5 ? 8192 : 0) + w * 1024);
      } else {
        constexpr int u = s + 3 - 18;
        gldsA(acm[u % 3] + (u / 3) * 16384 + (ks / 3) * 8192, pvo, lds0 + ARING + (u & 3) * 16384 + w * 1024 + (ks / 3) * 8192);
      }
    }
    if constexpr (op_cp(R, n)) code_touch(3, 3072, lds0 + ((s + 3) & 3) * SLOT + w * 1024 + 2 * 8192);
    if constexpr (op_pf(R, n)) {
      const unsigned l = lane_now();
      constexpr int sl = s < 3 ? s : s - 3;
      if constexpr (SSM) {
        constexpr unsigned lim = s < 3 ? 72 : 108;
        unsigned id = sl * 32 + rg * 8 + (l >> 3); id = id < lim ? id : lim - 1;
        const u16* mat = s < 3 ? (set ? P.bim : P.bre) : (set ? P.cim : P.cre);
        glds4((const char*)mat + (size_t)(192 * mi + (s < 3 ? 64 : 0)) * (D * 2) + c * lim * 128, id * 128 + (l & 7) * 4, lds0 + JUNK_OFF);
      } else if constexpr (s < 3) {
        unsigned id = sl * 64 + w * 8 + (l >> 3); id = id < 144 ? id : 143;
        glds4((const char*)P.w1 + (size_t)(36 * c) * (F * 2) + 768 * j + 256, (id >> 2) * (F * 2) + (id & 3) * 128 + (l & 7) * 4, lds0 + JUNK_OFF);
      } else {
        unsigned id = sl * 64 + w * 8 + (l >> 3); id = id < 216 ? id : 215;
        glds4((const char*)P.w2 + (size_t)(384 * j) * (D * 2) + c * 216 * 128, id * 128 + (l & 7) * 4, lds0 + JUNK_OFF);
      }
    }
    if constexpr (SSM && n + 2 < (R == 1 ? 144 : 216)) fr[(n + 2) & 3] = wfrag(n + 2);
    if constexpr ((R == 0 || (R == 1 && n >= 213)) && n + 3 < 432) fr[(n + 3) & 3] = wfrag(n + 3);
    if constexpr (R == 2) {
    } else if constexpr (SSM) {
      constexpr int u = s - 18;
      if constexpr (s >= 18 && s < 35 && (ks == 3 || ks == 6)) af[ks / 3 + 1] = afrag(4 * u + ks / 3 + 1);
      if constexpr (s >= 17 && s < 35 && (ks == 9 || ks == 10)) af[ks - 9] = afrag(4 * (u + 1) + ks - 9);
      if constexpr (n == 419 || n == 420) af[n - 417] = afrag(68 + n - 417);
    } else {
      if constexpr (n + 3 >= 216 && n + 3 <= 420 && (n + 3) % 3 == 0) af[((n + 3 - 216) / 3) & 3] = afrag((n + 3 - 216) / 3);
      if constexpr (n >= 418 && n <= 420) af[n - 417] = afrag(68 + n - 417);
      if constexpr (n == 408) {
        const int t = w * 64 + lane_now();
        const char* xb2 = (const char*)P.x + (size_t)(128 * c + 32 * gq) * (D * 2) + 384 * mi;
#pragma unroll
        for (int cb = 0; cb < 3; ++cb) { fx[cb] = gload8(xb2 + cb * 128, (t >> 4) * (D * 2) + (t & 15) * 8); fdk[cb] = gload8((const char*)P.dsk + 384 * mi + cb * 128, (t & 15) * 8); }
        fst = gload8_sc1(wc + C_STAT + j * 256, (t >> 4) * 8);
        flg = gload8_sc1(ffl, 0u);
      }
      if constexpr (n == 418) {
        SCHED();
        wait0_2(flg);
        got = fin_ready();
        if (got) fin_fetch();
      }
    }
    if constexpr (!mm_on) {
    } else if constexpr (s < 18) {
      constexpr int T = p1_tile(s), kt = p1_kt(s);
      if constexpr (SSM) { acc[T & 3] = MFMA32(xn[12 * kt + ks], fr[n & 3], (kt == 0 && ks == 0) ? zero : acc[T & 3]); opaque16(acc[T & 3]); }
      else { acc[T % 3] = MFMA32(fr[n & 3], xn[12 * kt + ks], acc[T % 3]); opaque16(acc[T % 3]); }
      if constexpr (s < 4) {
        constexpr int v = n - 24 * kt;
        constexpr int d0 = (6 * v + 4) / 5, d1 = (6 * (v + 1) + 4) / 5;
        if constexpr (d0 < 24) { constexpr int ch = d0 >> 2; xr[ch / 3][3 * (kt + 1) + ch % 3][d0 & 3] = norm4(xr[ch / 3][3 * (kt + 1) + ch % 3][d0 & 3], rs[ch / 3], nb[ch / 3]); }
        if constexpr (d0 + 1 < d1 && d0 + 1 < 24) { constexpr int d = d0 + 1, ch = d >> 2; xr[ch / 3][3 * (kt + 1) + ch % 3][d & 3] = norm4(xr[ch / 3][3 * (kt + 1) + ch % 3][d & 3], rs[ch / 3], nb[ch / 3]); }
      }
      if constexpr (!SSM && ks == 9 && s < 17 && p1_kt(s + 1) == 0) acc[p1_tile(s + 1) % 3] = bias_init(p1_tile(s + 1));
    } else {
      oacc[p2_cb(n)] = MFMA32(fr[n & 3], af[p2_k4(n)], n < 219 ? zero : oacc[p2_cb(n)]);
      opaque16(oacc[p2_cb(n)]);
      if constexpr (n == 425 || n == 426 || n == 429 || n == 430) pstore((n - 425) / 4, (n - 425) & 1);
    }
    if constexpr (!SSM) {
      if constexpr (n >= EPI0 && n < EPI0 + 198) {
        constexpr int T = (n - EPI0) / 33, u = (n - EPI0) % 33, e = u >> 1;
        const f32x16& pa = acc[T % 3];
        if constexpr ((u & 1) == 0) {
          if constexpr (e < 16) { tn = __builtin_amdgcn_exp2f(pa[e]); opaquef(tn); }
          if constexpr (e >= 1) {
            const float a = __builtin_fmaf(-pa[e - 1], tr, pa[e - 1]);
            if constexpr (((e - 1) & 1) == 0) { tprev = a; opaquef(tprev); }
            else { unsigned pk = pk2(tprev, a); opaque(pk); actp[(e - 1) >> 3][((e - 1) >> 1) & 3] = pk; }
          }
        } else { tr = __builtin_amdgcn_rcpf(__builtin_fmaf(tn, __builtin_fmaf(tn, 0.5f, 1.f), 1.f)); opaquef(tr); }
      }
      if constexpr (op_st(0, n)) { constexpr int id = st_id(s); gstore_sc1(amine + (id >> 1) * 16384 + (id & 1) * 4096, pvo, actp[id & 1]); }
    } else if constexpr (ph >= 0) {
      f32x16& ur = acc[(2 * pb) & 3];
      f32x16& ui = acc[(2 * pb + 1) & 3];
      const unsigned lt = tabb + pb * 512;
      const unsigned wt = tabb + (WT_OFF - TAB_OFF) + 2048, gt = tabb + (GT_OFF - TAB_OFF);
      if constexpr (ph == 0) {
        if constexpr (ks == 0) { ee = cfma(Lam, f32x2{ur[0], ui[0]}, f32x2{ur[1], ui[1]}); opaquec(ee); }
        if constexpr (ks >= 1 && ks <= 9) {
          constexpr int t0 = 2 + ((ks - 1) * 14) / 9, t1 = 2 + (ks * 14) / 9;
          ee = cfma(Lam, ee, f32x2{ur[t0], ui[t0]});
          if constexpr (t0 + 1 < t1) ee = cfma(Lam, ee, f32x2{ur[t0 + 1], ui[t0 + 1]});
          opaquec(ee);
        }
        if constexpr (ks == 7) Lx = lds_rd8(lt + 2 * 1536);
        if constexpr (ks == 10) {
          const f32x2 elo = {up32(ee[0]), up32(ee[1])};
          lds_wr8(tabh + (WT_OFF - TAB_OFF), cfma_s(Lx, elo, ee));
        }
        if constexpr (ks == 11) { Lam = csq(Lx); opaquec(Lam); }
      }
      if constexpr (ph == 1) {
        if constexpr (ks == 0) { g0 = lds_rd8(wt); g1 = lds_rd8(wt + 512); g2 = lds_rd8(wt + 1024); ee = lds_rd8(wt + 1536); }
        if constexpr (ks == 2) {
          cl = f32x2{0.f, 0.f}; cq = f32x2{1.f, 0.f};
          if (rg > 0) { cl = g0; cq = Lam; }
          if (rg > 1) { cl = cfma_s(Lam, cl, g1); cq = cmul_s(Lam, Lam); }
          if (rg > 2) {
            cl = cfma_s(Lam, cl, g2); cq = cmul_s(Lam, cq);
            const f32x2 E = cfma_s(Lam, cl, ee);
            gstore_sc1(ws + WS_GRAN + ((R == 2 ? pb * 3 + 2 : mi * 3 + pb) * 16 + c) * 1024 - 2 * (lds0 + TAB_OFF), 2 * tabb,
                       u32x4{__float_as_uint(E[0]), tag2, __float_as_uint(E[1]), tag2});
          }
          opaquec(cl); opaquec(cq);
          g0 = lds_rd8(wt - 2048 + rg * 512); cc = lds_rd8(lt + 1536);
        }
        if constexpr (ks == 4) {
          const f32x2 a = cfma_s(Lx, cl, g0);
          cl = f32x2{usel(cl[0], a[0]), usel(cl[1], a[1])};
          opaquec(cl);
        }
        if constexpr (ks == 5) { const f32x2 a = cmul_s(Lx, cq); cq = f32x2{usel(cq[0], a[0]), usel(cq[1], a[1])}; opaquec(cq); }
        if constexpr (ks == 6) { cl = cmul_s(cc, cl); opaquec(cl); }
        if constexpr (ks == 7) { cq = cmul_s(cc, cq); opaquec(cq); }
      }
      if constexpr ((ph == 1 && ks >= 8) || ph == 2) {
        constexpr int t = (ph == 1) ? ks - 8 : ks + 4;
        const float t1 = cc[1] * ui[t], t2 = cc[1] * ur[t];
        float a = __builtin_fmaf(cc[0], ur[t], -t1), b = __builtin_fmaf(-cc[0], ui[t], -t2);
        opaquef(a); opaquef(b);
        ur[t] = a; ui[t] = b;
      }
      if constexpr (ph == 3) {
        if constexpr (ks == 4) cc = lds_rd8(lt + 3 * 1536);
        if constexpr (ks == 7) { Lx = csq(cc); opaquec(Lx); }
        if constexpr (ks == 9) {
          SCHED();
          wait2<ops_between(R, 12 * s, n)>(gr[0], gr[1]);
          auto stale = [&]() INL {
            const unsigned b0 = (gr[0][1] ^ tag2) | (gr[0][3] ^ tag2), b1 = (gr[1][1] ^ tag2) | (gr[1][3] ^ tag2);
            return ((__builtin_amdgcn_ballot_w64(b0 != 0u) & gm0) | (__builtin_amdgcn_ballot_w64(b1 != 0u) & gm1)) != 0ull;
          };
          if (__builtin_expect(stale(), 0)) {
            const unsigned go = 3 * tabb - tabh;
            const char* gb = ws + WS_GRAN + ((R == 2 ? pb * 3 + 2 : mi * 3 + pb) * 16 + c - 1 - 4 * rg) * 1024 + rg * 512 - 2 * (lds0 + TAB_OFF);
            int spins = 200000;
            do { reload2(gr[0], gr[1], gb, gb - 1024, go); } while (stale() && --spins > 0);
          }
          const f32x2 a0 = {msel(gr[0][0], gm0), msel(gr[0][2], gm0)}, a1 = {msel(gr[1][0], gm1), msel(gr[1][2], gm1)};
          ee = cfma_s(cc, a1, a0); opaquec(ee);
        }
        if constexpr (ks == 10) {
          const f32x2 lo = {up32(ee[0]), up32(ee[1])};
          lds_wr8(tabh + (GT_OFF - TAB_OFF), cfma_s(Lx, ee, lo));
        }
        if constexpr (ks == 11) { cc = csq(Lx); opaquec(cc); }
      }
      if constexpr (ph == 4 && ks <= 3) {
        if constexpr (ks == 0) { ee = lds_rd8(gt + 2048 + 1536); g0 = lds_rd8(gt + 2048 + 1024); g1 = lds_rd8(gt + 2048 + 512); g2 = lds_rd8(gt + 2048); Lam = lds_rd8(lt); }
        if constexpr (ks == 2) { ee = cfma(cc, cfma(cc, ee, g0), g1); opaquec(ee); }
        if constexpr (ks == 3) { ee = cfma(cq, cfma(cc, ee, g2), cl); ee[1] = -ee[1]; opaquec(ee); }
      }
      if constexpr ((ph == 4 && ks >= 4) || (ph == 5 && ks <= 7)) {
        constexpr int tt = (ph == 4) ? ks - 4 : ks + 8;
        ee = cfma(f32x2{Lam[0], -Lam[1]}, ee, f32x2{ur[tt], ui[tt]});
        opaquec(ee);
        if constexpr ((tt & 1) == 0) { tprev = ee[0]; tprev2 = ee[1]; }
        else {
          unsigned p0 = pk2(tprev, ee[0]), p1 = pk2(tprev2, ee[1]); opaque(p0); opaque(p1);
          if constexpr (tt < 8) { gr[0][tt >> 1] = p0; gr[1][tt >> 1] = p1; }
          else { actp[0][(tt >> 1) & 3] = p0; actp[1][(tt >> 1) & 3] = p1; }
        }
      }
      if constexpr (op_st(R, n)) {
        constexpr int o = (ks - 7) >> 1, part = (ks - 7) & 1;
        if constexpr (ks == 7) { const int l = lane_now(); sto = (l ^ (8 * (l >> 5))) * 16; }
        if constexpr (ks == 9) sto ^= 64;
        gstore_sc1(amine + (2 * pb + part) * 16384 + o * 1024, sto, o ? actp[part] : gr[part]);
      }
    }
    if constexpr (SSM && ks == 9 && (s == 5 || s == 11 || (R == 2 && s == 17))) Lam = lds_rd8(tabb + ((s - 5) / 6) * 512);
    if constexpr (n == 8) { SCHED(); WAIT_V(ops_between(R, 0, 8)); BAR(); }
    if constexpr (ks == 11 && s < 34) {
      SCHED();
      if constexpr (s == 1 || s == 3) WAIT_L0();
      if constexpr (SSM && (ph == 0 || ph == 3 || s >= 17)) WAIT_L0();
      WAIT_V(drain(R, s) ? 0 : vm_n(R, s == 0 ? 2 : 12 * s - 1, n));
      if constexpr (chk_need(R, s) != 0) {
        if (w == 0) {
          constexpr unsigned need = chk_need(R, s);
          constexpr unsigned long long lanes = (R == 1 && s >= 26) ? 8ull : 7ull;
          if constexpr (SSM) WAIT_V(ops_between(R, 12 * s, n));
          pin2(flg);
          auto late = [&]() INL { return (__builtin_amdgcn_ballot_w64(flg[1] != tag || flg[0] < need) & lanes) != 0ull; };
          if (__builtin_expect(late(), 0)) {
            const int l = lane_now();
            int spins = 200000;
            do { flg = gload8_sc1(flagb, (l & 3) * 128); wait0_2(flg); } while (late() && --spins > 0);
          }
        }
      }
      BAR();
      STAMP(5 + s);
    }
    SCHED();
  };
  run_seq<0>(step, __make_integer_seq<ISeq, int, 216>{});
  run_seq<216>(step, __make_integer_seq<ISeq, int, R == 2 ? 71 : 216>{});
  if constexpr (R != 2) { pstore(2, 0); pstore(2, 1); }
  STAMP(39);
  WAIT_L0();
  WAIT_V(0);
  BAR();
  STAMP(40);
  if (w == 1) gstore8_sc1(flagb + mi * 128, 0u, u32x2{F_FIN, tag});
  if constexpr (SSM) {
    if (c == 15 && w == 0 && pvo == 0) *(unsigned*)(ws + WS_EP2 + mi * 128) = tag2;
  } else {
    const int l = lane_now(), t = w * 64 + l;
    const int c4 = t & 15, rho = c4 >> 1;
    const unsigned ra = lds0 + (c4 >> 3) * 2048 + (rho & 1) * 1024 + ((c4 >> 2) & 1) * 512 + (((t >> 4) ^ (2 * rho)) * 16) + (c4 & 1) * 8;
    float* op = P.out + (size_t)(128 * c + 32 * gq + (t >> 4)) * D + 192 * mi + c4 * 4;
    if (!got) {
      int spins = 400000;
      do { flg = gload8_sc1(ffl, 0u); wait0_2(flg); } while (!fin_ready() && --spins > 0);
      fin_fetch();
    }
    STAMP(41);
    float sk[12], xf[12], zo[12];
    pin2(fx[0]); pin2(fx[1]); pin2(fx[2]); pin2(fdk[0]); pin2(fdk[1]); pin2(fdk[2]); pin2(fst);
#pragma unroll
    for (int e = 0; e < 12; ++e) {
      const unsigned xv = fx[e >> 2][(e >> 1) & 1], dv = fdk[e >> 2][(e >> 1) & 1];
      const unsigned ov = *(LAS unsigned*)(ra + ARING + 32768 + (e >> 2) * 4096 + ((e >> 1) & 1) * 4);
      xf[e] = (e & 1) ? bfhi(xv) : bflo(xv);
      sk[e] = __builtin_fmaf(xf[e], __uint_as_float(fst[0]), __uint_as_float(fst[1])) * ((e & 1) ? bfhi(dv) : bflo(dv)) * 0.693147180559945f;
      zo[e] = (e & 1) ? bfhi(ov) : bflo(ov);
      opaquef(xf[e]); opaquef(sk[e]); opaquef(zo[e]);
    }
    STAMP(42);
    WAIT_V(0);
    STAMP(43);
    BAR();
    STAMP(44);
#pragma unroll
    for (int cb = 0; cb < 3; ++cb) {
      u32x2 pv[4];
#pragma unroll
      for (int k = 0; k < 4; ++k) pv[k] = *(LAS u32x2*)(ra + k * 12288 + cb * 4096);
      f32x4 o;
#pragma unroll
      for (int e = 0; e < 4; ++e) {
        auto up = [&](unsigned v) INL { return (e & 1) ? bfhi(v) : bflo(v); };
        const float z = (zo[4 * cb + e] + up(pv[0][e >> 1])) + (up(pv[1][e >> 1]) + up(pv[2][e >> 1]));
        const float gt = z * __builtin_amdgcn_rcpf(1.f + __builtin_amdgcn_exp2f(-z));
        o[e] = __builtin_fmaf(__builtin_fmaf(up(pv[3][e >> 1]), 0.693147180559945f, sk[4 * cb + e]), gt, xf[4 * cb + e]);
      }
      asm volatile("global_store_dwordx4 %0, %1, off sc1\n\ts_nop 1" ::"v"(op + 64 * cb), "v"(o) : "memory");
    }
    if (j == 0 && t == 0) *(unsigned*)(wc + C_FLAG + 16 * 128) = tag;
  }
  if constexpr (R == 2) asm volatile("fk_hlp_end:"); else if constexpr (R == 1) asm volatile("fk_ssm_end:"); else asm volatile("fk_mlp_end:");
  if constexpr (SSM) { STAMP(41); STAMP(42); STAMP(43); STAMP(44); }
  STAMP(45);
  STAMP(46);
  RSTAMP(61);
  STAMP_OUT();
}

__global__ __launch_bounds__(512, 2) void main_kernel(const Params P) {
  __shared__ __attribute__((aligned(16))) char smem[163840];
  const unsigned lds0 = (unsigned)(size_t)(LAS char*)smem;
  const int b = blockIdx.x, xcd = b & 7, idx = b >> 3;
#define MAP 1
  int j, c;
  if (MAP == 0) { j = idx >> 1; c = 2 * xcd + (idx & 1); }
  else if (idx >= 24) { j = 12 + ((idx - 24) >> 1); c = 2 * xcd + (idx & 1); }
  else if (MAP == 1) { j = 6 * (xcd & 1) + (idx >> 2); c = 4 * (xcd >> 1) + (idx & 3); }
  else { j = 3 * (xcd & 3) + (idx >> 3); c = 8 * (xcd >> 2) + (idx & 7); }
#if defined(__HIP_DEVICE_COMPILE__)
  if (j < 12) body<0>(P, lds0, c, j);
  else if (j < 15) body<1>(P, lds0, c, j);
  else body<2>(P, lds0, c, j);
#endif
}

extern "C" void kernel_launch(void* const* d_in, const int* in_sizes, int n_in, void* d_out, int out_size, void* d_ws, size_t ws_size,
                              hipStream_t stream) {
  Params P;
  P.x = (const u16*)d_in[0]; P.lre = (const u16*)d_in[1]; P.lim = (const u16*)d_in[2];
  P.bre = (const u16*)d_in[3]; P.bim = (const u16*)d_in[4]; P.cre = (const u16*)d_in[5]; P.cim = (const u16*)d_in[6];
  P.ldt = (const u16*)d_in[7]; P.w1 = (const u16*)d_in[10]; P.b1 = (const u16*)d_in[11]; P.w2 = (const u16*)d_in[12];
  P.dsk = (const u16*)d_in[8]; P.ws = (char*)d_ws; P.out = (float*)d_out;
  main_kernel<<<256, 512, 0, stream>>>(P);
}
```

```cpp
#include <hip/hip_runtime.h>
#include <hip/hip_bf16.h>
#pragma clang diagnostic ignored "-Wint-to-pointer-cast"

typedef unsigned short u16;
typedef __attribute__((ext_vector_type(2))) __bf16 bf16x2;
typedef __attribute__((ext_vector_type(4))) __bf16 bf16x4;
typedef __attribute__((ext_vector_type(8))) __bf16 bf16x8;
typedef __attribute__((ext_vector_type(4))) short s16x4;
typedef __attribute__((ext_vector_type(2))) float f32x2;
typedef __attribute__((ext_vector_type(4))) float f32x4;
typedef __attribute__((ext_vector_type(16))) float f32x16;
typedef __attribute__((ext_vector_type(2))) unsigned u32x2;
typedef __attribute__((ext_vector_type(4))) unsigned u32x4;
#define LAS __attribute__((address_space(3)))
#define GAS __attribute__((address_space(1)))
#define DI __device__ __forceinline__
#define INL __attribute__((always_inline))

constexpr int S = 2048, D = 576, F = 4608;
constexpr float LOG2E = 1.44269504088896f;
constexpr int SLOT = 24576;
constexpr int X_OFF = 4 * SLOT;
constexpr int ARING = X_OFF;
constexpr int TAB_OFF = ARING + 49152;
constexpr int LT_OFF = TAB_OFF;
constexpr int WT_OFF = TAB_OFF + 6144;
constexpr int JUNK_OFF = TAB_OFF + 14336;
constexpr int GT_OFF = TAB_OFF + 10240;
constexpr size_t WS_EPOCH = 0;
constexpr size_t WS_GRAN = 32768;
constexpr size_t WS_CSUM = 196608;
constexpr size_t WS_C0 = 327680, WS_CS = 2318336;
constexpr int C_FLAG = 0;
constexpr int C_STAT = 4096;
constexpr int C_ACT = 8192;
constexpr int C_PART = C_ACT + 1572864;

DI float bflo(unsigned v) { return __uint_as_float(v << 16); }
DI float bfhi(unsigned v) { return __uint_as_float(v & 0xffff0000u); }
DI unsigned pk2(float a, float b) { f32x2 v = {a, b}; return __builtin_bit_cast(unsigned, __builtin_convertvector(v, bf16x2)); }
#define WAIT_V(n) asm volatile("s_waitcnt vmcnt(%0)" ::"n"(n) : "memory")
#define WAIT_L0() asm volatile("s_waitcnt lgkmcnt(0)" ::: "memory")
#define BAR() asm volatile("s_barrier" ::: "memory")
#define SCHED() __builtin_amdgcn_sched_barrier(0)
#define BF8(x) __builtin_bit_cast(bf16x8, (x))
#define MFMA16(a, b, c) __builtin_amdgcn_mfma_f32_16x16x32_bf16((a), (b), (c), 0, 0, 0)
#define MFMA32(a, b, c) __builtin_amdgcn_mfma_f32_32x32x16_bf16((a), (b), (c), 0, 0, 0)
template <int I> struct IC { static constexpr int value = I; };
template <int B, int E, class Fn> DI void static_for(Fn&& f) { if constexpr (B < E) { f(IC<B>{}); static_for<B + 1, E>(f); } }

template <class T, T... I> struct ISeq {};
template <int B, class Fn, int... I> DI void run_seq(Fn&& f, ISeq<int, I...>) { (f(IC<B + I>{}), ...); }

DI void gldsW(const void* sbase, unsigned voff, unsigned lds_addr) {
  asm volatile("s_mov_b32 m0, %2\n\ts_nop 0\n\tglobal_load_lds_dwordx4 %0, %1" ::"v"(voff), "s"(sbase), "s"(lds_addr) : "memory");
}
DI void gldsA(const void* sbase, unsigned voff, unsigned lds_addr) {
  asm volatile("s_mov_b32 m0, %2\n\ts_nop 0\n\tglobal_load_lds_dwordx4 %0, %1 sc1" ::"v"(voff), "s"(sbase), "s"(lds_addr) : "memory");
}
DI void glds4(const void* sbase, unsigned voff, unsigned lds_addr) {
  asm volatile("s_mov_b32 m0, %2\n\ts_nop 0\n\tglobal_load_lds_dword %0, %1" ::"v"(voff), "s"(sbase), "s"(lds_addr) : "memory");
}
DI void gstore_sc1(const void* sbase, unsigned voff, u32x4 v) {
  asm volatile("global_store_dwordx4 %0, %1, %2 sc1\n\ts_nop 1" ::"v"(voff), "v"(v), "s"(sbase) : "memory");
}
DI void gstore8_sc1(const void* sbase, unsigned voff, u32x2 v) {
  asm volatile("global_store_dwordx2 %0, %1, %2 sc1\n\ts_nop 1" ::"v"(voff), "v"(v), "s"(sbase) : "memory");
}
template <int OFF> DI u32x4 gload16(const void* sbase, unsigned voff) {
  u32x4 v; asm volatile("global_load_dwordx4 %0, %1, %2 offset:%3" : "=v"(v) : "v"(voff), "s"(sbase), "n"(OFF) : "memory"); return v;
}
DI u32x4 gload16_sc1(const void* sbase, unsigned voff) {
  u32x4 v; asm volatile("global_load_dwordx4 %0, %1, %2 sc1" : "=v"(v) : "v"(voff), "s"(sbase) : "memory"); return v;
}
DI u32x2 gload8_sc1(const void* sbase, unsigned voff) {
  u32x2 v; asm volatile("global_load_dwordx2 %0, %1, %2 sc1" : "=v"(v) : "v"(voff), "s"(sbase) : "memory"); return v;
}
DI u32x2 gload8(const void* sbase, unsigned voff) {
  u32x2 v; asm volatile("global_load_dwordx2 %0, %1, %2" : "=v"(v) : "v"(voff), "s"(sbase) : "memory"); return v;
}
DI unsigned gload4_sc1(const void* sbase, unsigned voff) {
  unsigned v; asm volatile("global_load_dword %0, %1, %2 sc1" : "=v"(v) : "v"(voff), "s"(sbase) : "memory"); return v;
}
DI unsigned gload2(const void* sbase, unsigned voff) {
  unsigned v; asm volatile("global_load_ushort %0, %1, %2" : "=v"(v) : "v"(voff), "s"(sbase) : "memory"); return v;
}
template <int N> DI void wait1(u32x4& a) { asm volatile("s_waitcnt vmcnt(%1)" : "+v"(a) : "n"(N) : "memory"); }
template <int N, int X> DI void wait1x(u32x4& a, unsigned more) {
  asm volatile("s_waitcnt vmcnt(%2)\n\ts_cmp_eq_u32 %1, 0\n\ts_cbranch_scc1 1f\n\ts_waitcnt vmcnt(%3)\n1:" : "+v"(a) : "s"(more), "n"(N + X), "n"(N) : "memory", "scc");
}
template <int N> DI void wait3(u32x4& a, u32x4& b, u32x4& c) { asm volatile("s_waitcnt vmcnt(%3)" : "+v"(a), "+v"(b), "+v"(c) : "n"(N) : "memory"); }
template <int N> DI void wait2(u32x4& a, u32x4& b) { asm volatile("s_waitcnt vmcnt(%2)" : "+v"(a), "+v"(b) : "n"(N) : "memory"); }
template <int N> DI void wait4(u32x2& a, u32x2& b, u32x2& c, u32x2& d) { asm volatile("s_waitcnt vmcnt(%4)" : "+v"(a), "+v"(b), "+v"(c), "+v"(d) : "n"(N) : "memory"); }
template <int N> DI void waitn2(u32x2& a) { asm volatile("s_waitcnt vmcnt(%1)" : "+v"(a) : "n"(N) : "memory"); }
template <int N> DI void wait5(u32x2& a, u32x2& b, u32x2& c, u32x2& d, u32x2& e) { asm volatile("s_waitcnt vmcnt(%5)" : "+v"(a), "+v"(b), "+v"(c), "+v"(d), "+v"(e) : "n"(N) : "memory"); }
DI void reload2(u32x4& a, u32x4& b, const void* pa, const void* pb, unsigned voff) {
  asm volatile("global_load_dwordx4 %0, %2, %3 sc1\n\tglobal_load_dwordx4 %1, %2, %4 sc1\n\ts_waitcnt vmcnt(0)" : "+&v"(a), "+&v"(b) : "v"(voff), "s"(pa), "s"(pb) : "memory");
}
DI float msel(unsigned v, unsigned long long m) { float o; asm("v_cndmask_b32 %0, 0, %1, %2" : "=v"(o) : "v"(v), "s"(m)); return o; }
DI float bsel(float lo, float hi, unsigned long long m) { float o; asm("v_cndmask_b32 %0, %1, %2, %3" : "=v"(o) : "v"(lo), "v"(hi), "s"(m)); return o; }
DI unsigned msel_u(unsigned v, unsigned long long m) { unsigned o; asm("v_cndmask_b32 %0, 0, %1, %2" : "=v"(o) : "v"(v), "s"(m)); return o; }
template <int N> DI void waitg(u32x4& a, u32x2& b, u32x2& c) { asm volatile("s_waitcnt vmcnt(%3)" : "+v"(a), "+v"(b), "+v"(c) : "n"(N) : "memory"); }
DI void reloadg(u32x4& a, u32x2& b, u32x2& c, const void* pa, const void* pb, const void* pc, unsigned oa, unsigned ob) {
  asm volatile("global_load_dwordx4 %0, %3, %5 sc1\n\tglobal_load_dwordx2 %1, %4, %6 sc1\n\tglobal_load_dwordx2 %2, %4, %7 sc1\n\ts_waitcnt vmcnt(0)"
               : "+&v"(a), "+&v"(b), "+&v"(c) : "v"(oa), "v"(ob), "s"(pa), "s"(pb), "s"(pc) : "memory");
}
DI void pin2(u32x2& a) { asm volatile("" : "+v"(a)::"memory"); }
DI void wait0_2(u32x2& a) { asm volatile("s_waitcnt vmcnt(0)" : "+v"(a)::"memory"); }
DI void opaque(unsigned& a) { asm volatile("" : "+v"(a)); }
DI void opaquef(float& a) { asm volatile("" : "+v"(a)); }
DI int lane_now() { int l; asm volatile("v_mbcnt_lo_u32_b32 %0, -1, 0\n\tv_mbcnt_hi_u32_b32 %0, -1, %0" : "=v"(l)); return l; }
DI void opaque16(f32x16& a) { asm volatile("" : "+v"(a)); }
DI void opaquec(f32x2& a) { float p = a[0], q = a[1]; asm volatile("" : "+v"(p), "+v"(q)); a[0] = p; a[1] = q; }
DI bf16x8 tr_frag(unsigned a0, unsigned a1) {
  bf16x4 lo = __builtin_amdgcn_ds_read_tr16_b64_v4bf16((LAS bf16x4*)a0);
  bf16x4 hi = __builtin_amdgcn_ds_read_tr16_b64_v4bf16((LAS bf16x4*)a1);
  return __builtin_shufflevector(lo, hi, 0, 1, 2, 3, 4, 5, 6, 7);
}
DI u32x4 lds_rd(unsigned a) { return *(LAS u32x4*)a; }
DI void lds_wr(unsigned a, u32x4 v) { *(LAS u32x4*)a = v; }
DI f32x2 lds_rd8(unsigned a) { return *(LAS f32x2*)a; }
DI void lds_wr8(unsigned a, f32x2 v) { *(LAS f32x2*)a = v; }
DI u32x4 norm16(u32x4 v, float a, float b) {
  unsigned a0 = v[0], a1 = v[1], a2 = v[2], a3 = v[3], t0, t1, t2, t3;
  asm("v_lshlrev_b32 %4, 16, %0\n\tv_lshlrev_b32 %5, 16, %1\n\tv_lshlrev_b32 %6, 16, %2\n\tv_lshlrev_b32 %7, 16, %3\n\t"
      "v_and_b32 %0, 0xffff0000, %0\n\tv_and_b32 %1, 0xffff0000, %1\n\tv_and_b32 %2, 0xffff0000, %2\n\tv_and_b32 %3, 0xffff0000, %3\n\t"
      "v_fma_f32 %4, %4, %8, %9\n\tv_fma_f32 %5, %5, %8, %9\n\tv_fma_f32 %6, %6, %8, %9\n\tv_fma_f32 %7, %7, %8, %9\n\t"
      "v_fma_f32 %0, %0, %8, %9\n\tv_fma_f32 %1, %1, %8, %9\n\tv_fma_f32 %2, %2, %8, %9\n\tv_fma_f32 %3, %3, %8, %9\n\t"
      "v_cvt_pk_bf16_f32 %0, %4, %0\n\tv_cvt_pk_bf16_f32 %1, %5, %1\n\tv_cvt_pk_bf16_f32 %2, %6, %2\n\tv_cvt_pk_bf16_f32 %3, %7, %3"
      : "+v"(a0), "+v"(a1), "+v"(a2), "+v"(a3), "=&v"(t0), "=&v"(t1), "=&v"(t2), "=&v"(t3) : "v"(a), "v"(b));
  return u32x4{a0, a1, a2, a3};
}
DI unsigned norm4(unsigned a, float sc, float bi) {
  unsigned t;
  asm volatile("v_lshlrev_b32 %1, 16, %0\n\tv_and_b32 %0, 0xffff0000, %0\n\tv_fma_f32 %1, %1, %2, %3\n\tv_fma_f32 %0, %0, %2, %3\n\tv_cvt_pk_bf16_f32 %0, %1, %0"
               : "+v"(a), "=&v"(t) : "v"(sc), "v"(bi));
  return a;
}
template <int CTRL> DI float dpp_add(float a) {
  return a + __builtin_bit_cast(float, __builtin_amdgcn_update_dpp(0, __builtin_bit_cast(int, a), CTRL, 0xf, 0xf, true));
}
DI f32x2 cfma(f32x2 a, f32x2 b, f32x2 c) {
  f32x2 o;
  o[0] = __builtin_fmaf(a[0], b[0], __builtin_fmaf(-a[1], b[1], c[0]));
  o[1] = __builtin_fmaf(a[0], b[1], __builtin_fmaf(a[1], b[0], c[1]));
  return o;
}
DI f32x2 cfma_s(f32x2 a, f32x2 b, f32x2 c) {
  float o0, o1;
  asm("v_fma_f32 %0, -%3, %5, %6\n\tv_fma_f32 %1, %3, %4, %7\n\tv_fmac_f32 %0, %2, %4\n\tv_fmac_f32 %1, %2, %5"
      : "=&v"(o0), "=&v"(o1) : "v"(a[0]), "v"(a[1]), "v"(b[0]), "v"(b[1]), "v"(c[0]), "v"(c[1]));
  return f32x2{o0, o1};
}
DI f32x2 cmul_s(f32x2 a, f32x2 b) {
  float o0, o1;
  asm("v_mul_f32_e64 %0, -%3, %5\n\tv_mul_f32 %1, %3, %4\n\tv_fmac_f32 %0, %2, %4\n\tv_fmac_f32 %1, %2, %5"
      : "=&v"(o0), "=&v"(o1) : "v"(a[0]), "v"(a[1]), "v"(b[0]), "v"(b[1]));
  return f32x2{o0, o1};
}
DI float usel(float lo, float hi) { float o; asm("v_cndmask_b32 %0, %1, %2, %3" : "=v"(o) : "v"(lo), "v"(hi), "s"(0xffffffff00000000ull)); return o; }
DI f32x2 csq(f32x2 a) { f32x2 o; o[0] = __builtin_fmaf(a[0], a[0], -a[1] * a[1]); o[1] = 2.f * a[0] * a[1]; return o; }
DI float sum16(float v) {
  auto t = __builtin_amdgcn_permlane16_swap(__float_as_uint(v), __float_as_uint(v), false, false);
  return __uint_as_float(t[0]) + __uint_as_float(t[1]);
}
DI float up32(float v) {
  auto t = __builtin_amdgcn_permlane32_swap(0u, __float_as_uint(v), false, false);
  return __uint_as_float(t[0]);
}

#define STAMP(i)
#define RSTAMP(i)
#define STAMP_OUT()

constexpr int KM = 3;
constexpr int km(int R) { return R == 0 ? KM : 2; }
constexpr int p1_tile(int R, int s) { return s < 3 * km(R) ? s % km(R) : s / 3; }
constexpr int p1_kt(int R, int s) { return s < 3 * km(R) ? s / km(R) : s % 3; }
constexpr int p2_cb(int n) { return n >= 420 ? (n - 420) / 4 : (n % 12) % 3; }
constexpr int p2_k4(int n) { return n >= 420 ? (n - 420) % 4 : (n % 12) / 3; }
constexpr int EPI0 = 90;
constexpr int corr_tile(int n) { return n == 87 ? 0 : n == 97 ? 1 : n == 109 ? 2 : n == 145 ? 3 : n == 181 ? 4 : n == 217 ? 5 : -1; }
constexpr int st_slot(int T, int o) { int r = EPI0 + 33 * T + (o ? 33 : 17); return (r % 12 <= 7) ? r / 12 : r / 12 + 1; }
constexpr int st_id(int s) { for (int i = 0; i < 12; ++i) if (st_slot(i >> 1, i & 1) == s) return i; return -1; }
constexpr bool op_w(int R, int n) {
  const int s = n / 12, k = n % 12;
  if ((R == 1 && s >= 9 && s <= 14) || (R == 2 && s >= 15)) return false;
  return (R != 0 && s == 0) ? k < 6 : (s <= 32 && k % 3 == 0 && k <= 6);
}
constexpr int x_piece(int n) { const int t = n % 36; return n >= 72 ? -1 : (t >= 9 && t <= 11) ? t - 9 : t == 13 ? 3 : t == 14 ? 4 : t == 16 ? 5 : -1; }
constexpr bool op_x(int R, int n) { return R == 0 && x_piece(n) >= 0; }
constexpr bool op_a(int R, int n) {
  const int s = n / 12, k = n % 12;
  if (R == 2) return false;
  if (R == 1) return (k == 2 && s >= 16 && s <= 32) || (k == 5 && s >= 15 && s <= 32) || n == 12 * 32 + 4;
  return (k == 2 || k == 5) && s >= 15 && s <= 32;
}
constexpr bool op_pf(int R, int n) { return n % 12 == 8 && (R == 1 ? n / 12 <= 6 : R == 0 && n / 12 >= 3 && n / 12 <= 6); }
constexpr bool op_cp(int R, int n) { const int s = n / 12; return n % 12 == 5 && s >= 7 && s <= 28 && s % 7 == 0 && !(R == 1 && s == 14); }
constexpr bool op_st(int R, int n) {
  const int s = n / 12, k = n % 12;
  if (R) return (s == 11 || (R == 1 ? s == 20 : s == 17 || s == 23)) && k >= 7 && k <= 10;
  return k == 7 && st_id(s) >= 0;
}
constexpr int cs_piece(int n) { return n == 1 ? 0 : n == 2 ? 1 : n == 4 ? 2 : n == 5 ? 3 : n == 7 ? 4 : -1; }
constexpr bool op_cs(int R, int n) { return R == 0 && cs_piece(n) >= 0; }
constexpr bool op_fo(int R, int n) { return R == 0 && n % 12 == 8 && n / 12 >= 25 && n / 12 <= 31; }
constexpr bool drain(int R, int s) { return R == 2 && s == 18; }
constexpr int n_fill(int R, int n) { return op_w(R, n) + op_x(R, n) + op_a(R, n); }
constexpr int n_lazy(int R, int n) { return op_pf(R, n) + op_cp(R, n) + op_st(R, n) + op_fo(R, n) + op_cs(R, n); }
constexpr int last_fill(int R, int L) { while (L >= 0 && n_fill(R, L) == 0) --L; return L; }
constexpr int ops_between(int R, int a, int e) { int n = 0; for (int i = a; i <= e; ++i) n += n_fill(R, i) + n_lazy(R, i); return n; }
constexpr int vm_n(int R, int L, int e) { const int l = last_fill(R, L); return (l >= 0 ? n_lazy(R, l) : 0) + ops_between(R, l + 1, e); }
constexpr int done_slot(int R, int m) { int s = m / 12; while (!drain(R, s) && last_fill(R, 12 * s - 1) <= m && s < 40) ++s; return s + 1; }
constexpr int pub_at(int R, int s) {
  if (R == 0) { for (int T = 0; T < 6; ++T) if (done_slot(0, 12 * st_slot(T, 1) + 7) == s) return T + 2; }
  else if (R == 1) { if (done_slot(1, 12 * 20 + 10) == s) return 3; }
  else { for (int b = 0; b < 2; ++b) if (done_slot(R, 12 * (11 + 6 * b) + 10) == s) return b + 2; }
  return 0;
}
static_assert(done_slot(0, 12 * 31 + 8) <= 34, "schedule");
static_assert(pub_at(1, 23) == 3 && pub_at(2, 14) == 2 && pub_at(2, 19) == 3, "schedule");
static_assert(done_slot(0, 12 * st_slot(0, 1) + 7) <= 13 && done_slot(0, 12 * st_slot(5, 1) + 7) <= 27, "schedule");
constexpr unsigned chk_need(int R, int s) {
  if (R == 0) return (s >= 15 && s <= 30 && (s - 15) % 3 == 0) ? (s - 15) / 3 + 2 : 0;
  if (R == 1) return s == 16 ? 2 : s == 24 ? 3 : s == 20 ? 2 : s == 28 ? 3 : s == 30 ? 4 : 0;
  return 0;
}
constexpr int u_hlp(int u) { return (u == 6 || u == 7) ? 0 : u >= 14 ? (u - 12) >> 1 : -1; }
constexpr int u_mem(int u) { return (u < 6 ? u : u - 8) >> 1; }
constexpr int u_blk(int u) { return u < 6 ? 0 : 1; }
constexpr unsigned F_FIN = 8;

struct Params {
  const u16 *x, *lre, *lim, *bre, *bim, *cre, *cim, *ldt, *dsk, *w1, *b1, *w2;
  char* ws;
  float* out;
};

template <int R>
DI void body(const Params& P, const unsigned lds0, const int c, const int j) {
  constexpr bool SSM = R != 0;
  constexpr int PSTR = R == 2 ? 192 : 64;
  char* const ws = P.ws;
  const int tid = threadIdx.x, lane = tid & 63;
  const int w = __builtin_amdgcn_readfirstlane(tid >> 6);
  const int rg = w & 3, set = w >> 2;
  const int r = lane & 31, h = lane >> 5;
  unsigned stampv = 0; (void)stampv;
  RSTAMP(60); STAMP(0);
  const int gq = SSM ? 4 : j / 3, mi = j - 3 * gq;

  char* const wc = ws + WS_C0 + (size_t)c * WS_CS;
  u32x4 xr[2][9];
  unsigned aux[3], epoch, tag;
  const int rb0 = 8 * w, rb1_ = rb0 + 64;
  const char* xb1 = (const char*)P.x + (size_t)(128 * c + rb1_) * (D * 2);
  const unsigned xoff = (lane >> 3) * (D * 2) + (lane & 7) * 16;
  const char* xb0 = (const char*)P.x + (size_t)(128 * c + rb0) * (D * 2);
  unsigned voffx = (lane >> 3) * (D * 2) + (((lane & 7) ^ ((4 * (w & 1) + (lane >> 4)) & 7)) * 16);
  auto dmaX = [&](int third, int i) INL { gldsW(xb0 + (i & 1) * 64 * (D * 2) + (3 * third + (i >> 1)) * 128, voffx, lds0 + X_OFF + (i >> 1) * 16384 + (i & 1) * 8192 + w * 1024); };
  constexpr int NAUX = 4;
  if constexpr (SSM) {
    __builtin_amdgcn_s_setprio(2);
    static_for<0, 9>([&](auto I) INL { xr[0][I.value] = gload16<I.value * 128>(xb0, xoff); });
    epoch = gload4_sc1(ws + WS_EPOCH, 0u);
    const unsigned o = (R == 2 ? 128 + 192 * (w < 3 ? w : 0) + lane : 192 * mi + (tid < 192 ? tid : 0)) * 2;
    aux[0] = gload2(P.lre, o); aux[1] = gload2(P.lim, o); aux[2] = gload2(P.ldt, o);
  } else {
    __builtin_amdgcn_s_setprio(2);
#pragma unroll
    for (int i = 0; i < 6; ++i) dmaX(0, i);
  }

  unsigned pvo = lane * 16;
  unsigned voff1, voff2;
  const char *wa[2], *wb[3][2], *acm[3];
  if constexpr (SSM) {
    voff1 = voff2 = voffx;
    wa[0] = (const char*)P.bre + (size_t)((R == 2 ? 128 : 192 * mi) + 8 * w) * (D * 2);
    wa[1] = (const char*)P.bim + (size_t)((R == 2 ? 128 : 192 * mi) + 8 * w) * (D * 2);
  } else {
    const unsigned swz = ((lane & 7) ^ (((lane >> 4) & 1) << 2)) * 16;
    voff1 = (lane >> 3) * (F * 2) + swz;
    voff2 = (lane >> 3) * (D * 2) + swz;
    wa[0] = wa[1] = (const char*)P.w1 + ((size_t)(8 * w) * F + 384 * j) * 2;
  }
#pragma unroll
  for (int k = 0; k < 3; ++k) {
    const int mm = (mi + k >= 3) ? mi + k - 3 : mi + k;
    if constexpr (SSM) {
      wb[k][0] = (const char*)P.cre + (size_t)(192 * mi + 8 * w) * (D * 2) + 384 * mm;
      wb[k][1] = (const char*)P.cim + (size_t)(192 * mi + 8 * w) * (D * 2) + 384 * mm;
    } else {
      wb[k][0] = wb[k][1] = (const char*)P.w2 + ((size_t)(384 * (3 * gq + mm) + 8 * w) * D + 192 * mi) * 2;
    }
    acm[k] = wc + C_ACT + ((3 * gq + mm) * 6) * 16384 + w * 1024;
  }
  auto dmaW = [&](int s, int i) INL {
    const unsigned dst = lds0 + (s & 3) * SLOT + w * 1024 + i * 8192;
    const int u = s - 18;
    if constexpr (SSM) {
      if (s < 18) gldsW(wa[p1_tile(R, s) & 1] + (PSTR * (p1_tile(R, s) >> 1)) * (D * 2) + (192 * p1_kt(R, s) + 64 * i) * 2, voff1, dst);
      else if (u_hlp(u) < 0) gldsW(wb[u_mem(u)][u & 1] + (64 * i) * (D * 2) + 128 * u_blk(u), voff2, dst);
      else gldsW(wb[0][u & 1] - 384 * mi + (64 * i) * (D * 2) + 384 * u_hlp(u) + 256, voff2, dst);
    } else {
      if (s < 18) gldsW(wa[0] + ((size_t)(192 * p1_kt(R, s) + 64 * i) * F + 64 * p1_tile(R, s)) * 2, voff1, dst);
      else gldsW(wb[u % 3][0] + ((size_t)(64 * (u / 3)) * D + 64 * i) * 2, voff2, dst);
    }
  };
  auto code_touch = [&](int lines_per_wave_log2, unsigned skip, unsigned dst) INL {
    unsigned long long pc; unsigned len;
    if constexpr (R == 2) asm volatile("s_getpc_b64 %0\n\ts_mov_b32 %1, fk_hlp_end-." : "=s"(pc), "=s"(len));
    else if constexpr (R == 1) asm volatile("s_getpc_b64 %0\n\ts_mov_b32 %1, fk_ssm_end-." : "=s"(pc), "=s"(len));
    else asm volatile("s_getpc_b64 %0\n\ts_mov_b32 %1, fk_mlp_end-." : "=s"(pc), "=s"(len));
    const unsigned co = skip + (((unsigned)w << lines_per_wave_log2) + ((unsigned)lane_now() >> (6 - lines_per_wave_log2))) * 128;
    glds4((const void*)pc, co < len ? co : 0u, dst);
  };
  const unsigned ximg = (lane >> 3) * 128 + (((lane & 7) ^ ((4 * (w & 1) + (lane >> 4)) & 7)) * 16);
  float rs[2], nb[2], mn[2];
  unsigned xw = lds0 + X_OFF + rb0 * 128 + ximg;
  auto xwrite1 = [&](int ps, int i) INL { lds_wr(xw + ps * 8192 + (i % 3) * 16384, xr[ps][i]); };
  auto stats = [&](auto PI) INL {
    constexpr int ps = PI.value;
    f32x4 s1 = {}, s2 = {}, s3 = {};
    const unsigned selv = (((lane >> 4) & 3) == (lane & 3)) ? 0x3f803f80u : 0u;
    const u32x4 sel = {selv, selv, selv, selv};
    static_for<0, 9>([&](auto GI) INL {
      constexpr int i = GI.value;
      if constexpr (ps == 0) { xr[1][i] = gload16<i * 128>(xb1, xoff); if constexpr (i == 8) __builtin_amdgcn_s_setprio(0); wait1<9 + NAUX>(xr[0][i]); }
      else wait1<8 - i + 4>(xr[1][i]);
      const u32x2 t0 = {xr[ps][i][0], xr[ps][i][1]}, t1 = {xr[ps][i][2], xr[ps][i][3]};
      s2 = __builtin_amdgcn_mfma_f32_4x4x4bf16_1k(__builtin_bit_cast(s16x4, t0), __builtin_bit_cast(s16x4, t0), s2, 0, 0, 0);
      s3 = __builtin_amdgcn_mfma_f32_4x4x4bf16_1k(__builtin_bit_cast(s16x4, t1), __builtin_bit_cast(s16x4, t1), s3, 0, 0, 0);
      s1 = MFMA16(BF8(sel), BF8(xr[ps][i]), s1);
      SCHED();
    });
    if constexpr (ps == 0) { dmaW(0, 0); dmaW(0, 1); dmaW(0, 2); code_touch(5, 0, lds0 + JUNK_OFF); }
    s2 += s3;
    const float e0 = s2[0], e1 = s2[1], e2 = s2[2], e3 = s2[3];
    const float lo2 = (lane & 1) ? e1 : e0, hi2 = (lane & 1) ? e3 : e2;
    float a2 = (lane & 2) ? hi2 : lo2;
    const float f0 = s1[0], f1 = s1[1], f2 = s1[2], f3 = s1[3];
    const float lo1 = (lane & 16) ? f1 : f0, hi1 = (lane & 16) ? f3 : f2;
    float a1 = (lane & 32) ? hi1 : lo1;
    a1 = dpp_add<0xB1>(a1); a2 = dpp_add<0xB1>(a2);
    a1 = dpp_add<0x4E>(a1); a2 = dpp_add<0x4E>(a2);
    a1 = dpp_add<0x141>(a1); a2 = dpp_add<0x141>(a2);
    const float m = a1 * (1.f / D);
    const float var = fmaxf(a2 * (1.f / D) - m * m, 0.f);
    rs[ps] = __builtin_amdgcn_rsqf(var + 1e-5f);
    nb[ps] = -m * rs[ps];
    mn[ps] = m;
    if constexpr (ps == 1) {
      opaque(epoch);
      tag = __builtin_amdgcn_readfirstlane(epoch) + 1u;
      if (j == 12) {
        gstore_sc1(wc + C_STAT + rb0 * 16, (lane >> 3) * 16, u32x4{__float_as_uint(mn[0]), tag, __float_as_uint(rs[0]), tag});
        gstore_sc1(wc + C_STAT + rb1_ * 16, (lane >> 3) * 16, u32x4{__float_as_uint(mn[1]), tag, __float_as_uint(rs[1]), tag});
      }
    }
#pragma unroll
    for (int i = 0; i < 3; ++i) { xr[ps][i] = norm16(xr[ps][i], rs[ps], nb[ps]); xwrite1(ps, i); }
  };
  if constexpr (!SSM) {
    dmaW(0, 0); dmaW(0, 1); dmaW(0, 2);
    __builtin_amdgcn_s_setprio(0);
    dmaW(1, 0); dmaW(1, 1); dmaW(1, 2);
    dmaW(2, 0); dmaW(2, 1); dmaW(2, 2);
    epoch = gload4_sc1(ws + WS_EPOCH, 0u);
    aux[0] = gload2(P.b1, (384 * j + (tid < 384 ? tid : 0)) * 2);
    code_touch(5, 0, lds0 + JUNK_OFF);
    STAMP(1); STAMP(2);
  } else {
    stats(IC<0>{});
    STAMP(1);
    stats(IC<1>{});
    STAMP(2);
    dmaW(1, 0); dmaW(1, 1); dmaW(1, 2);
    opaque(aux[0]); opaque(aux[1]); opaque(aux[2]);
    if (w < 3) {
      const float lr = bflo(aux[0]), li = bflo(aux[1]);
      const float dt = __builtin_amdgcn_exp2f(bflo(aux[2]) * LOG2E);
      const float mag = __builtin_amdgcn_exp2f(dt * lr * LOG2E);
      const float rev = __builtin_amdgcn_fractf(dt * li * 0.15915494309189535f);
      f32x2 L = {mag * __builtin_amdgcn_cosf(rev), mag * __builtin_amdgcn_sinf(rev)};
      const float inv = 2.f / (lr * lr + li * li);
      const float nr = L[0] - 1.f, ni = L[1];
      const f32x2 c2 = {(nr * lr + ni * li) * inv, (ni * lr - nr * li) * inv};
      const f32x2 L16 = csq(csq(csq(csq(L))));
      const f32x2 L128 = csq(csq(csq(L16)));
      lds_wr8(lds0 + LT_OFF + tid * 8, L); lds_wr8(lds0 + LT_OFF + 1536 + tid * 8, c2);
      lds_wr8(lds0 + LT_OFF + 2 * 1536 + tid * 8, L16); lds_wr8(lds0 + LT_OFF + 3 * 1536 + tid * 8, L128);
    }
  }
  u32x2 flg;
  const int rperm = 16 * ((r >> 2) & 1) + (r & 3) + 4 * (r >> 3);
  bf16x8 xn[36];
  unsigned xn_ad[4];
  { const int row = 32 * rg + (SSM ? rperm : r); xn_ad[0] = lds0 + X_OFF + row * 128 + ((h ^ ((row >> 1) & 7)) * 16); }
  xn_ad[1] = xn_ad[0] ^ 32; xn_ad[2] = xn_ad[0] ^ 64; xn_ad[3] = xn_ad[0] ^ 96;
  auto xn_read = [&](int rd, int ks) INL { xn[12 * rd + ks] = BF8(lds_rd(xn_ad[ks & 3] + (ks >> 2) * 16384)); };

  const int G1 = (lane >> 4) & 1, q = (lane >> 2) & 3, p4 = lane & 3;
  unsigned rb1[4], rb2[4][2], ab[2];
  if constexpr (SSM) {
    const int row = 32 * set + r;
    rb1[0] = lds0 + row * 128 + ((h ^ ((row >> 1) & 7)) * 16);
    rb1[1] = rb1[0] ^ 32; rb1[2] = rb1[0] ^ 64; rb1[3] = rb1[0] ^ 96;
    opaque(rb1[0]); opaque(rb1[1]); opaque(rb1[2]); opaque(rb1[3]);
  } else {
    rb1[0] = lds0 + (8 * h + q) * 128 + (((set * 4 + G1 * 2 + (p4 >> 1)) ^ (((q >> 1) & 1) << 2)) * 16) + (p4 & 1) * 8;
    rb1[1] = rb1[0] + 2 * SLOT;
    opaque(rb1[0]); opaque(rb1[1]);
  }
  unsigned tabb = lds0 + TAB_OFF + (32 * set + r) * (SSM ? 8 : 16);
  opaque(tabb);
  unsigned tabh = tabb + h * 2048 + rg * 512;
  if constexpr (SSM) opaque(tabh);
  auto wfrag = [&](int n) INL {
    const int s = n / 12, ks = n % 12;
    if constexpr (SSM) {
      if (s < 18) return BF8(lds_rd(rb1[ks & 3] + (s & 1) * SLOT + (ks >> 2) * 8192));
      else return BF8(lds_rd(rb2[p2_k4(n)][(s >> 1) & 1] + (s & 1) * SLOT + p2_cb(n) * 8192));
    } else {
      if (s < 18) { unsigned a = rb1[(s >> 1) & 1] + (s & 1) * SLOT + ks * 2048; return tr_frag(a, a + 512); }
      else { unsigned a = rb2[0][(s >> 1) & 1] + (s & 1) * SLOT + p2_cb(n) * 8192 + p2_k4(n) * 2048; return tr_frag(a, a + 1024); }
    }
  };
  auto afrag = [&](int kq) INL {
    if constexpr (SSM) {
      const int u = kq >> 2, k4 = kq & 3;
      const int v = (k4 < 2) ? u - 1 : u;
      const unsigned off = (v == 17 ? 3 : (v + 1) % 3) * 16384 + ((k4 < 2) ? 8192 : 0) + (k4 & 1) * 256;
      return tr_frag(ab[0] + off, ab[1] + off);
    } else {
      return BF8(lds_rd(ab[0] + ((kq >> 2) & 3) * 16384 + (kq & 3) * 4096));
    }
  };
  WAIT_L0();
  STAMP(3);
  WAIT_V(SSM ? 4 : 9);
  BAR();
  STAMP(4);

  f32x16 acc[4];
  constexpr int NA = KM + 1;
  f32x16 oacc[3];
  u32x4 actp[2], gr[2];
  float tn, tr, tprev, tprev2;
  f32x2 Lam, Lx, ee, cc, cl, cq, g0, g1, g2;
  const unsigned long long gm0 = (c - 1 - 4 * rg >= 0 ? 0xffffffffull : 0ull) | (c - 3 - 4 * rg >= 0 ? 0xffffffff00000000ull : 0ull);
  const unsigned long long gm1 = (c - 2 - 4 * rg >= 0 ? 0xffffffffull : 0ull) | (c - 4 - 4 * rg >= 0 ? 0xffffffff00000000ull : 0ull);
  bf16x8 fr[4], af[4];
  u32x2 fx[3], fdk[3], gc[2];
  u32x4 fst, corrA, corrB;
  float cs[8], rsl;
  const int w67 = (w + 2) >> 3, cu0 = (w << w67) * 512, cu1 = cu0 + (6 - 5 * w67) * 512;
  float sk[12], xf[12];
  unsigned sto;
  const f32x16 zero = {};
  const char* amine = wc + C_ACT + (j * 6) * 16384 + set * 8192 + rg * (SSM ? 2048 : 1024);
  const char* flagb = wc + C_FLAG + (3 * gq) * 128;
  fr[0] = wfrag(0); fr[1] = wfrag(1);
  if constexpr (!SSM) fr[2] = wfrag(2);

  const char* pbase = wc + C_PART + (rg * 9 + 3 * mi) * 20480 + gq * 4096 + set * 2048;
  auto pstore = [&](int cb, int hh) INL {
    u32x4 v;
#pragma unroll
    for (int e = 0; e < 4; ++e) v[e] = pk2(oacc[cb][8 * hh + 2 * e], oacc[cb][8 * hh + 2 * e + 1]);
    const unsigned o = pvo ^ (set * 128 + ((pvo >> 3) & 64) + hh * 32);
    if (!SSM && rg == gq) lds_wr(lds0 + ARING + 32768 + cb * 4096 + set * 2048 + hh * 1024 + o, v);
    else gstore_sc1(pbase + cb * 20480 + hh * 1024, o, v);
  };
  const int fpk = (w >> 1) < 3 ? (gq + 1 + (w >> 1)) & 3 : 4;
  const char* ffl = wc + C_FLAG + (3 * fpk + mi) * 128;
  bool got = false;
  auto fin_ready = [&]() INL { return __builtin_amdgcn_readfirstlane(flg[1]) == tag && __builtin_amdgcn_readfirstlane(flg[0]) >= F_FIN; };
  auto fin_fetch = [&]() INL {
    const char* src = wc + C_PART + (gq * 9 + 3 * mi) * 20480 + fpk * 4096 + (w & 1) * 2048;
#pragma unroll
    for (int i = 0; i < 6; ++i) gldsA(src + (i >> 1) * 20480 + (i & 1) * 1024, pvo, lds0 + (w >> 1) * 12288 + (i >> 1) * 4096 + (w & 1) * 2048 + (i & 1) * 1024);
  };
  auto step = [&](auto NI) INL {
    constexpr int n = NI.value;
    constexpr int s = n / 12, ks = n % 12;
    if constexpr (R != 2 && n == 216 - 4) {
      const int lane = lane_now(), r = lane & 31, h = lane >> 5, G1 = (lane >> 4) & 1, q = (lane >> 2) & 3, p4 = lane & 3;
      if constexpr (SSM) {
        const int rperm = 16 * ((r >> 2) & 1) + (r & 3) + 4 * (r >> 3);
        const int row = 32 * set + rperm;
        rb2[0][0] = lds0 + row * 128 + ((h ^ ((row >> 1) & 7)) * 16);
        rb2[1][0] = rb2[0][0] ^ 32; rb2[2][0] = rb2[0][0] ^ 64; rb2[3][0] = rb2[0][0] ^ 96;
#pragma unroll
        for (int k = 0; k < 4; ++k) { rb2[k][1] = rb2[k][0] + 2 * SLOT; opaque(rb2[k][0]); opaque(rb2[k][1]); }
        const int o = p4 >> 1;
        ab[0] = lds0 + ARING + rg * 2048 + o * 1024 + G1 * 512 + (((2 * h) ^ (2 * G1 + o)) * 64) + q * 16 + (p4 & 1) * 8;
        ab[1] = ab[0] ^ 64;
        opaque(ab[0]); opaque(ab[1]);
      } else {
        const int dch = 4 * (p4 & 1) + 2 * G1 + (p4 >> 1);
        rb2[0][0] = lds0 + (4 * h + q) * 128 + (((set * 4 + (dch >> 1)) ^ (((q >> 1) & 1) << 2)) * 16) + (dch & 1) * 8;
        rb2[0][1] = rb2[0][0] + 2 * SLOT;
        ab[0] = lds0 + ARING + rg * 1024 + lane * 16;
        opaque(rb2[0][0]); opaque(rb2[0][1]); opaque(ab[0]);
      }
    }
    if constexpr (SSM && n == 15 * 12) pvo = lane_now() * 16;
    if constexpr (SSM && ks == 10 && (s & 1) == 1 && s < (R == 1 ? 11 : 17)) {
#pragma unroll
      for (int k = 0; k < 4; ++k) { rb1[k] += (s & 2) ? -2 * SLOT : 2 * SLOT; opaque(rb1[k]); }
    }
    constexpr int ss = (R == 1 && s >= 15) ? s - 3 : s;
    constexpr int pb = (ss >= 6) ? (ss - 6) / 6 : 0, ph = (ss >= 6 && ss < (R == 1 ? 18 : 24)) ? (ss - 6) % 6 : -1;
    constexpr bool mm_on = R == 0 || (R == 1 ? (n < 144 || n >= 216) : n < 216);
    if constexpr (ks == 0) {
      constexpr int pub = pub_at(R, s);
      constexpr bool poll = chk_need(R, SSM ? s : s + 1) != 0;
      if constexpr (poll) { if (w == 0) { const int l = lane_now(); flg = gload8_sc1(flagb, (l & 3) * 128); } }
      if constexpr (pub > 0) { if (w == 1) gstore8_sc1(flagb + mi * 128, 0u, u32x2{(unsigned)pub, tag}); }
      if constexpr (SSM && ph == 3) {
        const unsigned go = 3 * tabb - tabh;
        const char* gb = ws + WS_GRAN + ((R == 2 ? pb * 3 + 2 : mi * 3 + pb) * 16 + c - 1 - 4 * rg) * 1024 + rg * 512 - 2 * (lds0 + TAB_OFF);
        gr[0] = gload16_sc1(gb, go);
        gr[1] = gload16_sc1(gb - 1024, go);
      }
    }
    if constexpr (s < 3 * km(R)) {
      constexpr int kt = s / km(R), v = n - 12 * km(R) * kt;
      if constexpr (v == 0) { xn_read(kt, 0); xn_read(kt, 1); xn_read(kt, 2); xn_read(kt, 3); }
      if constexpr (SSM) {
        if constexpr (v >= 1 && v + 3 < 12) xn_read(kt, v + 3);
        constexpr int ch = v == 21 ? 5 : (v >= 12 && v <= 20 && v % 2 == 0) ? (v - 12) / 2 : -1;
        if constexpr (kt < 2 && ch >= 0) { xwrite1(ch / 3, 3 * (kt + 1) + ch % 3); SCHED(); }
      } else if constexpr (v >= 1 && v <= 4) { xn_read(kt, 2 * v + 2); xn_read(kt, 2 * v + 3); }
    }
    if constexpr (!SSM && n == 72) {
      fst = gload16_sc1(wc + C_STAT, (32 * rg) * 16 + (pvo & 496));
      const char* gb = ws + WS_CSUM + j * 8192;
      gc[0] = gload8_sc1(gb + cu0, pvo >> 1);
      gc[1] = gload8_sc1(gb + cu1, pvo >> 1);
    }
    if constexpr (SSM && ks == 4 && (s == 8 || s == 11 || (R == 2 && (s == 14 || s == 17)))) {
      const int l = lane_now(), r = l & 31;
      const int row = 32 * rg + 16 * ((r >> 2) & 1) + (r & 3) + 4 * (r >> 3);
      const unsigned a = lds0 + X_OFF + 32768 + row * 128 + (((l >> 5) ^ ((row >> 1) & 7)) * 16);
      xn[32] = BF8(lds_rd(a)); xn[33] = BF8(lds_rd(a ^ 32)); xn[34] = BF8(lds_rd(a ^ 64)); xn[35] = BF8(lds_rd(a ^ 96));
    }
    if constexpr (op_w(R, n)) { if constexpr (SSM && s == 0) dmaW(2 + ks / 3, ks % 3); else dmaW(s + 3, ks / 3); }
    if constexpr (op_x(R, n)) dmaX(n / 36 + 1, x_piece(n));
    if constexpr (op_a(R, n)) {
      if constexpr (SSM) {
        constexpr int v = s - 16, u = (ks == 2) ? v : v + 1;
        gldsA((u_hlp(u) < 0 ? acm[u_hlp(u) < 0 ? u_mem(u) : 0] + (2 * u_blk(u) + (u & 1)) * 16384 : wc + C_ACT + (15 * 6 + 2 * u_hlp(u) + (u & 1)) * 16384 + w * 1024) + (ks == 5 ? 0 : 8192), pvo, lds0 + ARING + (ks == 4 ? 3 : (v + 1) % 3) * 16384 + (ks == 5 ? 8192 : 0) + w * 1024);
      } else {
        constexpr int u = s + 3 - 18;
        gldsA(acm[u % 3] + (u / 3) * 16384 + (ks / 3) * 8192, pvo, lds0 + ARING + (u & 3) * 16384 + w * 1024 + (ks / 3) * 8192);
      }
    }
    if constexpr (op_cs(R, n)) {
      constexpr int i = cs_piece(n);
      const char* cb_ = (const char*)P.w1 + (size_t)(288 * ((c / 6) & 1) + 36 * w + 8 * i) * (F * 2) + 768 * j + (c % 6) * 128;
      xr[0][i] = gload16<0>(cb_, ((pvo >> 7) & (i < 4 ? 7 : 3)) * (F * 2) + (pvo & 112));
    }
    if constexpr (op_cp(R, n)) code_touch(3, 3072, lds0 + ((s + 3) & 3) * SLOT + w * 1024 + 2 * 8192);
    if constexpr (op_pf(R, n)) {
      const unsigned l = lane_now();
      constexpr int sl = s < 3 ? s : s - 3;
      if constexpr (SSM) {
        constexpr unsigned lim = s < 3 ? 72 : 108;
        unsigned id = sl * 32 + rg * 8 + (l >> 3); id = id < lim ? id : lim - 1;
        const u16* mat = s < 3 ? (set ? P.bim : P.bre) : (set ? P.cim : P.cre);
        glds4((const char*)mat + (size_t)(192 * mi + (s < 3 ? 64 : 0)) * (D * 2) + c * lim * 128, id * 128 + (l & 7) * 4, lds0 + JUNK_OFF);
      } else if constexpr (s < 3) {
        unsigned id = sl * 64 + w * 8 + (l >> 3); id = id < 144 ? id : 143;
        glds4((const char*)P.w1 + (size_t)(36 * c) * (F * 2) + 768 * j + 256, (id >> 2) * (F * 2) + (id & 3) * 128 + (l & 7) * 4, lds0 + JUNK_OFF);
      } else {
        unsigned id = sl * 64 + w * 8 + (l >> 3); id = id < 216 ? id : 215;
        glds4((const char*)P.w2 + (size_t)(384 * j) * (D * 2) + c * 216 * 128, id * 128 + (l & 7) * 4, lds0 + JUNK_OFF);
      }
    }
    if constexpr (SSM && n + 2 < (R == 1 ? 144 : 216)) fr[(n + 2) & 3] = wfrag(n + 2);
    if constexpr ((R == 0 || (R == 1 && n >= 213)) && n + 3 < 432) fr[(n + 3) & 3] = wfrag(n + 3);
    if constexpr (R == 2) {
    } else if constexpr (SSM) {
      constexpr int u = s - 18;
      if constexpr (s >= 18 && s < 35 && (ks == 3 || ks == 6)) af[ks / 3 + 1] = afrag(4 * u + ks / 3 + 1);
      if constexpr (s >= 17 && s < 35 && (ks == 9 || ks == 10)) af[ks - 9] = afrag(4 * (u + 1) + ks - 9);
      if constexpr (n == 419 || n == 420) af[n - 417] = afrag(68 + n - 417);
    } else {
      if constexpr (n + 3 >= 216 && n + 3 <= 420 && (n + 3) % 3 == 0) af[((n + 3 - 216) / 3) & 3] = afrag((n + 3 - 216) / 3);
      if constexpr (n >= 418 && n <= 420) af[n - 417] = afrag(68 + n - 417);
      if constexpr (op_fo(R, n)) {
        constexpr int i = s - 25;
        const unsigned rw = w * 4 + (pvo >> 8), co = (pvo >> 1) & 120;
        if constexpr (i < 3) fx[i] = gload8((const char*)P.x + (size_t)(128 * c + 32 * gq) * (D * 2) + 384 * mi + i * 128, rw * (D * 2) + co);
        else if constexpr (i < 6) fdk[i - 3] = gload8((const char*)P.dsk + 384 * mi + (i - 3) * 128, co);
        else fst = gload16_sc1(wc + C_STAT, (32 * gq + rw) * 16);
      }
      if constexpr (n == 408) { pin2(fx[0]); pin2(fx[1]); pin2(fx[2]); pin2(fdk[0]); pin2(fdk[1]); pin2(fdk[2]); wait1<63>(fst); flg = gload8_sc1(ffl, 0u); rsl = __uint_as_float(fst[2]) * LOG2E; tn = __uint_as_float(fst[2]) * (1.f / LOG2E); opaquef(rsl); opaquef(tn); }
      if constexpr (n >= 408 && n < 420) {
        constexpr int e = n - 408;
        const unsigned xv = fx[e >> 2][(e >> 1) & 1], dv = fdk[e >> 2][(e >> 1) & 1];
        xf[e] = (e & 1) ? bfhi(xv) : bflo(xv);
        sk[e] = (xf[e] - __uint_as_float(fst[0])) * tn * ((e & 1) ? bfhi(dv) : bflo(dv));
        opaquef(xf[e]); opaquef(sk[e]);
      }
      if constexpr (n == 422) {
        SCHED();
        wait0_2(flg);
        got = fin_ready();
        if (got) fin_fetch();
      }
    }
    if constexpr (!mm_on) {
    } else if constexpr (s < 18) {
      constexpr int T = p1_tile(R, s), kt = p1_kt(R, s);
      if constexpr (SSM) { acc[T & 3] = MFMA32(xn[12 * kt + ks], fr[n & 3], (kt == 0 && ks == 0) ? zero : acc[T & 3]); opaque16(acc[T & 3]); }
      else { acc[T % NA] = MFMA32(fr[n & 3], xn[12 * kt + ks], (kt == 0 && ks == 0) ? zero : acc[T % NA]); opaque16(acc[T % NA]); }
      if constexpr (SSM && s < 2 * km(R)) {
        constexpr int v = n - 12 * km(R) * kt, q = km(R) == 3 ? 4 : 6;
        constexpr int d0 = (q * v + 4) / 5, d1 = (q * (v + 1) + 4) / 5;
        if constexpr (d0 < d1 && d0 < 24) { constexpr int ch = d0 >> 2; xr[ch / 3][3 * (kt + 1) + ch % 3][d0 & 3] = norm4(xr[ch / 3][3 * (kt + 1) + ch % 3][d0 & 3], rs[ch / 3], nb[ch / 3]); }
        if constexpr (d0 + 1 < d1 && d0 + 1 < 24) { constexpr int d = d0 + 1, ch = d >> 2; xr[ch / 3][3 * (kt + 1) + ch % 3][d & 3] = norm4(xr[ch / 3][3 * (kt + 1) + ch % 3][d & 3], rs[ch / 3], nb[ch / 3]); }
      }
    } else {
      oacc[p2_cb(n)] = MFMA32(fr[n & 3], af[p2_k4(n)], n < 219 ? zero : oacc[p2_cb(n)]);
      opaque16(oacc[p2_cb(n)]);
      if constexpr (n == 425 || n == 426 || n == 429 || n == 430) pstore((n - 425) / 4, (n - 425) & 1);
    }
    if constexpr (!SSM) {
      if constexpr (n == 9) { opaque(epoch); opaque(aux[0]); tag = __builtin_amdgcn_readfirstlane(epoch) + 1u; }
      if constexpr (n >= 19 && n <= 32) {
        if constexpr (n == 19) {
          SCHED();
          wait3<ops_between(0, 8, 19)>(xr[0][0], xr[0][1], xr[0][2]); wait2<63>(xr[0][3], xr[0][4]);
#pragma unroll
          for (int d = 0; d < 4; ++d) { const unsigned v = msel_u(xr[0][4][d], 0xffffffffull); cs[2 * d] = bflo(v); cs[2 * d + 1] = bfhi(v); opaquef(cs[2 * d]); opaquef(cs[2 * d + 1]); }
        } else {
          constexpr int d0 = ((n - 20) * 16) / 13, d1 = ((n - 19) * 16) / 13;
#pragma unroll
          for (int d = d0; d < d1; ++d) { const unsigned v = xr[0][d >> 2][d & 3]; cs[2 * (d & 3)] += bflo(v); opaquef(cs[2 * (d & 3)]); cs[2 * (d & 3) + 1] += bfhi(v); opaquef(cs[2 * (d & 3) + 1]); }
        }
      }
      if constexpr (n == 33) {
#pragma unroll
        for (int e = 0; e < 4; ++e) { auto t = __builtin_amdgcn_permlane32_swap(__float_as_uint(cs[e]), __float_as_uint(cs[e + 4]), false, false); cs[e] = __uint_as_float(t[0]) + __uint_as_float(t[1]); opaquef(cs[e]); }
      }
      if constexpr (n == 34) {
#pragma unroll
        for (int e = 0; e < 2; ++e) { auto t = __builtin_amdgcn_permlane16_swap(__float_as_uint(cs[e]), __float_as_uint(cs[e + 2]), false, false); cs[e] = dpp_add<0x128>(__uint_as_float(t[0]) + __uint_as_float(t[1])); opaquef(cs[e]); }
        const unsigned l = pvo >> 4;
        *(LAS float*)(lds0 + TAB_OFF + 8192 + w * 256 + (8 * (l & 7) + (l >> 3)) * 4) = bsel(cs[0], cs[1], 0xff00ff00ff00ff00ull);
      }
      if constexpr (n == 36) {
        if (w == 2) {
          float t = 0.f;
#pragma unroll
          for (int k = 0; k < 8; ++k) t += *(LAS float*)(lds0 + TAB_OFF + 8192 + k * 256 + (pvo >> 2));
          gstore8_sc1(ws + WS_CSUM + j * 8192 + c * 512, pvo >> 1, u32x2{__float_as_uint(t), tag});
        }
      }
      if constexpr (n == 80) {
        SCHED();
        waitg<ops_between(0, 72, 80)>(fst, gc[0], gc[1]);
        auto stale = [&]() INL { return __builtin_amdgcn_ballot_w64(((fst[1] ^ tag) | (fst[3] ^ tag) | (gc[0][1] ^ tag) | (gc[1][1] ^ tag)) != 0u) != 0ull; };
        if (__builtin_expect(stale(), 0)) {
          const char* gb = ws + WS_CSUM + j * 8192;
          int spins = 200000;
          do { reloadg(fst, gc[0], gc[1], wc + C_STAT + (32 * rg) * 16, gb + cu0, gb + cu1, pvo & 496, pvo >> 1); } while (stale() && --spins > 0);
        }
        const float Sv = __uint_as_float(gc[0][0]) + __uint_as_float(gc[1][0]);
        const unsigned Sh = pk2(Sv, Sv), Sl = pk2(Sv - bfhi(Sh), 0.f);
        lds_wr(lds0 + TAB_OFF + w * 1024 + pvo, u32x4{Sh, (Sl & 0xffffu) | (aux[0] << 16), aux[0], 0u});
      }
      if constexpr (n == 81) {
        const float mu = -__uint_as_float(fst[0]), sg = __builtin_amdgcn_rcpf(__uint_as_float(fst[2]));
        const unsigned hi = pk2(mu, sg), lo = pk2(mu - bflo(hi), sg - bfhi(hi));
        corrB = u32x4{msel_u((hi & 0xffffu) | (lo << 16), 0xffffffffull), msel_u(hi, 0xffffffffull), msel_u(lo >> 16, 0xffffffffull), 0u};
        rsl = __uint_as_float(fst[2]) * LOG2E;
        wait1<63>(corrB); opaquef(rsl);
      }
      if constexpr (corr_tile(n + (n == 84 ? 3 : 6)) >= (n == 84 ? 0 : 1)) corrA = lds_rd(tabb + corr_tile(n + (n == 84 ? 3 : 6)) * 1024);
      if constexpr (corr_tile(n) >= 0) { acc[corr_tile(n) % NA] = MFMA32(BF8(corrA), BF8(corrB), acc[corr_tile(n) % NA]); opaque16(acc[corr_tile(n) % NA]); }
      if constexpr (n >= EPI0 && n < EPI0 + 198) {
        constexpr int T = (n - EPI0) / 33, u = (n - EPI0) % 33, e = u >> 1;
        const f32x16& pa = acc[T % NA];
        if constexpr ((u & 1) == 0) {
          if constexpr (e < 16) { tn = __builtin_amdgcn_exp2f(pa[e] * rsl); opaquef(tn); }
          if constexpr (e >= 1) {
            const float a = __builtin_fmaf(-pa[e - 1], tr, pa[e - 1]);
            if constexpr (((e - 1) & 1) == 0) { tprev = a; opaquef(tprev); }
            else { unsigned pk = pk2(tprev, a); opaque(pk); actp[(e - 1) >> 3][((e - 1) >> 1) & 3] = pk; }
          }
        } else { tr = __builtin_amdgcn_rcpf(__builtin_fmaf(tn, __builtin_fmaf(tn, 0.5f, 1.f), 1.f)); opaquef(tr); }
      }
      if constexpr (op_st(0, n)) { constexpr int id = st_id(s); gstore_sc1(amine + (id >> 1) * 16384 + (id & 1) * 4096, pvo, actp[id & 1]); }
    } else if constexpr (ph >= 0) {
      f32x16& ur = acc[(2 * pb) & 3];
      f32x16& ui = acc[(2 * pb + 1) & 3];
      const unsigned lt = tabb + pb * 512;
      const unsigned wt = tabb + (WT_OFF - TAB_OFF) + 2048, gt = tabb + (GT_OFF - TAB_OFF);
      if constexpr (ph == 0) {
        if constexpr (ks == 0) { ee = cfma(Lam, f32x2{ur[0], ui[0]}, f32x2{ur[1], ui[1]}); opaquec(ee); }
        if constexpr (ks >= 1 && ks <= 9) {
          constexpr int t0 = 2 + ((ks - 1) * 14) / 9, t1 = 2 + (ks * 14) / 9;
          ee = cfma(Lam, ee, f32x2{ur[t0], ui[t0]});
          if constexpr (t0 + 1 < t1) ee = cfma(Lam, ee, f32x2{ur[t0 + 1], ui[t0 + 1]});
          opaquec(ee);
        }
        if constexpr (ks == 7) Lx = lds_rd8(lt + 2 * 1536);
        if constexpr (ks == 10) {
          const f32x2 elo = {up32(ee[0]), up32(ee[1])};
          lds_wr8(tabh + (WT_OFF - TAB_OFF), cfma_s(Lx, elo, ee));
        }
        if constexpr (ks == 11) { Lam = csq(Lx); opaquec(Lam); }
      }
      if constexpr (ph == 1) {
        if constexpr (ks == 0) { g0 = lds_rd8(wt); g1 = lds_rd8(wt + 512); g2 = lds_rd8(wt + 1024); ee = lds_rd8(wt + 1536); }
        if constexpr (ks == 2) {
          cl = f32x2{0.f, 0.f}; cq = f32x2{1.f, 0.f};
          if (rg > 0) { cl = g0; cq = Lam; }
          if (rg > 1) { cl = cfma_s(Lam, cl, g1); cq = cmul_s(Lam, Lam); }
          if (rg > 2) {
            cl = cfma_s(Lam, cl, g2); cq = cmul_s(Lam, cq);
            const f32x2 E = cfma_s(Lam, cl, ee);
            gstore_sc1(ws + WS_GRAN + ((R == 2 ? pb * 3 + 2 : mi * 3 + pb) * 16 + c) * 1024 - 2 * (lds0 + TAB_OFF), 2 * tabb,
                       u32x4{__float_as_uint(E[0]), tag, __float_as_uint(E[1]), tag});
          }
          opaquec(cl); opaquec(cq);
          g0 = lds_rd8(wt - 2048 + rg * 512); cc = lds_rd8(lt + 1536);
        }
        if constexpr (ks == 4) {
          const f32x2 a = cfma_s(Lx, cl, g0);
          cl = f32x2{usel(cl[0], a[0]), usel(cl[1], a[1])};
          opaquec(cl);
        }
        if constexpr (ks == 5) { const f32x2 a = cmul_s(Lx, cq); cq = f32x2{usel(cq[0], a[0]), usel(cq[1], a[1])}; opaquec(cq); }
        if constexpr (ks == 6) { cl = cmul_s(cc, cl); opaquec(cl); }
        if constexpr (ks == 7) { cq = cmul_s(cc, cq); opaquec(cq); }
      }
      if constexpr ((ph == 1 && ks >= 8) || ph == 2) {
        constexpr int t = (ph == 1) ? ks - 8 : ks + 4;
        const float t1 = cc[1] * ui[t], t2 = cc[1] * ur[t];
        float a = __builtin_fmaf(cc[0], ur[t], -t1), b = __builtin_fmaf(-cc[0], ui[t], -t2);
        opaquef(a); opaquef(b);
        ur[t] = a; ui[t] = b;
      }
      if constexpr (ph == 3) {
        if constexpr (ks == 4) cc = lds_rd8(lt + 3 * 1536);
        if constexpr (ks == 7) { Lx = csq(cc); opaquec(Lx); }
        if constexpr (ks == 9) {
          SCHED();
          wait2<ops_between(R, 12 * s, n)>(gr[0], gr[1]);
          auto stale = [&]() INL {
            const unsigned b0 = (gr[0][1] ^ tag) | (gr[0][3] ^ tag), b1 = (gr[1][1] ^ tag) | (gr[1][3] ^ tag);
            return ((__builtin_amdgcn_ballot_w64(b0 != 0u) & gm0) | (__builtin_amdgcn_ballot_w64(b1 != 0u) & gm1)) != 0ull;
          };
          if (__builtin_expect(stale(), 0)) {
            const unsigned go = 3 * tabb - tabh;
            const char* gb = ws + WS_GRAN + ((R == 2 ? pb * 3 + 2 : mi * 3 + pb) * 16 + c - 1 - 4 * rg) * 1024 + rg * 512 - 2 * (lds0 + TAB_OFF);
            int spins = 200000;
            do { reload2(gr[0], gr[1], gb, gb - 1024, go); } while (stale() && --spins > 0);
          }
          const f32x2 a0 = {msel(gr[0][0], gm0), msel(gr[0][2], gm0)}, a1 = {msel(gr[1][0], gm1), msel(gr[1][2], gm1)};
          ee = cfma_s(cc, a1, a0); opaquec(ee);
        }
        if constexpr (ks == 10) {
          const f32x2 lo = {up32(ee[0]), up32(ee[1])};
          lds_wr8(tabh + (GT_OFF - TAB_OFF), cfma_s(Lx, ee, lo));
        }
        if constexpr (ks == 11) { cc = csq(Lx); opaquec(cc); }
      }
      if constexpr (ph == 4 && ks <= 3) {
        if constexpr (ks == 0) { ee = lds_rd8(gt + 2048 + 1536); g0 = lds_rd8(gt + 2048 + 1024); g1 = lds_rd8(gt + 2048 + 512); g2 = lds_rd8(gt + 2048); Lam = lds_rd8(lt); }
        if constexpr (ks == 2) { ee = cfma(cc, cfma(cc, ee, g0), g1); opaquec(ee); }
        if constexpr (ks == 3) { ee = cfma(cq, cfma(cc, ee, g2), cl); ee[1] = -ee[1]; opaquec(ee); }
      }
      if constexpr ((ph == 4 && ks >= 4) || (ph == 5 && ks <= 7)) {
        constexpr int tt = (ph == 4) ? ks - 4 : ks + 8;
        ee = cfma(f32x2{Lam[0], -Lam[1]}, ee, f32x2{ur[tt], ui[tt]});
        opaquec(ee);
        if constexpr ((tt & 1) == 0) { tprev = ee[0]; tprev2 = ee[1]; }
        else {
          unsigned p0 = pk2(tprev, ee[0]), p1 = pk2(tprev2, ee[1]); opaque(p0); opaque(p1);
          if constexpr (tt < 8) { gr[0][tt >> 1] = p0; gr[1][tt >> 1] = p1; }
          else { actp[0][(tt >> 1) & 3] = p0; actp[1][(tt >> 1) & 3] = p1; }
        }
      }
      if constexpr (op_st(R, n)) {
        constexpr int o = (ks - 7) >> 1, part = (ks - 7) & 1;
        if constexpr (ks == 7) { const int l = lane_now(); sto = (l ^ (8 * (l >> 5))) * 16; }
        if constexpr (ks == 9) sto ^= 64;
        gstore_sc1(amine + (2 * pb + part) * 16384 + o * 1024, sto, o ? actp[part] : gr[part]);
      }
    }
    if constexpr (SSM && ks == 9 && (s == 5 || s == 11 || (R == 2 && s == 17))) Lam = lds_rd8(tabb + ((s - 5) / 6) * 512);
    if constexpr (n == 8) { SCHED(); WAIT_V(ops_between(R, 0, 8)); BAR(); }
    if constexpr (R == 1 && n == 184) {
      SCHED(); WAIT_V(ops_between(1, 180, 184)); BAR();
      if (w == 1) gstore8_sc1(flagb + mi * 128, 0u, u32x2{2u, tag});
    }
    if constexpr (!SSM && n == 44) { SCHED(); BAR(); }
    if constexpr (ks == 11 && s < 34) {
      SCHED();
      if constexpr (SSM ? (s == 1 || s == 3) : (s == 2 || s == 6)) WAIT_L0();
      if constexpr (SSM && (ph == 0 || ph == 3 || s >= 17)) WAIT_L0();
      WAIT_V(drain(R, s) ? 0 : vm_n(R, (SSM && s == 0) ? 2 : 12 * s - 1, n));
      if constexpr (chk_need(R, s) != 0) {
        if (w == 0) {
          constexpr unsigned need = chk_need(R, s);
          constexpr unsigned long long lanes = (R == 1 && s != 16 && s != 24) ? 8ull : 7ull;
          if constexpr (SSM) WAIT_V(ops_between(R, 12 * s, n));
          pin2(flg);
          auto late = [&]() INL { return (__builtin_amdgcn_ballot_w64(flg[1] != tag || flg[0] < need) & lanes) != 0ull; };
          if (__builtin_expect(late(), 0)) {
            const int l = lane_now();
            int spins = 200000;
            do { flg = gload8_sc1(flagb, (l & 3) * 128); wait0_2(flg); } while (late() && --spins > 0);
          }
        }
      }
      BAR();
      STAMP(5 + s);
    }
    SCHED();
  };
  run_seq<0>(step, __make_integer_seq<ISeq, int, 144>{});
  if constexpr (R != 1) run_seq<144>(step, __make_integer_seq<ISeq, int, 36>{});
  else { STAMP(17); STAMP(18); STAMP(19); }
  run_seq<180>(step, __make_integer_seq<ISeq, int, 36>{});
  run_seq<216>(step, __make_integer_seq<ISeq, int, R == 2 ? 71 : 216>{});
  if constexpr (R != 2) { pstore(2, 0); pstore(2, 1); }
  STAMP(39);
  WAIT_L0();
  WAIT_V(0);
  BAR();
  STAMP(40);
  if (w == 1) gstore8_sc1(flagb + mi * 128, 0u, u32x2{F_FIN, tag});
  if constexpr (!SSM) {
    const int l = lane_now(), t = w * 64 + l;
    const int c4 = t & 15, rho = c4 >> 1;
    const unsigned ra = lds0 + (c4 >> 3) * 2048 + (rho & 1) * 1024 + ((c4 >> 2) & 1) * 512 + (((t >> 4) ^ (2 * rho)) * 16) + (c4 & 1) * 8;
    float* op = P.out + (size_t)(128 * c + 32 * gq + (t >> 4)) * D + 192 * mi + c4 * 4;
    if (!got) {
      int spins = 400000;
      do { flg = gload8_sc1(ffl, 0u); wait0_2(flg); } while (!fin_ready() && --spins > 0);
      fin_fetch();
    }
    STAMP(41);
    float zo[12];
#pragma unroll
    for (int cb = 0; cb < 3; ++cb) {
      const u32x2 ov = *(LAS u32x2*)(ra + ARING + 32768 + cb * 4096);
      zo[4 * cb] = bflo(ov[0]); zo[4 * cb + 1] = bfhi(ov[0]); zo[4 * cb + 2] = bflo(ov[1]); zo[4 * cb + 3] = bfhi(ov[1]);
    }
#pragma unroll
    for (int e = 0; e < 12; ++e) opaquef(zo[e]);
    STAMP(42);
    WAIT_V(0);
    STAMP(43);
    BAR();
    STAMP(44);
#pragma unroll
    for (int cb = 0; cb < 3; ++cb) {
      u32x2 pv[4];
#pragma unroll
      for (int k = 0; k < 4; ++k) pv[k] = *(LAS u32x2*)(ra + k * 12288 + cb * 4096);
      f32x4 o;
#pragma unroll
      for (int e = 0; e < 4; ++e) {
        auto up = [&](unsigned v) INL { return (e & 1) ? bfhi(v) : bflo(v); };
        const float z = ((zo[4 * cb + e] + up(pv[0][e >> 1])) + (up(pv[1][e >> 1]) + up(pv[2][e >> 1]))) * rsl;
        const float gt = z * __builtin_amdgcn_rcpf(1.f + __builtin_amdgcn_exp2f(-z));
        o[e] = __builtin_fmaf(__builtin_fmaf(up(pv[3][e >> 1]), 0.693147180559945f, sk[4 * cb + e]), gt, xf[4 * cb + e]);
      }
      asm volatile("global_store_dwordx4 %0, %1, off sc1\n\ts_nop 1" ::"v"(op + 64 * cb), "v"(o) : "memory");
    }
    if (c == 15 && j == 0 && t == 0) *(unsigned*)(ws + WS_EPOCH) = tag;
  }
  if constexpr (R == 2) asm volatile("fk_hlp_end:"); else if constexpr (R == 1) asm volatile("fk_ssm_end:"); else asm volatile("fk_mlp_end:");
  if constexpr (SSM) { STAMP(41); STAMP(42); STAMP(43); STAMP(44); }
  STAMP(45);
  STAMP(46);
  RSTAMP(61);
  STAMP_OUT();
}

__global__ __launch_bounds__(512, 2) void main_kernel(const Params P) {
  __shared__ __attribute__((aligned(16))) char smem[163840];
  const unsigned lds0 = (unsigned)(size_t)(LAS char*)smem;
  const int b = blockIdx.x, xcd = b & 7, idx = b >> 3;
#define MAP 2
  int j, c;
  if (MAP == 0) { j = idx >> 1; c = 2 * xcd + (idx & 1); }
  else if (idx >= 24) { j = 12 + ((idx - 24) >> 1); c = 2 * xcd + (idx & 1); }
  else if (MAP == 1) { j = 6 * (xcd & 1) + (idx >> 2); c = 4 * (xcd >> 1) + (idx & 3); }
  else { j = 3 * (xcd & 3) + (idx >> 3); c = 8 * (xcd >> 2) + (idx & 7); }
#if defined(__HIP_DEVICE_COMPILE__)
  if (__builtin_expect(j < 12, 1)) body<0>(P, lds0, c, j);
  else if (j < 15) body<1>(P, lds0, c, j);
  else body<2>(P, lds0, c, j);
#endif
}

extern "C" void kernel_launch(void* const* d_in, const int* in_sizes, int n_in, void* d_out, int out_size, void* d_ws, size_t ws_size,
                              hipStream_t stream) {
  Params P;
  P.x = (const u16*)d_in[0]; P.lre = (const u16*)d_in[1]; P.lim = (const u16*)d_in[2];
  P.bre = (const u16*)d_in[3]; P.bim = (const u16*)d_in[4]; P.cre = (const u16*)d_in[5]; P.cim = (const u16*)d_in[6];
  P.ldt = (const u16*)d_in[7]; P.w1 = (const u16*)d_in[10]; P.b1 = (const u16*)d_in[11]; P.w2 = (const u16*)d_in[12];
  P.dsk = (const u16*)d_in[8]; P.ws = (char*)d_ws; P.out = (float*)d_out;
  main_kernel<<<256, 512, 0, stream>>>(P);
}
```

```cpp
#include <hip/hip_runtime.h>
#include <hip/hip_bf16.h>
#pragma clang diagnostic ignored "-Wint-to-pointer-cast"

typedef unsigned short u16;
typedef __attribute__((ext_vector_type(2))) __bf16 bf16x2;
typedef __attribute__((ext_vector_type(4))) __bf16 bf16x4;
typedef __attribute__((ext_vector_type(8))) __bf16 bf16x8;
typedef __attribute__((ext_vector_type(4))) short s16x4;
typedef __attribute__((ext_vector_type(2))) float f32x2;
typedef __attribute__((ext_vector_type(4))) float f32x4;
typedef __attribute__((ext_vector_type(16))) float f32x16;
typedef __attribute__((ext_vector_type(2))) unsigned u32x2;
typedef __attribute__((ext_vector_type(4))) unsigned u32x4;
#define LAS __attribute__((address_space(3)))
#define GAS __attribute__((address_space(1)))
#define DI __device__ __forceinline__
#define INL __attribute__((always_inline))

constexpr int S = 2048, D = 576, F = 4608;
constexpr float LOG2E = 1.44269504088896f;
constexpr int SLOT = 24576;
constexpr int X_OFF = 4 * SLOT;
constexpr int ARING = X_OFF;
constexpr int TAB_OFF = ARING + 49152;
constexpr int LT_OFF = TAB_OFF;
constexpr int WT_OFF = TAB_OFF + 6144;
constexpr int JUNK_OFF = TAB_OFF + 14336;
constexpr int GT_OFF = TAB_OFF + 10240;
constexpr size_t WS_EPOCH = 0;
constexpr size_t WS_GRAN = 32768;
constexpr size_t WS_CSUM = 196608;
constexpr size_t WS_C0 = 327680, WS_CS = 2318336;
constexpr int C_FLAG = 0;
constexpr int C_STAT = 4096;
constexpr int C_ACT = 8192;
constexpr int C_PART = C_ACT + 1572864;

DI float bflo(unsigned v) { return __uint_as_float(v << 16); }
DI float bfhi(unsigned v) { return __uint_as_float(v & 0xffff0000u); }
DI unsigned pk2(float a, float b) { f32x2 v = {a, b}; return __builtin_bit_cast(unsigned, __builtin_convertvector(v, bf16x2)); }
#define WAIT_V(n) asm volatile("s_waitcnt vmcnt(%0)" ::"n"(n) : "memory")
#define WAIT_L0() asm volatile("s_waitcnt lgkmcnt(0)" ::: "memory")
#define BAR() asm volatile("s_barrier" ::: "memory")
#define SCHED() __builtin_amdgcn_sched_barrier(0)
#define BF8(x) __builtin_bit_cast(bf16x8, (x))
#define MFMA16(a, b, c) __builtin_amdgcn_mfma_f32_16x16x32_bf16((a), (b), (c), 0, 0, 0)
#define MFMA32(a, b, c) __builtin_amdgcn_mfma_f32_32x32x16_bf16((a), (b), (c), 0, 0, 0)
template <int I> struct IC { static constexpr int value = I; };
template <int B, int E, class Fn> DI void static_for(Fn&& f) { if constexpr (B < E) { f(IC<B>{}); static_for<B + 1, E>(f); } }

template <class T, T... I> struct ISeq {};
template <int B, class Fn, int... I> DI void run_seq(Fn&& f, ISeq<int, I...>) { (f(IC<B + I>{}), ...); }

DI void gldsW(const void* sbase, unsigned voff, unsigned lds_addr) {
  asm volatile("s_mov_b32 m0, %2\n\ts_nop 0\n\tglobal_load_lds_dwordx4 %0, %1" ::"v"(voff), "s"(sbase), "s"(lds_addr) : "memory");
}
DI void gldsA(const void* sbase, unsigned voff, unsigned lds_addr) {
  asm volatile("s_mov_b32 m0, %2\n\ts_nop 0\n\tglobal_load_lds_dwordx4 %0, %1 sc1" ::"v"(voff), "s"(sbase), "s"(lds_addr) : "memory");
}
DI void glds4(const void* sbase, unsigned voff, unsigned lds_addr) {
  asm volatile("s_mov_b32 m0, %2\n\ts_nop 0\n\tglobal_load_lds_dword %0, %1" ::"v"(voff), "s"(sbase), "s"(lds_addr) : "memory");
}
DI void gstore_sc1(const void* sbase, unsigned voff, u32x4 v) {
  asm volatile("global_store_dwordx4 %0, %1, %2 sc1\n\ts_nop 1" ::"v"(voff), "v"(v), "s"(sbase) : "memory");
}
DI void gstore8_sc1(const void* sbase, unsigned voff, u32x2 v) {
  asm volatile("global_store_dwordx2 %0, %1, %2 sc1\n\ts_nop 1" ::"v"(voff), "v"(v), "s"(sbase) : "memory");
}
template <int OFF> DI u32x4 gload16(const void* sbase, unsigned voff) {
  u32x4 v; asm volatile("global_load_dwordx4 %0, %1, %2 offset:%3" : "=v"(v) : "v"(voff), "s"(sbase), "n"(OFF) : "memory"); return v;
}
DI u32x4 gload16_sc1(const void* sbase, unsigned voff) {
  u32x4 v; asm volatile("global_load_dwordx4 %0, %1, %2 sc1" : "=v"(v) : "v"(voff), "s"(sbase) : "memory"); return v;
}
DI u32x2 gload8_sc1(const void* sbase, unsigned voff) {
  u32x2 v; asm volatile("global_load_dwordx2 %0, %1, %2 sc1" : "=v"(v) : "v"(voff), "s"(sbase) : "memory"); return v;
}
DI u32x2 gload8(const void* sbase, unsigned voff) {
  u32x2 v; asm volatile("global_load_dwordx2 %0, %1, %2" : "=v"(v) : "v"(voff), "s"(sbase) : "memory"); return v;
}
DI unsigned gload4_sc1(const void* sbase, unsigned voff) {
  unsigned v; asm volatile("global_load_dword %0, %1, %2 sc1" : "=v"(v) : "v"(voff), "s"(sbase) : "memory"); return v;
}
DI unsigned gload2(const void* sbase, unsigned voff) {
  unsigned v; asm volatile("global_load_ushort %0, %1, %2" : "=v"(v) : "v"(voff), "s"(sbase) : "memory"); return v;
}
template <int N> DI void wait1(u32x4& a) { asm volatile("s_waitcnt vmcnt(%1)" : "+v"(a) : "n"(N) : "memory"); }
template <int N, int X> DI void wait1x(u32x4& a, unsigned more) {
  asm volatile("s_waitcnt vmcnt(%2)\n\ts_cmp_eq_u32 %1, 0\n\ts_cbranch_scc1 1f\n\ts_waitcnt vmcnt(%3)\n1:" : "+v"(a) : "s"(more), "n"(N + X), "n"(N) : "memory", "scc");
}
template <int N> DI void wait3(u32x4& a, u32x4& b, u32x4& c) { asm volatile("s_waitcnt vmcnt(%3)" : "+v"(a), "+v"(b), "+v"(c) : "n"(N) : "memory"); }
template <int N> DI void wait2(u32x4& a, u32x4& b) { asm volatile("s_waitcnt vmcnt(%2)" : "+v"(a), "+v"(b) : "n"(N) : "memory"); }
template <int N> DI void wait4(u32x2& a, u32x2& b, u32x2& c, u32x2& d) { asm volatile("s_waitcnt vmcnt(%4)" : "+v"(a), "+v"(b), "+v"(c), "+v"(d) : "n"(N) : "memory"); }
template <int N> DI void waitn2(u32x2& a) { asm volatile("s_waitcnt vmcnt(%1)" : "+v"(a) : "n"(N) : "memory"); }
template <int N> DI void wait5(u32x2& a, u32x2& b, u32x2& c, u32x2& d, u32x2& e) { asm volatile("s_waitcnt vmcnt(%5)" : "+v"(a), "+v"(b), "+v"(c), "+v"(d), "+v"(e) : "n"(N) : "memory"); }
DI void reload2(u32x4& a, u32x4& b, const void* pa, const void* pb, unsigned voff) {
  asm volatile("global_load_dwordx4 %0, %2, %3 sc1\n\tglobal_load_dwordx4 %1, %2, %4 sc1\n\ts_waitcnt vmcnt(0)" : "+&v"(a), "+&v"(b) : "v"(voff), "s"(pa), "s"(pb) : "memory");
}
DI float msel(unsigned v, unsigned long long m) { float o; asm("v_cndmask_b32 %0, 0, %1, %2" : "=v"(o) : "v"(v), "s"(m)); return o; }
DI float bsel(float lo, float hi, unsigned long long m) { float o; asm("v_cndmask_b32 %0, %1, %2, %3" : "=v"(o) : "v"(lo), "v"(hi), "s"(m)); return o; }
DI unsigned msel_u(unsigned v, unsigned long long m) { unsigned o; asm("v_cndmask_b32 %0, 0, %1, %2" : "=v"(o) : "v"(v), "s"(m)); return o; }
template <int N> DI void waitg(u32x4& a, u32x2& b, u32x2& c) { asm volatile("s_waitcnt vmcnt(%3)" : "+v"(a), "+v"(b), "+v"(c) : "n"(N) : "memory"); }
DI void reloadg(u32x4& a, u32x2& b, u32x2& c, const void* pa, const void* pb, const void* pc, unsigned oa, unsigned ob) {
  asm volatile("global_load_dwordx4 %0, %3, %5 sc1\n\tglobal_load_dwordx2 %1, %4, %6 sc1\n\tglobal_load_dwordx2 %2, %4, %7 sc1\n\ts_waitcnt vmcnt(0)"
               : "+&v"(a), "+&v"(b), "+&v"(c) : "v"(oa), "v"(ob), "s"(pa), "s"(pb), "s"(pc) : "memory");
}
DI void pin2(u32x2& a) { asm volatile("" : "+v"(a)::"memory"); }
DI void wait0_2(u32x2& a) { asm volatile("s_waitcnt vmcnt(0)" : "+v"(a)::"memory"); }
DI void opaque(unsigned& a) { asm volatile("" : "+v"(a)); }
DI void opaquef(float& a) { asm volatile("" : "+v"(a)); }
DI int lane_now() { int l; asm volatile("v_mbcnt_lo_u32_b32 %0, -1, 0\n\tv_mbcnt_hi_u32_b32 %0, -1, %0" : "=v"(l)); return l; }
DI void opaque16(f32x16& a) { asm volatile("" : "+v"(a)); }
DI void opaquec(f32x2& a) { float p = a[0], q = a[1]; asm volatile("" : "+v"(p), "+v"(q)); a[0] = p; a[1] = q; }
DI bf16x8 tr_frag(unsigned a0, unsigned a1) {
  bf16x4 lo = __builtin_amdgcn_ds_read_tr16_b64_v4bf16((LAS bf16x4*)a0);
  bf16x4 hi = __builtin_amdgcn_ds_read_tr16_b64_v4bf16((LAS bf16x4*)a1);
  return __builtin_shufflevector(lo, hi, 0, 1, 2, 3, 4, 5, 6, 7);
}
DI u32x4 lds_rd(unsigned a) { return *(LAS u32x4*)a; }
DI void lds_wr(unsigned a, u32x4 v) { *(LAS u32x4*)a = v; }
DI f32x2 lds_rd8(unsigned a) { return *(LAS f32x2*)a; }
DI void lds_wr8(unsigned a, f32x2 v) { *(LAS f32x2*)a = v; }
DI u32x4 norm16(u32x4 v, float a, float b) {
  unsigned a0 = v[0], a1 = v[1], a2 = v[2], a3 = v[3], t0, t1, t2, t3;
  asm("v_lshlrev_b32 %4, 16, %0\n\tv_lshlrev_b32 %5, 16, %1\n\tv_lshlrev_b32 %6, 16, %2\n\tv_lshlrev_b32 %7, 16, %3\n\t"
      "v_and_b32 %0, 0xffff0000, %0\n\tv_and_b32 %1, 0xffff0000, %1\n\tv_and_b32 %2, 0xffff0000, %2\n\tv_and_b32 %3, 0xffff0000, %3\n\t"
      "v_fma_f32 %4, %4, %8, %9\n\tv_fma_f32 %5, %5, %8, %9\n\tv_fma_f32 %6, %6, %8, %9\n\tv_fma_f32 %7, %7, %8, %9\n\t"
      "v_fma_f32 %0, %0, %8, %9\n\tv_fma_f32 %1, %1, %8, %9\n\tv_fma_f32 %2, %2, %8, %9\n\tv_fma_f32 %3, %3, %8, %9\n\t"
      "v_cvt_pk_bf16_f32 %0, %4, %0\n\tv_cvt_pk_bf16_f32 %1, %5, %1\n\tv_cvt_pk_bf16_f32 %2, %6, %2\n\tv_cvt_pk_bf16_f32 %3, %7, %3"
      : "+v"(a0), "+v"(a1), "+v"(a2), "+v"(a3), "=&v"(t0), "=&v"(t1), "=&v"(t2), "=&v"(t3) : "v"(a), "v"(b));
  return u32x4{a0, a1, a2, a3};
}
DI unsigned norm4(unsigned a, float sc, float bi) {
  unsigned t;
  asm volatile("v_lshlrev_b32 %1, 16, %0\n\tv_and_b32 %0, 0xffff0000, %0\n\tv_fma_f32 %1, %1, %2, %3\n\tv_fma_f32 %0, %0, %2, %3\n\tv_cvt_pk_bf16_f32 %0, %1, %0"
               : "+v"(a), "=&v"(t) : "v"(sc), "v"(bi));
  return a;
}
template <int CTRL> DI float dpp_add(float a) {
  return a + __builtin_bit_cast(float, __builtin_amdgcn_update_dpp(0, __builtin_bit_cast(int, a), CTRL, 0xf, 0xf, true));
}
DI f32x2 cfma(f32x2 a, f32x2 b, f32x2 c) {
  f32x2 o;
  o[0] = __builtin_fmaf(a[0], b[0], __builtin_fmaf(-a[1], b[1], c[0]));
  o[1] = __builtin_fmaf(a[0], b[1], __builtin_fmaf(a[1], b[0], c[1]));
  return o;
}
DI f32x2 cfma_s(f32x2 a, f32x2 b, f32x2 c) {
  float o0, o1;
  asm("v_fma_f32 %0, -%3, %5, %6\n\tv_fma_f32 %1, %3, %4, %7\n\tv_fmac_f32 %0, %2, %4\n\tv_fmac_f32 %1, %2, %5"
      : "=&v"(o0), "=&v"(o1) : "v"(a[0]), "v"(a[1]), "v"(b[0]), "v"(b[1]), "v"(c[0]), "v"(c[1]));
  return f32x2{o0, o1};
}
DI f32x2 cmul_s(f32x2 a, f32x2 b) {
  float o0, o1;
  asm("v_mul_f32_e64 %0, -%3, %5\n\tv_mul_f32 %1, %3, %4\n\tv_fmac_f32 %0, %2, %4\n\tv_fmac_f32 %1, %2, %5"
      : "=&v"(o0), "=&v"(o1) : "v"(a[0]), "v"(a[1]), "v"(b[0]), "v"(b[1]));
  return f32x2{o0, o1};
}
DI float usel(float lo, float hi) { float o; asm("v_cndmask_b32 %0, %1, %2, %3" : "=v"(o) : "v"(lo), "v"(hi), "s"(0xffffffff00000000ull)); return o; }
DI f32x2 csq(f32x2 a) { f32x2 o; o[0] = __builtin_fmaf(a[0], a[0], -a[1] * a[1]); o[1] = 2.f * a[0] * a[1]; return o; }
DI float sum16(float v) {
  auto t = __builtin_amdgcn_permlane16_swap(__float_as_uint(v), __float_as_uint(v), false, false);
  return __uint_as_float(t[0]) + __uint_as_float(t[1]);
}
DI float up32(float v) {
  auto t = __builtin_amdgcn_permlane32_swap(0u, __float_as_uint(v), false, false);
  return __uint_as_float(t[0]);
}

#define STAMP(i)
#define RSTAMP(i)
#define STAMP_OUT()

constexpr int KM = 3;
constexpr int km(int R) { return R == 0 ? KM : 2; }
constexpr int p1_tile(int R, int s) { return s < 3 * km(R) ? s % km(R) : s / 3; }
constexpr int p1_kt(int R, int s) { return s < 3 * km(R) ? s / km(R) : s % 3; }
constexpr int p2_cb(int n) { return n >= 420 ? (n - 420) / 4 : (n % 12) % 3; }
constexpr int p2_k4(int n) { return n >= 420 ? (n - 420) % 4 : (n % 12) / 3; }
constexpr int EPI0 = 101;
constexpr int corr_tile(int n) { return n == 99 ? 0 : n == 106 ? 1 : n == 113 ? 2 : n == 145 ? 3 : n == 181 ? 4 : n == 217 ? 5 : -1; }
constexpr int st_slot(int T, int o) { int r = EPI0 + 33 * T + (o ? 33 : 17); return (r % 12 <= 7) ? r / 12 : r / 12 + 1; }
constexpr int st_id(int s) { for (int i = 0; i < 12; ++i) if (st_slot(i >> 1, i & 1) == s) return i; return -1; }
constexpr bool op_w(int R, int n) {
  const int s = n / 12, k = n % 12;
  if ((R == 1 && s >= 12 && s <= 18 && s != 16) || (R == 2 && s >= 15)) return false;
  return s == 0 ? k < 6 : (s <= 32 && k % 3 == 0 && k <= 6);
}
constexpr int w_slot(int R, int s) { return R == 1 && s >= 9 && s <= 11 ? (s == 11 ? 18 : s + 11) : s + 3; }
constexpr int x_piece(int n) { const int t = n % 36; return n >= 72 ? -1 : (t >= 9 && t <= 11) ? t - 9 : t == 13 ? 3 : t == 14 ? 4 : t == 16 ? 5 : -1; }
constexpr bool op_x(int R, int n) { return R == 0 && x_piece(n) >= 0; }
constexpr int a_code(int n) {
  const int s = n / 12, k = n % 12;
  if (s == 17) return k < 4 ? k - 1 : -9;
  if (s == 18) return (k >= 6 && k <= 8) ? k - 3 : -9;
  if (n == 12 * 32 + 4) return 34;
  return (s >= 19 && s <= 32 && (k == 2 || k == 5)) ? 2 * (s - 16) + (k == 5) : -9;
}
constexpr bool op_a(int R, int n) {
  const int s = n / 12, k = n % 12;
  if (R == 2) return false;
  if (R == 1) return a_code(n) != -9;
  return (k == 2 || k == 5) && s >= 15 && s <= 32;
}
constexpr bool op_pf(int R, int n) { return n % 12 == 8 && (R == 1 ? n / 12 <= 6 : R == 0 && n / 12 >= 4 && n / 12 <= 8 && n / 12 != 6); }
constexpr bool op_cp(int R, int n) { const int s = n / 12; return n % 12 == 5 && s >= 7 && s <= 28 && s % 7 == 0 && !(R == 1 && s == 14); }
constexpr bool op_st(int R, int n) {
  const int s = n / 12, k = n % 12;
  if (R) return (s == 11 || (R == 1 ? s == 21 : s == 17 || s == 23)) && k >= 7 && k <= 10;
  return k == 7 && st_id(s) >= 0;
}
#define CSV 0
constexpr int cs_piece(int n) { return CSV == 0 ? (n == 19 ? 4 : n == 21 ? 0 : n == 23 ? 1 : n == 26 ? 2 : n == 29 ? 3 : n == 32 ? 5 : n == 34 ? 6 : -1)
                                                : (n == 7 ? 4 : n == 19 ? 0 : n == 21 ? 1 : n == 23 ? 2 : n == 26 ? 3 : n == 29 ? 5 : n == 32 ? 6 : -1); }
constexpr int CS0 = CSV == 0 ? 43 : 37;
constexpr bool op_cs(int R, int n) { return R == 0 && cs_piece(n) >= 0; }
constexpr bool op_fo(int R, int n) { return R == 0 && n % 12 == 8 && n / 12 >= 25 && n / 12 <= 31; }
constexpr bool drain(int R, int s) { return R == 2 && s == 18; }
constexpr int n_fill(int R, int n) { return op_w(R, n) + op_x(R, n) + op_a(R, n); }
constexpr int n_lazy(int R, int n) { return op_pf(R, n) + op_cp(R, n) + op_st(R, n) + op_fo(R, n) + op_cs(R, n); }
constexpr int last_fill(int R, int L) { while (L >= 0 && n_fill(R, L) == 0) --L; return L; }
constexpr int ops_between(int R, int a, int e) { int n = 0; for (int i = a; i <= e; ++i) n += n_fill(R, i) + n_lazy(R, i); return n; }
constexpr int vm_n(int R, int L, int e) { const int l = last_fill(R, L); return (l >= 0 ? n_lazy(R, l) : 0) + ops_between(R, l + 1, e); }
constexpr int done_slot(int R, int m) { int s = m / 12; while (!drain(R, s) && last_fill(R, 12 * s - 1) <= m && s < 40) ++s; return s + 1; }
constexpr int pub_at(int R, int s) {
  if (R == 0) { for (int T = 0; T < 6; ++T) if (done_slot(0, 12 * st_slot(T, 1) + 7) == s) return T + 2; }
  else if (R == 1) { if (s == 17) return 2; if (done_slot(1, 12 * 21 + 10) == s) return 3; }
  else { for (int b = 0; b < 2; ++b) if (done_slot(R, 12 * (11 + 6 * b) + 10) == s) return b + 2; }
  return 0;
}
static_assert(done_slot(0, 12 * 31 + 8) <= 34, "schedule");
static_assert(pub_at(1, 24) == 3 && pub_at(2, 14) == 2 && pub_at(2, 19) == 3, "schedule");
static_assert(done_slot(0, 12 * st_slot(0, 1) + 7) <= 14 && done_slot(0, 12 * st_slot(5, 1) + 7) <= 28, "schedule");
constexpr unsigned chk_need(int R, int s) {
  if (R == 0) return (s >= 15 && s <= 30 && (s - 15) % 3 == 0) ? (s - 15) / 3 + 2 : 0;
  if (R == 1) return s == 26 ? 3 : s == 20 ? 2 : s == 22 ? 3 : s == 30 ? 4 : 0;
  return 0;
}
constexpr int u_hlp(int u) { return (u >= 6 && u <= 9) ? (u - 6) >> 1 : u >= 16 ? 2 : -1; }
constexpr int u_mem(int u) { return (u < 6 ? u : u - 10) >> 1; }
constexpr int u_blk(int u) { return u < 6 ? 0 : 1; }
constexpr unsigned F_FIN = 8;

struct Params {
  const u16 *x, *lre, *lim, *bre, *bim, *cre, *cim, *ldt, *dsk, *w1, *b1, *w2;
  char* ws;
  float* out;
};

template <int R>
DI void body(const Params& P, const unsigned lds0, const int c, const int j) {
  constexpr bool SSM = R != 0;
  constexpr int PSTR = R == 2 ? 192 : 64;
  char* const ws = P.ws;
  const int tid = threadIdx.x, lane = tid & 63;
  const int w = __builtin_amdgcn_readfirstlane(tid >> 6);
  const int rg = w & 3, set = w >> 2;
  const int r = lane & 31, h = lane >> 5;
  unsigned stampv = 0; (void)stampv;
  RSTAMP(60); STAMP(0);
  const int gq = SSM ? 4 : j / 3, mi = j - 3 * gq;

  char* const wc = ws + WS_C0 + (size_t)c * WS_CS;
  u32x4 xr[2][9];
  unsigned aux[3], epoch, tag;
  const int ww = (w + (SSM ? 2 * c : c)) & 7;
  const int rb0 = 8 * ((w + (SSM ? 4 * mi : 4 * gq)) & 15), krot = mi;
  const int rb1_ = rb0 ^ 64;
  const char* xb1 = (const char*)P.x + (size_t)(128 * c + rb1_) * (D * 2);
  const unsigned xoff = (lane >> 3) * (D * 2) + (lane & 7) * 16;
  const char* xb0 = (const char*)P.x + (size_t)(128 * c + rb0) * (D * 2);
  unsigned voffx = (lane >> 3) * (D * 2) + (((lane & 7) ^ ((4 * (w & 1) + (lane >> 4)) & 7)) * 16);
  auto dmaX = [&](int third, int i) INL {
    int kb = (i >> 1) + krot; kb = kb >= 3 ? kb - 3 : kb;
    gldsW(((i & 1) ? xb1 : xb0) + (3 * third + kb) * 128, voffx, lds0 + X_OFF + kb * 16384 + ((i & 1) ? rb1_ : rb0) * 128);
  };
  constexpr int NAUX = 4;
  if constexpr (SSM) {
    __builtin_amdgcn_s_setprio(2);
    static_for<0, 9>([&](auto I) INL { xr[0][I.value] = gload16<I.value * 128>(xb0, xoff); });
    epoch = gload4_sc1(ws + WS_EPOCH, 0u);
    const unsigned o = (R == 2 ? 128 + 192 * (w < 3 ? w : 0) + lane : 192 * mi + (tid < 192 ? tid : 0)) * 2;
    aux[0] = gload2(P.lre, o); aux[1] = gload2(P.lim, o); aux[2] = gload2(P.ldt, o);
  } else {
    __builtin_amdgcn_s_setprio(2);
#pragma unroll
    for (int i = 0; i < 6; ++i) dmaX(0, i);
  }

  unsigned pvo = lane * 16;
  unsigned voff1, voff2;
  const char *wa[2], *wb[3][2], *acm[3];
  if constexpr (SSM) {
    voff1 = voff2 = voffx;
    wa[0] = (const char*)P.bre + (size_t)((R == 2 ? 128 : 192 * mi) + 8 * ww) * (D * 2);
    wa[1] = (const char*)P.bim + (size_t)((R == 2 ? 128 : 192 * mi) + 8 * ww) * (D * 2);
  } else {
    const unsigned swz = ((lane & 7) ^ (((lane >> 4) & 1) << 2)) * 16;
    voff1 = (lane >> 3) * (F * 2) + swz;
    voff2 = (lane >> 3) * (D * 2) + swz;
    wa[0] = wa[1] = (const char*)P.w1 + ((size_t)(8 * ww) * F + 384 * j) * 2;
  }
#pragma unroll
  for (int k = 0; k < 3; ++k) {
    const int mm = (mi + k >= 3) ? mi + k - 3 : mi + k;
    if constexpr (SSM) {
      wb[k][0] = (const char*)P.cre + (size_t)(192 * mi + 8 * ww) * (D * 2) + 384 * mm;
      wb[k][1] = (const char*)P.cim + (size_t)(192 * mi + 8 * ww) * (D * 2) + 384 * mm;
    } else {
      wb[k][0] = wb[k][1] = (const char*)P.w2 + ((size_t)(384 * (3 * gq + mm) + 8 * ww) * D + 192 * mi) * 2;
    }
    acm[k] = wc + C_ACT + ((3 * gq + mm) * 6) * 16384 + w * 1024;
  }
  auto dmaW = [&](int s, int i) INL {
    const unsigned dst = lds0 + (s & 3) * SLOT + ww * 1024 + i * 8192;
    const int u = s - 18;
    if constexpr (SSM) {
      if (s < 18) gldsW(wa[p1_tile(R, s) & 1] + (PSTR * (p1_tile(R, s) >> 1)) * (D * 2) + (192 * p1_kt(R, s) + 64 * i) * 2, voff1, dst);
      else if (u_hlp(u) < 0) gldsW(wb[u_mem(u)][u & 1] + (64 * i) * (D * 2) + 128 * u_blk(u), voff2, dst);
      else gldsW(wb[0][u & 1] - 384 * mi + (64 * i) * (D * 2) + 384 * u_hlp(u) + 256, voff2, dst);
    } else {
      if (s < 18) gldsW(wa[0] + ((size_t)(192 * p1_kt(R, s) + 64 * i) * F + 64 * p1_tile(R, s)) * 2, voff1, dst);
      else gldsW(wb[u % 3][0] + ((size_t)(64 * (u / 3)) * D + 64 * i) * 2, voff2, dst);
    }
  };
  auto code_touch = [&](int lines_per_wave_log2, unsigned skip, unsigned dst) INL {
    unsigned long long pc; unsigned len;
    if constexpr (R == 2) asm volatile("s_getpc_b64 %0\n\ts_mov_b32 %1, fk_hlp_end-." : "=s"(pc), "=s"(len));
    else if constexpr (R == 1) asm volatile("s_getpc_b64 %0\n\ts_mov_b32 %1, fk_ssm_end-." : "=s"(pc), "=s"(len));
    else asm volatile("s_getpc_b64 %0\n\ts_mov_b32 %1, fk_mlp_end-." : "=s"(pc), "=s"(len));
    const unsigned co = skip + (((unsigned)w << lines_per_wave_log2) + ((unsigned)lane_now() >> (6 - lines_per_wave_log2))) * 128;
    glds4((const void*)pc, co < len ? co : 0u, dst);
  };
  const unsigned ximg = (lane >> 3) * 128 + (((lane & 7) ^ ((4 * (w & 1) + (lane >> 4)) & 7)) * 16);
  float rs[2], nb[2], mn[2];
  unsigned xw = lds0 + X_OFF + rb0 * 128 + ximg;
  auto xwrite1 = [&](int ps, int i) INL { lds_wr((ps ? xw ^ 8192 : xw) + (i % 3) * 16384, xr[ps][i]); };
  auto stats = [&](auto PI) INL {
    constexpr int ps = PI.value;
    f32x4 s1 = {}, s2 = {}, s3 = {};
    const unsigned selv = (((lane >> 4) & 3) == (lane & 3)) ? 0x3f803f80u : 0u;
    const u32x4 sel = {selv, selv, selv, selv};
    static_for<0, 9>([&](auto GI) INL {
      constexpr int i = GI.value;
      if constexpr (ps == 0) { xr[1][i] = gload16<i * 128>(xb1, xoff); if constexpr (i == 8) __builtin_amdgcn_s_setprio(0); wait1<9 + NAUX>(xr[0][i]); }
      else wait1<8 - i + 5>(xr[1][i]);
      const u32x2 t0 = {xr[ps][i][0], xr[ps][i][1]}, t1 = {xr[ps][i][2], xr[ps][i][3]};
      s2 = __builtin_amdgcn_mfma_f32_4x4x4bf16_1k(__builtin_bit_cast(s16x4, t0), __builtin_bit_cast(s16x4, t0), s2, 0, 0, 0);
      s3 = __builtin_amdgcn_mfma_f32_4x4x4bf16_1k(__builtin_bit_cast(s16x4, t1), __builtin_bit_cast(s16x4, t1), s3, 0, 0, 0);
      s1 = MFMA16(BF8(sel), BF8(xr[ps][i]), s1);
      SCHED();
    });
    if constexpr (ps == 0) {
      dmaW(0, 0); dmaW(0, 1); dmaW(0, 2);
      unsigned id = rg * 16 + (lane >> 2); id = id < 36 ? id : 35;
      glds4(wa[0] + (set ? wa[1] - wa[0] : 0) - 8 * ww * (D * 2) + c * 36 * 128, id * 128 + (lane & 3) * 4, lds0 + JUNK_OFF);
      code_touch(5, 0, lds0 + JUNK_OFF);
    }
    s2 += s3;
    const float e0 = s2[0], e1 = s2[1], e2 = s2[2], e3 = s2[3];
    const float lo2 = (lane & 1) ? e1 : e0, hi2 = (lane & 1) ? e3 : e2;
    float a2 = (lane & 2) ? hi2 : lo2;
    const float f0 = s1[0], f1 = s1[1], f2 = s1[2], f3 = s1[3];
    const float lo1 = (lane & 16) ? f1 : f0, hi1 = (lane & 16) ? f3 : f2;
    float a1 = (lane & 32) ? hi1 : lo1;
    a1 = dpp_add<0xB1>(a1); a2 = dpp_add<0xB1>(a2);
    a1 = dpp_add<0x4E>(a1); a2 = dpp_add<0x4E>(a2);
    a1 = dpp_add<0x141>(a1); a2 = dpp_add<0x141>(a2);
    const float m = a1 * (1.f / D);
    const float var = fmaxf(a2 * (1.f / D) - m * m, 0.f);
    rs[ps] = __builtin_amdgcn_rsqf(var + 1e-5f);
    nb[ps] = -m * rs[ps];
    mn[ps] = m;
    if constexpr (ps == 1) {
      opaque(epoch);
      tag = __builtin_amdgcn_readfirstlane(epoch) + 1u;
      if (j == 12) {
        gstore_sc1(wc + C_STAT + rb0 * 16, (lane >> 3) * 16, u32x4{__float_as_uint(mn[0]), tag, __float_as_uint(rs[0]), tag});
        gstore_sc1(wc + C_STAT + rb1_ * 16, (lane >> 3) * 16, u32x4{__float_as_uint(mn[1]), tag, __float_as_uint(rs[1]), tag});
      }
    }
#pragma unroll
    for (int i = 0; i < 3; ++i) { xr[ps][i] = norm16(xr[ps][i], rs[ps], nb[ps]); xwrite1(ps, i); }
  };
  if constexpr (!SSM) {
    dmaW(0, 0); dmaW(0, 1); dmaW(0, 2);
    __builtin_amdgcn_s_setprio(0);
    dmaW(1, 0); dmaW(1, 1); dmaW(1, 2);
    {
      unsigned id = w * 16 + (lane >> 2); id = id < 108 ? id : 107;
      glds4((const char*)P.w1 + (size_t)(36 * c) * (F * 2) + 768 * j, (id / 3) * (F * 2) + (id % 3) * 128 + (lane & 3) * 4, lds0 + JUNK_OFF);
    }
    code_touch(5, 0, lds0 + JUNK_OFF);
    STAMP(1); STAMP(2);
  } else {
    stats(IC<0>{});
    STAMP(1);
    stats(IC<1>{});
    STAMP(2);
    dmaW(1, 0); dmaW(1, 1); dmaW(1, 2);
    opaque(aux[0]); opaque(aux[1]); opaque(aux[2]);
    if (w < 3) {
      const float lr = bflo(aux[0]), li = bflo(aux[1]);
      const float dt = __builtin_amdgcn_exp2f(bflo(aux[2]) * LOG2E);
      const float mag = __builtin_amdgcn_exp2f(dt * lr * LOG2E);
      const float rev = __builtin_amdgcn_fractf(dt * li * 0.15915494309189535f);
      f32x2 L = {mag * __builtin_amdgcn_cosf(rev), mag * __builtin_amdgcn_sinf(rev)};
      const float inv = 2.f / (lr * lr + li * li);
      const float nr = L[0] - 1.f, ni = L[1];
      const f32x2 c2 = {(nr * lr + ni * li) * inv, (ni * lr - nr * li) * inv};
      const f32x2 L16 = csq(csq(csq(csq(L))));
      const f32x2 L128 = csq(csq(csq(L16)));
      lds_wr8(lds0 + LT_OFF + tid * 8, L); lds_wr8(lds0 + LT_OFF + 1536 + tid * 8, c2);
      lds_wr8(lds0 + LT_OFF + 2 * 1536 + tid * 8, L16); lds_wr8(lds0 + LT_OFF + 3 * 1536 + tid * 8, L128);
    }
  }
  u32x2 flg;
  const int rperm = 16 * ((r >> 2) & 1) + (r & 3) + 4 * (r >> 3);
  bf16x8 xn[36];
  unsigned xn_ad[4];
  { const int row = 32 * rg + (SSM ? rperm : r); xn_ad[0] = lds0 + X_OFF + row * 128 + ((h ^ ((row >> 1) & 7)) * 16); }
  xn_ad[1] = xn_ad[0] ^ 32; xn_ad[2] = xn_ad[0] ^ 64; xn_ad[3] = xn_ad[0] ^ 96;
  auto xn_read = [&](int rd, int ks) INL { xn[12 * rd + ks] = BF8(lds_rd(xn_ad[ks & 3] + (ks >> 2) * 16384)); };

  const int G1 = (lane >> 4) & 1, q = (lane >> 2) & 3, p4 = lane & 3;
  unsigned rb1[4], rb2[4][2], ab[2];
  if constexpr (SSM) {
    const int row = 32 * set + r;
    rb1[0] = lds0 + row * 128 + ((h ^ ((row >> 1) & 7)) * 16);
    rb1[1] = rb1[0] ^ 32; rb1[2] = rb1[0] ^ 64; rb1[3] = rb1[0] ^ 96;
    opaque(rb1[0]); opaque(rb1[1]); opaque(rb1[2]); opaque(rb1[3]);
  } else {
    rb1[0] = lds0 + (8 * h + q) * 128 + (((set * 4 + G1 * 2 + (p4 >> 1)) ^ (((q >> 1) & 1) << 2)) * 16) + (p4 & 1) * 8;
    rb1[1] = rb1[0] + 2 * SLOT;
    opaque(rb1[0]); opaque(rb1[1]);
  }
  unsigned tabb = lds0 + TAB_OFF + (32 * set + r) * (SSM ? 8 : 16);
  opaque(tabb);
  unsigned tabh = tabb + h * 2048 + rg * 512;
  if constexpr (SSM) opaque(tabh);
  auto wfrag = [&](int n) INL {
    const int s = n / 12, ks = n % 12;
    if constexpr (SSM) {
      if (s < 18) return BF8(lds_rd(rb1[ks & 3] + (s & 1) * SLOT + (ks >> 2) * 8192));
      else return BF8(lds_rd(rb2[p2_k4(n)][(s >> 1) & 1] + (s & 1) * SLOT + p2_cb(n) * 8192));
    } else {
      if (s < 18) { unsigned a = rb1[(s >> 1) & 1] + (s & 1) * SLOT + ks * 2048; return tr_frag(a, a + 512); }
      else { unsigned a = rb2[0][(s >> 1) & 1] + (s & 1) * SLOT + p2_cb(n) * 8192 + p2_k4(n) * 2048; return tr_frag(a, a + 1024); }
    }
  };
  auto afrag = [&](int kq) INL {
    if constexpr (SSM) {
      const int u = kq >> 2, k4 = kq & 3;
      const int v = (k4 < 2) ? u - 1 : u;
      const unsigned off = (v == 17 ? 3 : (v + 1) % 3) * 16384 + ((k4 < 2) ? 8192 : 0) + (k4 & 1) * 256;
      return tr_frag(ab[0] + off, ab[1] + off);
    } else {
      return BF8(lds_rd(ab[0] + ((kq >> 2) & 3) * 16384 + (kq & 3) * 4096));
    }
  };
  WAIT_L0();
  STAMP(3);
  WAIT_V(5);
  BAR();
  STAMP(4);

  f32x16 acc[4];
  constexpr int NA = KM + 1;
  f32x16 oacc[3];
  u32x4 actp[2], gr[2];
  float tn, tr, tprev, tprev2;
  f32x2 Lam, Lx, ee, cc, cl, cq, g0, g1, g2;
  const unsigned long long gm0 = (c - 1 - 4 * rg >= 0 ? 0xffffffffull : 0ull) | (c - 3 - 4 * rg >= 0 ? 0xffffffff00000000ull : 0ull);
  const unsigned long long gm1 = (c - 2 - 4 * rg >= 0 ? 0xffffffffull : 0ull) | (c - 4 - 4 * rg >= 0 ? 0xffffffff00000000ull : 0ull);
  bf16x8 fr[4], af[4];
  u32x2 fx[3], fdk[3], gc[2];
  u32x4 fst, corrA, corrB;
  float cs[8], rsl;
  const int w67 = (w + 2) >> 3, cu0 = (w << w67) * 512, cu1 = cu0 + (6 - 5 * w67) * 512;
  float sk[12], xf[12];
  unsigned sto;
  const f32x16 zero = {};
  const char* amine = wc + C_ACT + (j * 6) * 16384 + set * 8192 + rg * (SSM ? 2048 : 1024);
  const char* flagb = wc + C_FLAG + (3 * gq) * 128;
  fr[0] = wfrag(0); fr[1] = wfrag(1);
  if constexpr (!SSM) fr[2] = wfrag(2);

  const char* pbase = wc + C_PART + (rg * 9 + 3 * mi) * 20480 + gq * 4096 + set * 2048;
  auto pstore = [&](int cb, int hh) INL {
    u32x4 v;
#pragma unroll
    for (int e = 0; e < 4; ++e) v[e] = pk2(oacc[cb][8 * hh + 2 * e], oacc[cb][8 * hh + 2 * e + 1]);
    const unsigned o = pvo ^ (set * 128 + ((pvo >> 3) & 64) + hh * 32);
    if (!SSM && rg == gq) lds_wr(lds0 + ARING + 32768 + cb * 4096 + set * 2048 + hh * 1024 + o, v);
    else gstore_sc1(pbase + cb * 20480 + hh * 1024, o, v);
  };
  const int fpk = (w >> 1) < 3 ? (gq + 1 + (w >> 1)) & 3 : 4;
  const char* ffl = wc + C_FLAG + (3 * fpk + mi) * 128;
  bool got = false;
  auto fin_ready = [&]() INL { return __builtin_amdgcn_readfirstlane(flg[1]) == tag && __builtin_amdgcn_readfirstlane(flg[0]) >= F_FIN; };
  auto fin_fetch = [&]() INL {
    const char* src = wc + C_PART + (gq * 9 + 3 * mi) * 20480 + fpk * 4096 + (w & 1) * 2048;
#pragma unroll
    for (int i = 0; i < 6; ++i) gldsA(src + (i >> 1) * 20480 + (i & 1) * 1024, pvo, lds0 + (w >> 1) * 12288 + (i >> 1) * 4096 + (w & 1) * 2048 + (i & 1) * 1024);
  };
  auto step = [&](auto NI) INL {
    constexpr int n = NI.value;
    constexpr int s = n / 12, ks = n % 12;
    if constexpr (R != 2 && n == 216 - 4) {
      const int lane = lane_now(), r = lane & 31, h = lane >> 5, G1 = (lane >> 4) & 1, q = (lane >> 2) & 3, p4 = lane & 3;
      if constexpr (SSM) {
        const int rperm = 16 * ((r >> 2) & 1) + (r & 3) + 4 * (r >> 3);
        const int row = 32 * set + rperm;
        rb2[0][0] = lds0 + row * 128 + ((h ^ ((row >> 1) & 7)) * 16);
        rb2[1][0] = rb2[0][0] ^ 32; rb2[2][0] = rb2[0][0] ^ 64; rb2[3][0] = rb2[0][0] ^ 96;
#pragma unroll
        for (int k = 0; k < 4; ++k) { rb2[k][1] = rb2[k][0] + 2 * SLOT; opaque(rb2[k][0]); opaque(rb2[k][1]); }
        const int o = p4 >> 1;
        ab[0] = lds0 + ARING + rg * 2048 + o * 1024 + G1 * 512 + (((2 * h) ^ (2 * G1 + o)) * 64) + q * 16 + (p4 & 1) * 8;
        ab[1] = ab[0] ^ 64;
        opaque(ab[0]); opaque(ab[1]);
      } else {
        const int dch = 4 * (p4 & 1) + 2 * G1 + (p4 >> 1);
        rb2[0][0] = lds0 + (4 * h + q) * 128 + (((set * 4 + (dch >> 1)) ^ (((q >> 1) & 1) << 2)) * 16) + (dch & 1) * 8;
        rb2[0][1] = rb2[0][0] + 2 * SLOT;
        ab[0] = lds0 + ARING + rg * 1024 + lane * 16;
        opaque(rb2[0][0]); opaque(rb2[0][1]); opaque(ab[0]);
      }
    }
    if constexpr (SSM && n == 16 * 12) pvo = lane_now() * 16;
    if constexpr (SSM && ks == 10 && (s & 1) == 1 && s < (R == 1 ? 11 : 17)) {
#pragma unroll
      for (int k = 0; k < 4; ++k) { rb1[k] += (s & 2) ? -2 * SLOT : 2 * SLOT; opaque(rb1[k]); }
    }
    constexpr int ss = (R == 1 && s >= 16) ? s - 4 : s;
    constexpr int pb = (ss >= 6) ? (ss - 6) / 6 : 0, ph = (ss >= 6 && ss < (R == 1 ? 18 : 24)) ? (ss - 6) % 6 : -1;
    constexpr bool mm_on = R == 0 || (R == 1 ? (n < 144 || n >= 216) : n < 216);
    if constexpr (ks == 0) {
      constexpr int pub = pub_at(R, s);
      constexpr bool poll = chk_need(R, s) != 0;
      if constexpr (poll) { if (w == 0) { const int l = lane_now(); flg = gload8_sc1(flagb, (l & 3) * 128); } }
      if constexpr (pub > 0) { if (w == 1) gstore8_sc1(flagb + mi * 128, 0u, u32x2{(unsigned)pub, tag}); }
      if constexpr (SSM && ph == 3) {
        const unsigned go = 3 * tabb - tabh;
        const char* gb = ws + WS_GRAN + ((R == 2 ? pb * 3 + 2 : mi * 3 + pb) * 16 + c - 1 - 4 * rg) * 1024 + rg * 512 - 2 * (lds0 + TAB_OFF);
        gr[0] = gload16_sc1(gb, go);
        gr[1] = gload16_sc1(gb - 1024, go);
      }
    }
    if constexpr (s < 3 * km(R)) {
      constexpr int kt = s / km(R), v = n - 12 * km(R) * kt;
      if constexpr (v == 0) { xn_read(kt, 0); xn_read(kt, 1); xn_read(kt, 2); xn_read(kt, 3); }
      if constexpr (SSM) {
        if constexpr (v >= 1 && v + 3 < 12) xn_read(kt, v + 3);
        constexpr int ch = v == 21 ? 5 : (v >= 12 && v <= 20 && v % 2 == 0) ? (v - 12) / 2 : -1;
        if constexpr (kt < 2 && ch >= 0) { xwrite1(ch / 3, 3 * (kt + 1) + ch % 3); SCHED(); }
      } else if constexpr (v >= 1 && v <= 4) { xn_read(kt, 2 * v + 2); xn_read(kt, 2 * v + 3); }
    }
    if constexpr (!SSM && n == 84) {
      fst = gload16_sc1(wc + C_STAT, (32 * rg) * 16 + (pvo & 496));
      const char* gb = ws + WS_CSUM + j * 8192;
      gc[0] = gload8_sc1(gb + cu0, pvo >> 1);
      gc[1] = gload8_sc1(gb + cu1, pvo >> 1);
    }
    if constexpr (SSM && ks == 4 && (s == 8 || s == 11 || (R == 2 && (s == 14 || s == 17)))) {
      const int l = lane_now(), r = l & 31;
      const int row = 32 * rg + 16 * ((r >> 2) & 1) + (r & 3) + 4 * (r >> 3);
      const unsigned a = lds0 + X_OFF + 32768 + row * 128 + (((l >> 5) ^ ((row >> 1) & 7)) * 16);
      xn[32] = BF8(lds_rd(a)); xn[33] = BF8(lds_rd(a ^ 32)); xn[34] = BF8(lds_rd(a ^ 64)); xn[35] = BF8(lds_rd(a ^ 96));
    }
    if constexpr (op_w(R, n)) { if constexpr (s == 0) dmaW(2 + ks / 3, ks % 3); else dmaW(w_slot(R, s), ks / 3); }
    if constexpr (op_x(R, n)) dmaX(n / 36 + 1, x_piece(n));
    if constexpr (op_a(R, n)) {
      if constexpr (SSM) {
        constexpr int pc = a_code(n) & 1, v = (a_code(n) - pc) / 2, u = v + pc;
        gldsA((u_hlp(u) < 0 ? acm[u_hlp(u) < 0 ? u_mem(u) : 0] + (2 * u_blk(u) + (u & 1)) * 16384 : wc + C_ACT + (15 * 6 + 2 * u_hlp(u) + (u & 1)) * 16384 + w * 1024) + (pc ? 0 : 8192), pvo,
              lds0 + ARING + (v == 17 ? 3 : (v + 1) % 3) * 16384 + pc * 8192 + w * 1024);
      } else {
        constexpr int u = s + 3 - 18;
        gldsA(acm[u % 3] + (u / 3) * 16384 + (ks / 3) * 8192, pvo, lds0 + ARING + (u & 3) * 16384 + w * 1024 + (ks / 3) * 8192);
      }
    }
    if constexpr (op_cs(R, n)) {
      constexpr int i = cs_piece(n);
      const char* cb_ = (const char*)P.w1 + (size_t)(288 * ((c / 6) & 1) + 36 * w + 8 * i) * (F * 2) + 768 * j + (c % 6) * 128;
      if constexpr (i < 5) xr[0][i] = gload16<0>(cb_, ((pvo >> 7) & (i < 4 ? 7 : 3)) * (F * 2) + (pvo & 112));
      else if constexpr (i == 5) epoch = gload4_sc1(ws + WS_EPOCH, 0u);
      else aux[0] = gload2((const char*)P.b1 + 768 * j + (w < 6 ? w : 0) * 128, pvo >> 3);
    }
    if constexpr (op_cp(R, n)) code_touch(3, 3072, lds0 + ((s + 3) & 3) * SLOT + ww * 1024 + 2 * 8192);
    if constexpr (op_pf(R, n)) {
      const unsigned l = lane_now();
      constexpr int sl = SSM ? (s < 3 ? s : s - 3) : (s < 6 ? s - 4 : s - 5);
      if constexpr (SSM) {
        constexpr unsigned lim = s < 3 ? 72 : 108;
        unsigned id = sl * 32 + rg * 8 + (l >> 3); id = id < lim ? id : lim - 1;
        const u16* mat = s < 3 ? (set ? P.bim : P.bre) : (set ? P.cim : P.cre);
        glds4((const char*)mat + (size_t)(192 * mi + (s < 3 ? 64 : 0)) * (D * 2) + c * lim * 128, id * 128 + (l & 7) * 4, lds0 + JUNK_OFF);
      } else if constexpr (s < 3) {
        unsigned id = sl * 64 + w * 8 + (l >> 3); id = id < 144 ? id : 143;
        glds4((const char*)P.w1 + (size_t)(36 * c) * (F * 2) + 768 * j + 256, (id >> 2) * (F * 2) + (id & 3) * 128 + (l & 7) * 4, lds0 + JUNK_OFF);
      } else {
        unsigned id = sl * 64 + w * 8 + (l >> 3); id = id < 216 ? id : 215;
        glds4((const char*)P.w2 + (size_t)(384 * j) * (D * 2) + c * 216 * 128, id * 128 + (l & 7) * 4, lds0 + JUNK_OFF);
      }
    }
    if constexpr (SSM && n + 2 < (R == 1 ? 144 : 216)) fr[(n + 2) & 3] = wfrag(n + 2);
    if constexpr ((R == 0 || (R == 1 && n >= 213)) && n + 3 < 432) fr[(n + 3) & 3] = wfrag(n + 3);
    if constexpr (R == 2) {
    } else if constexpr (SSM) {
      constexpr int u = s - 18;
      if constexpr (s >= 18 && s < 35 && (ks == 3 || ks == 6)) af[ks / 3 + 1] = afrag(4 * u + ks / 3 + 1);
      if constexpr (s >= 17 && s < 35 && (ks == 9 || ks == 10)) af[ks - 9] = afrag(4 * (u + 1) + ks - 9);
      if constexpr (n == 419 || n == 420) af[n - 417] = afrag(68 + n - 417);
    } else {
      if constexpr (n + 3 >= 216 && n + 3 <= 420 && (n + 3) % 3 == 0) af[((n + 3 - 216) / 3) & 3] = afrag((n + 3 - 216) / 3);
      if constexpr (n >= 418 && n <= 420) af[n - 417] = afrag(68 + n - 417);
      if constexpr (op_fo(R, n)) {
        constexpr int i = s - 25;
        const unsigned rw = w * 4 + (pvo >> 8), co = (pvo >> 1) & 120;
        if constexpr (i < 3) fx[i] = gload8((const char*)P.x + (size_t)(128 * c + 32 * gq) * (D * 2) + 384 * mi + i * 128, rw * (D * 2) + co);
        else if constexpr (i < 6) fdk[i - 3] = gload8((const char*)P.dsk + 384 * mi + (i - 3) * 128, co);
        else fst = gload16_sc1(wc + C_STAT, (32 * gq + rw) * 16);
      }
      if constexpr (n == 408) { pin2(fx[0]); pin2(fx[1]); pin2(fx[2]); pin2(fdk[0]); pin2(fdk[1]); pin2(fdk[2]); wait1<63>(fst); flg = gload8_sc1(ffl, 0u); rsl = __uint_as_float(fst[2]) * LOG2E; tn = __uint_as_float(fst[2]) * (1.f / LOG2E); opaquef(rsl); opaquef(tn); }
      if constexpr (n >= 408 && n < 420) {
        constexpr int e = n - 408;
        const unsigned xv = fx[e >> 2][(e >> 1) & 1], dv = fdk[e >> 2][(e >> 1) & 1];
        xf[e] = (e & 1) ? bfhi(xv) : bflo(xv);
        sk[e] = (xf[e] - __uint_as_float(fst[0])) * tn * ((e & 1) ? bfhi(dv) : bflo(dv));
        opaquef(xf[e]); opaquef(sk[e]);
      }
      if constexpr (n == 422) {
        SCHED();
        wait0_2(flg);
        got = fin_ready();
        if (got) fin_fetch();
      }
    }
    if constexpr (!mm_on) {
    } else if constexpr (s < 18) {
      constexpr int T = p1_tile(R, s), kt = p1_kt(R, s);
      if constexpr (SSM) { acc[T & 3] = MFMA32(xn[12 * kt + ks], fr[n & 3], (kt == 0 && ks == 0) ? zero : acc[T & 3]); opaque16(acc[T & 3]); }
      else { acc[T % NA] = MFMA32(fr[n & 3], xn[12 * kt + ks], (kt == 0 && ks == 0) ? zero : acc[T % NA]); opaque16(acc[T % NA]); }
      if constexpr (SSM && s < 2 * km(R)) {
        constexpr int v = n - 12 * km(R) * kt, q = km(R) == 3 ? 4 : 6;
        constexpr int d0 = (q * v + 4) / 5, d1 = (q * (v + 1) + 4) / 5;
        if constexpr (d0 < d1 && d0 < 24) { constexpr int ch = d0 >> 2; xr[ch / 3][3 * (kt + 1) + ch % 3][d0 & 3] = norm4(xr[ch / 3][3 * (kt + 1) + ch % 3][d0 & 3], rs[ch / 3], nb[ch / 3]); }
        if constexpr (d0 + 1 < d1 && d0 + 1 < 24) { constexpr int d = d0 + 1, ch = d >> 2; xr[ch / 3][3 * (kt + 1) + ch % 3][d & 3] = norm4(xr[ch / 3][3 * (kt + 1) + ch % 3][d & 3], rs[ch / 3], nb[ch / 3]); }
      }
    } else {
      oacc[p2_cb(n)] = MFMA32(fr[n & 3], af[p2_k4(n)], n < 219 ? zero : oacc[p2_cb(n)]);
      opaque16(oacc[p2_cb(n)]);
      if constexpr (n == 425 || n == 426 || n == 429 || n == 430) pstore((n - 425) / 4, (n - 425) & 1);
    }
    if constexpr (!SSM) {
      if constexpr (n >= CS0 && n <= 56) {
        if constexpr (n == CS0) {
          SCHED();
          wait3<ops_between(0, (CSV == 0 ? 29 : 26) + 1, CS0)>(xr[0][0], xr[0][1], xr[0][2]); wait2<63>(xr[0][3], xr[0][4]);
#pragma unroll
          for (int d = 0; d < 4; ++d) { const unsigned v = msel_u(xr[0][4][d], 0xffffffffull); cs[2 * d] = bflo(v); cs[2 * d + 1] = bfhi(v); opaquef(cs[2 * d]); opaquef(cs[2 * d + 1]); }
        } else {
          constexpr int d0 = ((n - CS0 - 1) * 16) / (56 - CS0), d1 = ((n - CS0) * 16) / (56 - CS0);
#pragma unroll
          for (int d = d0; d < d1; ++d) { const unsigned v = xr[0][d >> 2][d & 3]; cs[2 * (d & 3)] += bflo(v); opaquef(cs[2 * (d & 3)]); cs[2 * (d & 3) + 1] += bfhi(v); opaquef(cs[2 * (d & 3) + 1]); }
        }
      }
      if constexpr (n == 57) {
#pragma unroll
        for (int e = 0; e < 4; ++e) { auto t = __builtin_amdgcn_permlane32_swap(__float_as_uint(cs[e]), __float_as_uint(cs[e + 4]), false, false); cs[e] = __uint_as_float(t[0]) + __uint_as_float(t[1]); opaquef(cs[e]); }
      }
      if constexpr (n == 58) {
#pragma unroll
        for (int e = 0; e < 2; ++e) { auto t = __builtin_amdgcn_permlane16_swap(__float_as_uint(cs[e]), __float_as_uint(cs[e + 2]), false, false); cs[e] = dpp_add<0x128>(__uint_as_float(t[0]) + __uint_as_float(t[1])); opaquef(cs[e]); }
        const unsigned l = pvo >> 4;
        *(LAS float*)(lds0 + TAB_OFF + 8192 + w * 256 + (8 * (l & 7) + (l >> 3)) * 4) = bsel(cs[0], cs[1], 0xff00ff00ff00ff00ull);
      }
      if constexpr (n == 60) {
        opaque(epoch); opaque(aux[0]); tag = __builtin_amdgcn_readfirstlane(epoch) + 1u;
        if (w == 2) {
          float t = 0.f;
#pragma unroll
          for (int k = 0; k < 8; ++k) t += *(LAS float*)(lds0 + TAB_OFF + 8192 + k * 256 + (pvo >> 2));
          gstore8_sc1(ws + WS_CSUM + j * 8192 + c * 512, pvo >> 1, u32x2{__float_as_uint(t), tag});
        }
      }
      if constexpr (n == 92) {
        SCHED();
        waitg<ops_between(0, 84, 92)>(fst, gc[0], gc[1]);
        auto stale = [&]() INL { return __builtin_amdgcn_ballot_w64(((fst[1] ^ tag) | (fst[3] ^ tag) | (gc[0][1] ^ tag) | (gc[1][1] ^ tag)) != 0u) != 0ull; };
        if (__builtin_expect(stale(), 0)) {
          const char* gb = ws + WS_CSUM + j * 8192;
          int spins = 200000;
          do { reloadg(fst, gc[0], gc[1], wc + C_STAT + (32 * rg) * 16, gb + cu0, gb + cu1, pvo & 496, pvo >> 1); } while (stale() && --spins > 0);
        }
        const float Sv = __uint_as_float(gc[0][0]) + __uint_as_float(gc[1][0]);
        const unsigned Sh = pk2(Sv, Sv), Sl = pk2(Sv - bfhi(Sh), 0.f);
        lds_wr(lds0 + TAB_OFF + w * 1024 + pvo, u32x4{Sh, (Sl & 0xffffu) | (aux[0] << 16), aux[0], 0u});
      }
      if constexpr (n == 93) {
        const float mu = -__uint_as_float(fst[0]), sg = __builtin_amdgcn_rcpf(__uint_as_float(fst[2]));
        const unsigned hi = pk2(mu, sg), lo = pk2(mu - bflo(hi), sg - bfhi(hi));
        corrB = u32x4{msel_u((hi & 0xffffu) | (lo << 16), 0xffffffffull), msel_u(hi, 0xffffffffull), msel_u(lo >> 16, 0xffffffffull), 0u};
        rsl = __uint_as_float(fst[2]) * LOG2E;
        wait1<63>(corrB); opaquef(rsl);
      }
      if constexpr (corr_tile(n + (n == 96 ? 3 : 6)) >= (n == 96 ? 0 : 1)) corrA = lds_rd(tabb + corr_tile(n + (n == 96 ? 3 : 6)) * 1024);
      if constexpr (corr_tile(n) >= 0) { acc[corr_tile(n) % NA] = MFMA32(BF8(corrA), BF8(corrB), acc[corr_tile(n) % NA]); opaque16(acc[corr_tile(n) % NA]); }
      if constexpr (n >= EPI0 && n < EPI0 + 198) {
        constexpr int T = (n - EPI0) / 33, u = (n - EPI0) % 33, e = u >> 1;
        const f32x16& pa = acc[T % NA];
        if constexpr ((u & 1) == 0) {
          if constexpr (e < 16) { tn = __builtin_amdgcn_exp2f(pa[e] * rsl); opaquef(tn); }
          if constexpr (e >= 1) {
            const float a = __builtin_fmaf(-pa[e - 1], tr, pa[e - 1]);
            if constexpr (((e - 1) & 1) == 0) { tprev = a; opaquef(tprev); }
            else { unsigned pk = pk2(tprev, a); opaque(pk); actp[(e - 1) >> 3][((e - 1) >> 1) & 3] = pk; }
          }
        } else { tr = __builtin_amdgcn_rcpf(__builtin_fmaf(tn, __builtin_fmaf(tn, 0.5f, 1.f), 1.f)); opaquef(tr); }
      }
      if constexpr (op_st(0, n)) { constexpr int id = st_id(s); gstore_sc1(amine + (id >> 1) * 16384 + (id & 1) * 4096, pvo, actp[id & 1]); }
    } else if constexpr (ph >= 0) {
      f32x16& ur = acc[(2 * pb) & 3];
      f32x16& ui = acc[(2 * pb + 1) & 3];
      const unsigned lt = tabb + pb * 512;
      const unsigned wt = tabb + (WT_OFF - TAB_OFF) + 2048, gt = tabb + (GT_OFF - TAB_OFF);
      if constexpr (ph == 0) {
        if constexpr (ks == 0) { ee = cfma(Lam, f32x2{ur[0], ui[0]}, f32x2{ur[1], ui[1]}); opaquec(ee); }
        if constexpr (ks >= 1 && ks <= 9) {
          constexpr int t0 = 2 + ((ks - 1) * 14) / 9, t1 = 2 + (ks * 14) / 9;
          ee = cfma(Lam, ee, f32x2{ur[t0], ui[t0]});
          if constexpr (t0 + 1 < t1) ee = cfma(Lam, ee, f32x2{ur[t0 + 1], ui[t0 + 1]});
          opaquec(ee);
        }
        if constexpr (ks == 7) Lx = lds_rd8(lt + 2 * 1536);
        if constexpr (ks == 10) {
          const f32x2 elo = {up32(ee[0]), up32(ee[1])};
          lds_wr8(tabh + (WT_OFF - TAB_OFF), cfma_s(Lx, elo, ee));
        }
        if constexpr (ks == 11) { Lam = csq(Lx); opaquec(Lam); }
      }
      if constexpr (ph == 1) {
        if constexpr (ks == 0) { g0 = lds_rd8(wt); g1 = lds_rd8(wt + 512); g2 = lds_rd8(wt + 1024); ee = lds_rd8(wt + 1536); }
        if constexpr (ks == 2) {
          cl = f32x2{0.f, 0.f}; cq = f32x2{1.f, 0.f};
          if (rg > 0) { cl = g0; cq = Lam; }
          if (rg > 1) { cl = cfma_s(Lam, cl, g1); cq = cmul_s(Lam, Lam); }
          if (rg > 2) {
            cl = cfma_s(Lam, cl, g2); cq = cmul_s(Lam, cq);
            const f32x2 E = cfma_s(Lam, cl, ee);
            gstore_sc1(ws + WS_GRAN + ((R == 2 ? pb * 3 + 2 : mi * 3 + pb) * 16 + c) * 1024 - 2 * (lds0 + TAB_OFF), 2 * tabb,
                       u32x4{__float_as_uint(E[0]), tag, __float_as_uint(E[1]), tag});
          }
          opaquec(cl); opaquec(cq);
          g0 = lds_rd8(wt - 2048 + rg * 512); cc = lds_rd8(lt + 1536);
        }
        if constexpr (ks == 4) {
          const f32x2 a = cfma_s(Lx, cl, g0);
          cl = f32x2{usel(cl[0], a[0]), usel(cl[1], a[1])};
          opaquec(cl);
        }
        if constexpr (ks == 5) { const f32x2 a = cmul_s(Lx, cq); cq = f32x2{usel(cq[0], a[0]), usel(cq[1], a[1])}; opaquec(cq); }
        if constexpr (ks == 6) { cl = cmul_s(cc, cl); opaquec(cl); }
        if constexpr (ks == 7) { cq = cmul_s(cc, cq); opaquec(cq); }
      }
      if constexpr ((ph == 1 && ks >= 8) || ph == 2) {
        constexpr int t = (ph == 1) ? ks - 8 : ks + 4;
        const float t1 = cc[1] * ui[t], t2 = cc[1] * ur[t];
        float a = __builtin_fmaf(cc[0], ur[t], -t1), b = __builtin_fmaf(-cc[0], ui[t], -t2);
        opaquef(a); opaquef(b);
        ur[t] = a; ui[t] = b;
      }
      if constexpr (ph == 3) {
        if constexpr (ks == 4) cc = lds_rd8(lt + 3 * 1536);
        if constexpr (ks == 7) { Lx = csq(cc); opaquec(Lx); }
        if constexpr (ks == 9) {
          SCHED();
          wait2<ops_between(R, 12 * s, n)>(gr[0], gr[1]);
          auto stale = [&]() INL {
            const unsigned b0 = (gr[0][1] ^ tag) | (gr[0][3] ^ tag), b1 = (gr[1][1] ^ tag) | (gr[1][3] ^ tag);
            return ((__builtin_amdgcn_ballot_w64(b0 != 0u) & gm0) | (__builtin_amdgcn_ballot_w64(b1 != 0u) & gm1)) != 0ull;
          };
          if (__builtin_expect(stale(), 0)) {
            const unsigned go = 3 * tabb - tabh;
            const char* gb = ws + WS_GRAN + ((R == 2 ? pb * 3 + 2 : mi * 3 + pb) * 16 + c - 1 - 4 * rg) * 1024 + rg * 512 - 2 * (lds0 + TAB_OFF);
            int spins = 200000;
            do { reload2(gr[0], gr[1], gb, gb - 1024, go); } while (stale() && --spins > 0);
          }
          const f32x2 a0 = {msel(gr[0][0], gm0), msel(gr[0][2], gm0)}, a1 = {msel(gr[1][0], gm1), msel(gr[1][2], gm1)};
          ee = cfma_s(cc, a1, a0); opaquec(ee);
        }
        if constexpr (ks == 10) {
          const f32x2 lo = {up32(ee[0]), up32(ee[1])};
          lds_wr8(tabh + (GT_OFF - TAB_OFF), cfma_s(Lx, ee, lo));
        }
        if constexpr (ks == 11) { cc = csq(Lx); opaquec(cc); }
      }
      if constexpr (ph == 4 && ks <= 3) {
        if constexpr (ks == 0) { ee = lds_rd8(gt + 2048 + 1536); g0 = lds_rd8(gt + 2048 + 1024); g1 = lds_rd8(gt + 2048 + 512); g2 = lds_rd8(gt + 2048); Lam = lds_rd8(lt); }
        if constexpr (ks == 2) { ee = cfma(cc, cfma(cc, ee, g0), g1); opaquec(ee); }
        if constexpr (ks == 3) { ee = cfma(cq, cfma(cc, ee, g2), cl); ee[1] = -ee[1]; opaquec(ee); }
      }
      if constexpr ((ph == 4 && ks >= 4) || (ph == 5 && ks <= 7)) {
        constexpr int tt = (ph == 4) ? ks - 4 : ks + 8;
        ee = cfma(f32x2{Lam[0], -Lam[1]}, ee, f32x2{ur[tt], ui[tt]});
        opaquec(ee);
        if constexpr ((tt & 1) == 0) { tprev = ee[0]; tprev2 = ee[1]; }
        else {
          unsigned p0 = pk2(tprev, ee[0]), p1 = pk2(tprev2, ee[1]); opaque(p0); opaque(p1);
          if constexpr (tt < 8) { gr[0][tt >> 1] = p0; gr[1][tt >> 1] = p1; }
          else { actp[0][(tt >> 1) & 3] = p0; actp[1][(tt >> 1) & 3] = p1; }
        }
      }
      if constexpr (op_st(R, n)) {
        constexpr int o = (ks - 7) >> 1, part = (ks - 7) & 1;
        if constexpr (ks == 7) { const int l = lane_now(); sto = (l ^ (8 * (l >> 5))) * 16; }
        if constexpr (ks == 9) sto ^= 64;
        gstore_sc1(amine + (2 * pb + part) * 16384 + o * 1024, sto, o ? actp[part] : gr[part]);
      }
    }
    if constexpr (SSM && ks == 9 && (s == 5 || s == 11 || (R == 2 && s == 17))) Lam = lds_rd8(tabb + ((s - 5) / 6) * 512);
    if constexpr (n == 8) { SCHED(); WAIT_V(ops_between(R, 0, 8) + (SSM ? 0 : 2)); BAR(); }
    if constexpr (R == 1 && n == 212) {
      SCHED(); WAIT_V(0); BAR();
      if (w == 0) { const int l = lane_now(); flg = gload8_sc1(flagb, (l & 3) * 128); }
    }
    if constexpr (R == 1 && n == 221) {
      SCHED();
      if (w == 0) {
        wait0_2(flg);
        const int l = lane_now();
        int spins = 200000;
        while ((__builtin_amdgcn_ballot_w64(flg[1] != tag || flg[0] < 2u) & 7ull) != 0ull && --spins > 0) { flg = gload8_sc1(flagb, (l & 3) * 128); wait0_2(flg); }
      }
      BAR();
    }
    if constexpr (R == 1 && n == 236) { SCHED(); WAIT_V(vm_n(1, 222, 236)); BAR(); }
    if constexpr (!SSM && n == 44) { SCHED(); BAR(); }
    if constexpr (ks == 11 && s < 34) {
      SCHED();
      if constexpr (SSM ? (s == 1 || s == 3) : (s == 4 || s == 7)) WAIT_L0();
      if constexpr (SSM && (ph == 0 || ph == 3 || s >= 17)) WAIT_L0();
      WAIT_V(drain(R, s) ? 0 : (R == 1 && s == 16) ? ops_between(1, 192, n) : vm_n(R, s == 0 ? 2 : 12 * s - 1, n));
      if constexpr (chk_need(R, s) != 0) {
        if (w == 0) {
          constexpr unsigned need = chk_need(R, s);
          constexpr unsigned long long lanes = (R == 1 && s != 26) ? 8ull : 7ull;
          WAIT_V(ops_between(R, 12 * s, n));
          pin2(flg);
          auto late = [&]() INL { return (__builtin_amdgcn_ballot_w64(flg[1] != tag || flg[0] < need) & lanes) != 0ull; };
          if (__builtin_expect(late(), 0)) {
            const int l = lane_now();
            int spins = 200000;
            do { flg = gload8_sc1(flagb, (l & 3) * 128); wait0_2(flg); } while (late() && --spins > 0);
          }
        }
      }
      BAR();
      STAMP(5 + s);
    }
    SCHED();
  };
  run_seq<0>(step, __make_integer_seq<ISeq, int, 144>{});
  if constexpr (R != 1) run_seq<144>(step, __make_integer_seq<ISeq, int, 48>{});
  else { STAMP(17); STAMP(18); STAMP(19); STAMP(20); }
  run_seq<192>(step, __make_integer_seq<ISeq, int, 24>{});
  run_seq<216>(step, __make_integer_seq<ISeq, int, R == 2 ? 71 : 216>{});
  if constexpr (R != 2) { pstore(2, 0); pstore(2, 1); }
  STAMP(39);
  WAIT_L0();
  WAIT_V(0);
  BAR();
  STAMP(40);
  if (w == 1) gstore8_sc1(flagb + mi * 128, 0u, u32x2{F_FIN, tag});
  if constexpr (!SSM) {
    const int l = lane_now(), t = w * 64 + l;
    const int c4 = t & 15, rho = c4 >> 1;
    const unsigned ra = lds0 + (c4 >> 3) * 2048 + (rho & 1) * 1024 + ((c4 >> 2) & 1) * 512 + (((t >> 4) ^ (2 * rho)) * 16) + (c4 & 1) * 8;
    float* op = P.out + (size_t)(128 * c + 32 * gq + (t >> 4)) * D + 192 * mi + c4 * 4;
    if (!got) {
      int spins = 400000;
      do { flg = gload8_sc1(ffl, 0u); wait0_2(flg); } while (!fin_ready() && --spins > 0);
      fin_fetch();
    }
    STAMP(41);
    float zo[12];
#pragma unroll
    for (int cb = 0; cb < 3; ++cb) {
      const u32x2 ov = *(LAS u32x2*)(ra + ARING + 32768 + cb * 4096);
      zo[4 * cb] = bflo(ov[0]); zo[4 * cb + 1] = bfhi(ov[0]); zo[4 * cb + 2] = bflo(ov[1]); zo[4 * cb + 3] = bfhi(ov[1]);
    }
#pragma unroll
    for (int e = 0; e < 12; ++e) opaquef(zo[e]);
    STAMP(42);
    WAIT_V(0);
    STAMP(43);
    BAR();
    STAMP(44);
#pragma unroll
    for (int cb = 0; cb < 3; ++cb) {
      u32x2 pv[4];
#pragma unroll
      for (int k = 0; k < 4; ++k) pv[k] = *(LAS u32x2*)(ra + k * 12288 + cb * 4096);
      f32x4 o;
#pragma unroll
      for (int e = 0; e < 4; ++e) {
        auto up = [&](unsigned v) INL { return (e & 1) ? bfhi(v) : bflo(v); };
        const float z = ((zo[4 * cb + e] + up(pv[0][e >> 1])) + (up(pv[1][e >> 1]) + up(pv[2][e >> 1]))) * rsl;
        const float gt = z * __builtin_amdgcn_rcpf(1.f + __builtin_amdgcn_exp2f(-z));
        o[e] = __builtin_fmaf(__builtin_fmaf(up(pv[3][e >> 1]), 0.693147180559945f, sk[4 * cb + e]), gt, xf[4 * cb + e]);
      }
      asm volatile("global_store_dwordx4 %0, %1, off sc1\n\ts_nop 1" ::"v"(op + 64 * cb), "v"(o) : "memory");
    }
    if (c == 15 && j == 0 && t == 0) *(unsigned*)(ws + WS_EPOCH) = tag;
  }
  if constexpr (R == 2) asm volatile("fk_hlp_end:"); else if constexpr (R == 1) asm volatile("fk_ssm_end:"); else asm volatile("fk_mlp_end:");
  if constexpr (SSM) { STAMP(41); STAMP(42); STAMP(43); STAMP(44); }
  STAMP(45);
  STAMP(46);
  RSTAMP(61);
  STAMP_OUT();
}

__global__ __launch_bounds__(512, 2) void main_kernel(const Params P) {
  __shared__ __attribute__((aligned(16))) char smem[163840];
  const unsigned lds0 = (unsigned)(size_t)(LAS char*)smem;
  const int b = blockIdx.x, xcd = b & 7, idx = b >> 3;
#define MAP 2
  int j, c;
  if (MAP == 0) { j = idx >> 1; c = 2 * xcd + (idx & 1); }
  else if (idx >= 24) { j = 12 + ((idx - 24) >> 1); c = 2 * xcd + (idx & 1); }
  else if (MAP == 1) { j = 6 * (xcd & 1) + (idx >> 2); c = 4 * (xcd >> 1) + (idx & 3); }
  else { j = 3 * (xcd & 3) + (idx >> 3); c = 8 * (xcd >> 2) + (idx & 7); }
#if defined(__HIP_DEVICE_COMPILE__)
  if (__builtin_expect(j < 12, 1)) body<0>(P, lds0, c, j);
  else if (j < 15) body<1>(P, lds0, c, j);
  else body<2>(P, lds0, c, j);
#endif
}

extern "C" void kernel_launch(void* const* d_in, const int* in_sizes, int n_in, void* d_out, int out_size, void* d_ws, size_t ws_size,
                              hipStream_t stream) {
  Params P;
  P.x = (const u16*)d_in[0]; P.lre = (const u16*)d_in[1]; P.lim = (const u16*)d_in[2];
  P.bre = (const u16*)d_in[3]; P.bim = (const u16*)d_in[4]; P.cre = (const u16*)d_in[5]; P.cim = (const u16*)d_in[6];
  P.ldt = (const u16*)d_in[7]; P.w1 = (const u16*)d_in[10]; P.b1 = (const u16*)d_in[11]; P.w2 = (const u16*)d_in[12];
  P.dsk = (const u16*)d_in[8]; P.ws = (char*)d_ws; P.out = (float*)d_out;
  main_kernel<<<256, 512, 0, stream>>>(P);
}
```

```cpp
#include <hip/hip_runtime.h>
#include <hip/hip_bf16.h>
#pragma clang diagnostic ignored "-Wint-to-pointer-cast"

typedef unsigned short u16;
typedef __attribute__((ext_vector_type(2))) __bf16 bf16x2;
typedef __attribute__((ext_vector_type(4))) __bf16 bf16x4;
typedef __attribute__((ext_vector_type(8))) __bf16 bf16x8;
typedef __attribute__((ext_vector_type(4))) short s16x4;
typedef __attribute__((ext_vector_type(2))) float f32x2;
typedef __attribute__((ext_vector_type(4))) float f32x4;
typedef __attribute__((ext_vector_type(16))) float f32x16;
typedef __attribute__((ext_vector_type(2))) unsigned u32x2;
typedef __attribute__((ext_vector_type(4))) unsigned u32x4;
#define LAS __attribute__((address_space(3)))
#define GAS __attribute__((address_space(1)))
#define DI __device__ __forceinline__
#define INL __attribute__((always_inline))

constexpr int S = 2048, D = 576, F = 4608;
constexpr float LOG2E = 1.44269504088896f;
constexpr int SLOT = 24576;
constexpr int X_OFF = 4 * SLOT;
constexpr int ARING = X_OFF;
constexpr int TAB_OFF = ARING + 49152;
constexpr int LT_OFF = TAB_OFF;
constexpr int WT_OFF = TAB_OFF + 6144;
constexpr int JUNK_OFF = TAB_OFF + 14336;
constexpr int GT_OFF = TAB_OFF + 10240;
constexpr size_t WS_EPOCH = 0;
constexpr size_t WS_GRAN = 32768;
constexpr size_t WS_CSUM = 196608;
constexpr size_t WS_C0 = 327680, WS_CS = 2318336;
constexpr int C_FLAG = 0;
constexpr int C_FLGW = 2048;
constexpr int C_STAT = 4096;
constexpr int C_ACT = 8192;
constexpr int C_PART = C_ACT + 1572864;

DI float bflo(unsigned v) { return __uint_as_float(v << 16); }
DI float bfhi(unsigned v) { return __uint_as_float(v & 0xffff0000u); }
DI unsigned pk2(float a, float b) { f32x2 v = {a, b}; return __builtin_bit_cast(unsigned, __builtin_convertvector(v, bf16x2)); }
#define WAIT_V(n) asm volatile("s_waitcnt vmcnt(%0)" ::"n"(n) : "memory")
#define WAIT_L0() asm volatile("s_waitcnt lgkmcnt(0)" ::: "memory")
#define BAR() asm volatile("s_barrier" ::: "memory")
#define SCHED() __builtin_amdgcn_sched_barrier(0)
#define BF8(x) __builtin_bit_cast(bf16x8, (x))
#define MFMA16(a, b, c) __builtin_amdgcn_mfma_f32_16x16x32_bf16((a), (b), (c), 0, 0, 0)
#define MFMA32(a, b, c) __builtin_amdgcn_mfma_f32_32x32x16_bf16((a), (b), (c), 0, 0, 0)
template <int I> struct IC { static constexpr int value = I; };
template <int B, int E, class Fn> DI void static_for(Fn&& f) { if constexpr (B < E) { f(IC<B>{}); static_for<B + 1, E>(f); } }

template <class T, T... I> struct ISeq {};
template <int B, class Fn, int... I> DI void run_seq(Fn&& f, ISeq<int, I...>) { (f(IC<B + I>{}), ...); }

DI void gldsW(const void* sbase, unsigned voff, unsigned lds_base, int imm) {
  asm volatile("s_add_u32 m0, %2, %3\n\ts_nop 0\n\tglobal_load_lds_dwordx4 %0, %1" ::"v"(voff), "s"(sbase), "s"(lds_base), "n"(imm) : "memory", "scc");
}
DI void gldsA(const void* sbase, unsigned voff, unsigned lds_base, int imm) {
  asm volatile("s_add_u32 m0, %2, %3\n\ts_nop 0\n\tglobal_load_lds_dwordx4 %0, %1 sc1" ::"v"(voff), "s"(sbase), "s"(lds_base), "n"(imm) : "memory", "scc");
}
DI void glds4(const void* sbase, unsigned voff, unsigned lds_addr) {
  asm volatile("s_mov_b32 m0, %2\n\ts_nop 3\n\tglobal_load_lds_dword %0, %1" ::"v"(voff), "s"(sbase), "s"(lds_addr) : "memory");
}
DI void gstore_sc1(const void* sbase, unsigned voff, u32x4 v) {
  asm volatile("s_nop 4\n\tglobal_store_dwordx4 %0, %1, %2 sc1\n\ts_nop 1" ::"v"(voff), "v"(v), "s"(sbase) : "memory");
}
DI void gstore8_sc1(const void* sbase, unsigned voff, u32x2 v) {
  asm volatile("s_nop 4\n\tglobal_store_dwordx2 %0, %1, %2 sc1\n\ts_nop 1" ::"v"(voff), "v"(v), "s"(sbase) : "memory");
}
template <int OFF> DI u32x4 gload16(const void* sbase, unsigned voff) {
  u32x4 v; asm volatile("global_load_dwordx4 %0, %1, %2 offset:%3" : "=v"(v) : "v"(voff), "s"(sbase), "n"(OFF) : "memory"); return v;
}
DI u32x4 gload16_sc1(const void* sbase, unsigned voff) {
  u32x4 v; asm volatile("s_nop 4\n\tglobal_load_dwordx4 %0, %1, %2 sc1" : "=v"(v) : "v"(voff), "s"(sbase) : "memory"); return v;
}
DI u32x2 gload8_sc1(const void* sbase, unsigned voff) {
  u32x2 v; asm volatile("s_nop 4\n\tglobal_load_dwordx2 %0, %1, %2 sc1" : "=v"(v) : "v"(voff), "s"(sbase) : "memory"); return v;
}
DI u32x2 gload8(const void* sbase, unsigned voff) {
  u32x2 v; asm volatile("s_nop 4\n\tglobal_load_dwordx2 %0, %1, %2" : "=v"(v) : "v"(voff), "s"(sbase) : "memory"); return v;
}
DI unsigned gload4_sc1(const void* sbase, unsigned voff) {
  unsigned v; asm volatile("s_nop 4\n\tglobal_load_dword %0, %1, %2 sc1" : "=v"(v) : "v"(voff), "s"(sbase) : "memory"); return v;
}
DI unsigned gload2(const void* sbase, unsigned voff) {
  unsigned v; asm volatile("s_nop 4\n\tglobal_load_ushort %0, %1, %2" : "=v"(v) : "v"(voff), "s"(sbase) : "memory"); return v;
}
template <int N> DI void wait1(u32x4& a) { asm volatile("s_waitcnt vmcnt(%1)" : "+v"(a) : "n"(N) : "memory"); }
template <int N, int X> DI void wait1x(u32x4& a, unsigned more) {
  asm volatile("s_waitcnt vmcnt(%2)\n\ts_cmp_eq_u32 %1, 0\n\ts_cbranch_scc1 1f\n\ts_waitcnt vmcnt(%3)\n1:" : "+v"(a) : "s"(more), "n"(N + X), "n"(N) : "memory", "scc");
}
template <int N> DI void wait3(u32x4& a, u32x4& b, u32x4& c) { asm volatile("s_waitcnt vmcnt(%3)" : "+v"(a), "+v"(b), "+v"(c) : "n"(N) : "memory"); }
template <int N> DI void wait2(u32x4& a, u32x4& b) { asm volatile("s_waitcnt vmcnt(%2)" : "+v"(a), "+v"(b) : "n"(N) : "memory"); }
template <int N> DI void wait4(u32x2& a, u32x2& b, u32x2& c, u32x2& d) { asm volatile("s_waitcnt vmcnt(%4)" : "+v"(a), "+v"(b), "+v"(c), "+v"(d) : "n"(N) : "memory"); }
template <int N> DI void waitn2(u32x2& a) { asm volatile("s_waitcnt vmcnt(%1)" : "+v"(a) : "n"(N) : "memory"); }
template <int N> DI void wait5(u32x2& a, u32x2& b, u32x2& c, u32x2& d, u32x2& e) { asm volatile("s_waitcnt vmcnt(%5)" : "+v"(a), "+v"(b), "+v"(c), "+v"(d), "+v"(e) : "n"(N) : "memory"); }
DI void reload2(u32x4& a, u32x4& b, const void* pa, const void* pb, unsigned voff) {
  asm volatile("s_nop 4\n\tglobal_load_dwordx4 %0, %2, %3 sc1\n\tglobal_load_dwordx4 %1, %2, %4 sc1\n\ts_waitcnt vmcnt(0)" : "+&v"(a), "+&v"(b) : "v"(voff), "s"(pa), "s"(pb) : "memory");
}
DI float msel(unsigned v, unsigned long long m) { float o; asm("v_cndmask_b32 %0, 0, %1, %2" : "=v"(o) : "v"(v), "s"(m)); return o; }
DI float bsel(float lo, float hi, unsigned long long m) { float o; asm("v_cndmask_b32 %0, %1, %2, %3" : "=v"(o) : "v"(lo), "v"(hi), "s"(m)); return o; }
DI unsigned msel_u(unsigned v, unsigned long long m) { unsigned o; asm("v_cndmask_b32 %0, 0, %1, %2" : "=v"(o) : "v"(v), "s"(m)); return o; }
template <int N> DI void waitg(u32x4& a, u32x2& b, u32x2& c) { asm volatile("s_waitcnt vmcnt(%3)" : "+v"(a), "+v"(b), "+v"(c) : "n"(N) : "memory"); }
DI void reloadg(u32x4& a, u32x2& b, u32x2& c, const void* pa, const void* pb, const void* pc, unsigned oa, unsigned ob) {
  asm volatile("s_nop 4\n\tglobal_load_dwordx4 %0, %3, %5 sc1\n\tglobal_load_dwordx2 %1, %4, %6 sc1\n\tglobal_load_dwordx2 %2, %4, %7 sc1\n\ts_waitcnt vmcnt(0)"
               : "+&v"(a), "+&v"(b), "+&v"(c) : "v"(oa), "v"(ob), "s"(pa), "s"(pb), "s"(pc) : "memory");
}
DI void pin2(u32x2& a) { asm volatile("" : "+v"(a)::"memory"); }
DI void wait0_2(u32x2& a) { asm volatile("s_waitcnt vmcnt(0)" : "+v"(a)::"memory"); }
DI void opaque(unsigned& a) { asm volatile("" : "+v"(a)); }
DI void opaquef(float& a) { asm volatile("" : "+v"(a)); }
DI int lane_now() { int l; asm volatile("v_mbcnt_lo_u32_b32 %0, -1, 0\n\tv_mbcnt_hi_u32_b32 %0, -1, %0" : "=v"(l)); return l; }
DI void opaque16(f32x16& a) { asm volatile("" : "+v"(a)); }
DI void opaquec(f32x2& a) { float p = a[0], q = a[1]; asm volatile("" : "+v"(p), "+v"(q)); a[0] = p; a[1] = q; }
DI bf16x8 tr_frag(unsigned a0, unsigned a1) {
  bf16x4 lo = __builtin_amdgcn_ds_read_tr16_b64_v4bf16((LAS bf16x4*)a0);
  bf16x4 hi = __builtin_amdgcn_ds_read_tr16_b64_v4bf16((LAS bf16x4*)a1);
  return __builtin_shufflevector(lo, hi, 0, 1, 2, 3, 4, 5, 6, 7);
}
DI u32x4 lds_rd(unsigned a) { return *(LAS u32x4*)a; }
DI void lds_wr(unsigned a, u32x4 v) { *(LAS u32x4*)a = v; }
DI f32x2 lds_rd8(unsigned a) { return *(LAS f32x2*)a; }
DI void lds_wr8(unsigned a, f32x2 v) { *(LAS f32x2*)a = v; }
DI u32x4 norm16(u32x4 v, float a, float b) {
  unsigned a0 = v[0], a1 = v[1], a2 = v[2], a3 = v[3], t0, t1, t2, t3;
  asm("v_lshlrev_b32 %4, 16, %0\n\tv_lshlrev_b32 %5, 16, %1\n\tv_lshlrev_b32 %6, 16, %2\n\tv_lshlrev_b32 %7, 16, %3\n\t"
      "v_and_b32 %0, 0xffff0000, %0\n\tv_and_b32 %1, 0xffff0000, %1\n\tv_and_b32 %2, 0xffff0000, %2\n\tv_and_b32 %3, 0xffff0000, %3\n\t"
      "v_fma_f32 %4, %4, %8, %9\n\tv_fma_f32 %5, %5, %8, %9\n\tv_fma_f32 %6, %6, %8, %9\n\tv_fma_f32 %7, %7, %8, %9\n\t"
      "v_fma_f32 %0, %0, %8, %9\n\tv_fma_f32 %1, %1, %8, %9\n\tv_fma_f32 %2, %2, %8, %9\n\tv_fma_f32 %3, %3, %8, %9\n\t"
      "v_cvt_pk_bf16_f32 %0, %4, %0\n\tv_cvt_pk_bf16_f32 %1, %5, %1\n\tv_cvt_pk_bf16_f32 %2, %6, %2\n\tv_cvt_pk_bf16_f32 %3, %7, %3"
      : "+v"(a0), "+v"(a1), "+v"(a2), "+v"(a3), "=&v"(t0), "=&v"(t1), "=&v"(t2), "=&v"(t3) : "v"(a), "v"(b));
  return u32x4{a0, a1, a2, a3};
}
DI unsigned norm4(unsigned a, float sc, float bi) {
  unsigned t;
  asm volatile("v_lshlrev_b32 %1, 16, %0\n\tv_and_b32 %0, 0xffff0000, %0\n\tv_fma_f32 %1, %1, %2, %3\n\tv_fma_f32 %0, %0, %2, %3\n\tv_cvt_pk_bf16_f32 %0, %1, %0"
               : "+v"(a), "=&v"(t) : "v"(sc), "v"(bi));
  return a;
}
template <int CTRL> DI float dpp_add(float a) {
  return a + __builtin_bit_cast(float, __builtin_amdgcn_update_dpp(0, __builtin_bit_cast(int, a), CTRL, 0xf, 0xf, true));
}
DI f32x2 cfma(f32x2 a, f32x2 b, f32x2 c) {
  f32x2 o;
  o[0] = __builtin_fmaf(a[0], b[0], __builtin_fmaf(-a[1], b[1], c[0]));
  o[1] = __builtin_fmaf(a[0], b[1], __builtin_fmaf(a[1], b[0], c[1]));
  return o;
}
DI f32x2 cfma_s(f32x2 a, f32x2 b, f32x2 c) {
  float o0, o1;
  asm("v_fma_f32 %0, -%3, %5, %6\n\tv_fma_f32 %1, %3, %4, %7\n\tv_fmac_f32 %0, %2, %4\n\tv_fmac_f32 %1, %2, %5"
      : "=&v"(o0), "=&v"(o1) : "v"(a[0]), "v"(a[1]), "v"(b[0]), "v"(b[1]), "v"(c[0]), "v"(c[1]));
  return f32x2{o0, o1};
}
DI f32x2 cmul_s(f32x2 a, f32x2 b) {
  float o0, o1;
  asm("v_mul_f32_e64 %0, -%3, %5\n\tv_mul_f32 %1, %3, %4\n\tv_fmac_f32 %0, %2, %4\n\tv_fmac_f32 %1, %2, %5"
      : "=&v"(o0), "=&v"(o1) : "v"(a[0]), "v"(a[1]), "v"(b[0]), "v"(b[1]));
  return f32x2{o0, o1};
}
DI float usel(float lo, float hi) { float o; asm("v_cndmask_b32 %0, %1, %2, %3" : "=v"(o) : "v"(lo), "v"(hi), "s"(0xffffffff00000000ull)); return o; }
DI f32x2 csq(f32x2 a) { f32x2 o; o[0] = __builtin_fmaf(a[0], a[0], -a[1] * a[1]); o[1] = 2.f * a[0] * a[1]; return o; }
DI float sum16(float v) {
  auto t = __builtin_amdgcn_permlane16_swap(__float_as_uint(v), __float_as_uint(v), false, false);
  return __uint_as_float(t[0]) + __uint_as_float(t[1]);
}
DI float up32(float v) {
  auto t = __builtin_amdgcn_permlane32_swap(0u, __float_as_uint(v), false, false);
  return __uint_as_float(t[0]);
}

#define STAMP(i)
#define RSTAMP(i)
#define STAMP_OUT()

constexpr int KM = 3;
#define SPD 2
constexpr int km(int R) { return R == 0 ? KM : 2; }
constexpr int p1_tile(int R, int s) { return s < 3 * km(R) ? s % km(R) : s / 3; }
constexpr int p1_kt(int R, int s) { return s < 3 * km(R) ? s / km(R) : s % 3; }
constexpr int p2_cb(int n) { return n >= 420 ? (n - 420) / 4 : (n % 12) % 3; }
constexpr int p2_k4(int n) { return n >= 420 ? (n - 420) % 4 : (n % 12) / 3; }
constexpr int EPI0 = 101;
constexpr int corr_tile(int n) { return n == 99 ? 0 : n == 106 ? 1 : n == 113 ? 2 : n == 145 ? 3 : n == 181 ? 4 : n == 217 ? 5 : -1; }
constexpr int EP = 33;
#define STV 1
constexpr int st_step(int T, int o) { int r = EPI0 + EP * T + (o ? 33 : 17); while (STV ? (r % 12 != 1 && r % 12 != 4 && r % 12 != 7 && r % 12 != 9) : r % 12 != 7) ++r; return r; }
constexpr int st_id(int n) { for (int i = 0; i < 12; ++i) if (st_step(i >> 1, i & 1) == n) return i; return -1; }
constexpr bool op_w(int R, int n) {
  const int s = n / 12, k = n % 12;
  if ((R == 1 && s >= 12 && s <= 18 && s != 16) || (R == 2 && s >= 15)) return false;
  return s == 0 ? k < 6 : (s <= 32 && k % 3 == 0 && k <= 6);
}
constexpr int w_slot(int R, int s) { return R == 1 && s >= 9 && s <= 11 ? (s == 11 ? 18 : s + 11) : s + 3; }
constexpr int x_piece(int n) { const int t = n % 36; return n >= 72 ? -1 : t == 9 ? 0 : t == 11 ? 1 : t == 14 ? 2 : t == 17 ? 3 : t == 20 ? 4 : t == 22 ? 5 : -1; }
constexpr bool op_x(int R, int n) { return R == 0 && x_piece(n) >= 0; }
constexpr int a_code(int n) {
  const int s = n / 12, k = n % 12;
  if (s == 17) return k < 4 ? k - 1 : -9;
  if (s == 18) return (k >= 6 && k <= 8) ? k - 3 : -9;
  if (n == 12 * 32 + 4) return 34;
  return (s >= 19 && s <= 32 && (k == 2 || k == 5)) ? 2 * (s - 16) + (k == 5) : -9;
}
constexpr bool op_a(int R, int n) {
  const int s = n / 12, k = n % 12;
  if (R == 2) return false;
  if (R == 1) return a_code(n) != -9;
  return (k == 2 || k == 5) && s >= 15 && s <= 32;
}
constexpr bool op_pf(int R, int n) {
  return n % 12 == 8 && (R == 1 ? n / 12 <= 6 : R == 0 && n / 12 >= 5 && n / 12 <= 9 && n / 12 != 6);
}
#define NOCT 0
#define ET1 501
#define CT0 7
constexpr bool op_cp(int R, int n) { const int s = n / 12; return !NOCT && n % 12 == 5 && s >= 7 && s <= 28 && s % 7 == 0 && !(R == 1 && s == 14); }
constexpr bool op_st(int R, int n) {
  const int s = n / 12, k = n % 12;
  if (R) return (s == 11 || (R == 1 ? s == 21 : s == 17 || s == 23)) && k >= 7 && k <= 10;
  return st_id(n) >= 0;
}
constexpr int cs_piece(int n) { return n == 23 ? 4 : n == 25 ? 0 : n == 28 ? 1 : n == 31 ? 2 : n == 33 ? 3 : n == 35 ? 5 : n == 38 ? 6 : -1; }
constexpr int op_g(int R, int n) { return R == 0 && n >= 79 && n <= 81; }
constexpr bool op_cs(int R, int n) { return R == 0 && cs_piece(n) >= 0; }
#define ET2 500
constexpr bool op_e(int R, int n) { return R == 0 && (n == CT0 || n == ET1 || n == ET2); }
constexpr bool op_fo(int R, int n) { return R == 0 && n % 12 == 8 && n / 12 >= 25 && n / 12 <= 31; }
constexpr bool drain(int R, int s) { return R == 2 && s == 18; }
constexpr int n_fill(int R, int n) { return op_w(R, n) + op_x(R, n) + op_a(R, n); }
constexpr int n_lazy(int R, int n) { return op_pf(R, n) + op_cp(R, n) + op_st(R, n) + op_fo(R, n) + op_cs(R, n) + op_e(R, n) + op_g(R, n); }
constexpr int last_fill(int R, int L) { while (L >= 0 && n_fill(R, L) == 0) --L; return L; }
constexpr int ops_between(int R, int a, int e) { int n = 0; for (int i = a; i <= e; ++i) n += n_fill(R, i) + n_lazy(R, i); return n; }
constexpr int vm_n(int R, int L, int e) { const int l = last_fill(R, L); return (l >= 0 ? n_lazy(R, l) : 0) + ops_between(R, l + 1, e); }
constexpr int done_slot(int R, int m) { int s = m / 12; while (!drain(R, s) && last_fill(R, 12 * s - 1) <= m && s < 40) ++s; return s + 1; }
constexpr int pub_at(int R, int s) {
  if (R == 0) { for (int T = 0; T < 6; ++T) if (done_slot(0, st_step(T, 1)) == s) return T + 2; }
  else if (R == 1) { if (s == 17) return 2; if (done_slot(1, 12 * 21 + 10) == s) return 3; }
  else { for (int b = 0; b < 2; ++b) if (done_slot(R, 12 * (11 + 6 * b) + 10) == s) return b + 2; }
  return 0;
}
static_assert(done_slot(0, 12 * 31 + 8) <= 34, "schedule");
static_assert(pub_at(1, 24) == 3 && pub_at(2, 14) == 2 && pub_at(2, 19) == 3, "schedule");
static_assert(done_slot(0, st_step(0, 1)) <= 14 && done_slot(0, st_step(5, 1)) <= 29, "schedule");
constexpr unsigned chk_need(int R, int s) {
  if (R == 0) return (s >= 15 && s <= 30 && (s - 15) % 3 == 0) ? (s - 15) / 3 + 2 : 0;
  if (R == 1) return s == 26 ? 3 : s == 20 ? 2 : s == 22 ? 3 : s == 30 ? 4 : 0;
  return 0;
}
constexpr int u_hlp(int u) { return (u >= 6 && u <= 9) ? (u - 6) >> 1 : u >= 16 ? 2 : -1; }
constexpr int u_mem(int u) { return (u < 6 ? u : u - 10) >> 1; }
constexpr int u_blk(int u) { return u < 6 ? 0 : 1; }
constexpr unsigned F_FIN = 8;

struct Params {
  const u16 *x, *lre, *lim, *bre, *bim, *cre, *cim, *ldt, *dsk, *w1, *b1, *w2;
  char* ws;
  float* out;
};

template <int R>
DI void body(const Params& P, const unsigned lds0, const int c, const int j) {
  constexpr bool SSM = R != 0;
  constexpr int PSTR = R == 2 ? 192 : 64;
  char* const ws = P.ws;
  const int tid = threadIdx.x, lane = tid & 63;
  const int w = __builtin_amdgcn_readfirstlane(tid >> 6);
  const int rg = w & 3, set = w >> 2;
  const int r = lane & 31, h = lane >> 5;
  unsigned stampv = 0; (void)stampv;
  RSTAMP(60); STAMP(0);
  const int gq = SSM ? 4 : j / 3, mi = j - 3 * gq;

  char* const wc = ws + WS_C0 + (size_t)c * WS_CS;
  u32x4 xr[2][9];
  unsigned aux[3], epoch, tag;
  const int ww = (w + (SSM ? 2 * c : c)) & 7;
  const int rb0 = 8 * ((w + (SSM ? 4 * mi : 4 * gq)) & 15), krot = mi;
  const int rb1_ = rb0 ^ 64;
  const char* xb1 = (const char*)P.x + (size_t)(128 * c + rb1_) * (D * 2);
  const unsigned xoff = (lane >> 3) * (D * 2) + (lane & 7) * 16;
  const char* xb0 = (const char*)P.x + (size_t)(128 * c + rb0) * (D * 2);
  unsigned voffx = (lane >> 3) * (D * 2) + (((lane & 7) ^ ((4 * (w & 1) + (lane >> 4)) & 7)) * 16);
  auto dmaX = [&](int third, int i) INL {
    int kb = (i >> 1) + krot; kb = kb >= 3 ? kb - 3 : kb;
    gldsW(((i & 1) ? xb1 : xb0) + (3 * third + kb) * 128, voffx, lds0 + kb * 16384 + ((i & 1) ? rb1_ : rb0) * 128, X_OFF);
  };
  auto early_touch = [&](int half) INL {
    const unsigned l = lane_now();
    unsigned id = w * 16 + (l >> 2); id = id < 108 ? id : 107;
    glds4((const char*)P.w1 + (size_t)(36 * c) * (F * 2) + 768 * j + 384 * half, (id / 3) * (F * 2) + (id % 3) * 128 + (l & 3) * 4, lds0 + JUNK_OFF);
  };
  constexpr int NAUX = 4;
  if constexpr (SSM) {
    __builtin_amdgcn_s_setprio(2);
    static_for<0, 9>([&](auto I) INL { xr[0][I.value] = gload16<I.value * 128>(xb0, xoff); });
    epoch = gload4_sc1(ws + WS_EPOCH, 0u);
    const unsigned o = (R == 2 ? 128 + 192 * (w < 3 ? w : 0) + lane : 192 * mi + (tid < 192 ? tid : 0)) * 2;
    aux[0] = gload2(P.lre, o); aux[1] = gload2(P.lim, o); aux[2] = gload2(P.ldt, o);
  } else {
    __builtin_amdgcn_s_setprio(2);
#pragma unroll
    for (int i = 0; i < 6; ++i) dmaX(0, i);
  }

  unsigned pvo = lane * 16;
  unsigned voff1, voff2;
  const char *wa[2], *wb[3][2], *acm[3];
  if constexpr (SSM) {
    voff1 = voff2 = voffx;
    wa[0] = (const char*)P.bre + (size_t)((R == 2 ? 128 : 192 * mi) + 8 * ww) * (D * 2);
    wa[1] = (const char*)P.bim + (size_t)((R == 2 ? 128 : 192 * mi) + 8 * ww) * (D * 2);
  } else {
    const unsigned swz = ((lane & 7) ^ (((lane >> 4) & 1) << 2)) * 16;
    voff1 = (lane >> 3) * (F * 2) + swz;
    voff2 = (lane >> 3) * (D * 2) + swz;
    wa[0] = wa[1] = (const char*)P.w1 + ((size_t)(8 * ww) * F + 384 * j) * 2;
  }
#pragma unroll
  for (int k = 0; k < 3; ++k) {
    const int mm = (mi + k >= 3) ? mi + k - 3 : mi + k;
    if constexpr (SSM) {
      wb[k][0] = (const char*)P.cre + (size_t)(192 * mi + 8 * ww) * (D * 2) + 384 * mm;
      wb[k][1] = (const char*)P.cim + (size_t)(192 * mi + 8 * ww) * (D * 2) + 384 * mm;
    } else {
      wb[k][0] = wb[k][1] = (const char*)P.w2 + ((size_t)(384 * (3 * gq + mm) + 8 * ww) * D + 192 * mi) * 2;
    }
    acm[k] = wc + C_ACT + ((3 * gq + mm) * 6) * 16384 + w * 1024;
  }
  const unsigned ldw = lds0 + ww * 1024, lda = lds0 + w * 1024;
  auto dmaW = [&](int s, int i) INL {
    const int dimm = (s & 3) * SLOT + i * 8192;
    const int u = s - 18;
    if constexpr (SSM) {
      if (s < 18) gldsW(wa[p1_tile(R, s) & 1] + (PSTR * (p1_tile(R, s) >> 1)) * (D * 2) + (192 * p1_kt(R, s) + 64 * i) * 2, voff1, ldw, dimm);
      else if (u_hlp(u) < 0) gldsW(wb[u_mem(u)][u & 1] + (64 * i) * (D * 2) + 128 * u_blk(u), voff2, ldw, dimm);
      else gldsW(wb[0][u & 1] - 384 * mi + (64 * i) * (D * 2) + 384 * u_hlp(u) + 256, voff2, ldw, dimm);
    } else {
      if (s < 18) gldsW(wa[0] + ((size_t)(192 * p1_kt(R, s) + 64 * i) * F + 64 * p1_tile(R, s)) * 2, voff1, ldw, dimm);
      else gldsW(wb[u % 3][0] + ((size_t)(64 * (u / 3)) * D + 64 * i) * 2, voff2, ldw, dimm);
    }
  };
  auto code_touch = [&](int lines_per_wave_log2, unsigned skip, unsigned dst) INL {
    unsigned long long pc; unsigned len;
    if constexpr (R == 2) asm volatile("s_getpc_b64 %0\n\ts_mov_b32 %1, fk_hlp_end-." : "=s"(pc), "=s"(len));
    else if constexpr (R == 1) asm volatile("s_getpc_b64 %0\n\ts_mov_b32 %1, fk_ssm_end-." : "=s"(pc), "=s"(len));
    else asm volatile("s_getpc_b64 %0\n\ts_mov_b32 %1, fk_mlp_end-." : "=s"(pc), "=s"(len));
    const unsigned co = SSM ? skip + (((unsigned)w << lines_per_wave_log2) + ((unsigned)lane_now() >> (6 - lines_per_wave_log2))) * 128
                            : skip + ((((blockIdx.x >> 3) & ((1u << (lines_per_wave_log2 - 1)) - 1)) * 8 + w) * 2 + ((unsigned)lane_now() >> 5)) * 128;
    glds4((const void*)pc, co < len ? co : 0u, dst);
  };
  const unsigned ximg = (lane >> 3) * 128 + (((lane & 7) ^ ((4 * (w & 1) + (lane >> 4)) & 7)) * 16);
  float rs[2], nb[2], mn[2];
  unsigned xw = lds0 + X_OFF + rb0 * 128 + ximg;
  auto xwrite1 = [&](int ps, int i) INL { lds_wr((ps ? xw ^ 8192 : xw) + (i % 3) * 16384, xr[ps][i]); };
  auto stats = [&](auto PI) INL {
    constexpr int ps = PI.value;
    f32x4 s1 = {}, s2 = {}, s3 = {};
    const unsigned selv = (((lane >> 4) & 3) == (lane & 3)) ? 0x3f803f80u : 0u;
    const u32x4 sel = {selv, selv, selv, selv};
    static_for<0, 9>([&](auto GI) INL {
      constexpr int i = GI.value;
      if constexpr (ps == 0) { xr[1][i] = gload16<i * 128>(xb1, xoff); if constexpr (i == 8) __builtin_amdgcn_s_setprio(0); wait1<9 + NAUX>(xr[0][i]); }
      else wait1<8 - i + 5>(xr[1][i]);
      const u32x2 t0 = {xr[ps][i][0], xr[ps][i][1]}, t1 = {xr[ps][i][2], xr[ps][i][3]};
      s2 = __builtin_amdgcn_mfma_f32_4x4x4bf16_1k(__builtin_bit_cast(s16x4, t0), __builtin_bit_cast(s16x4, t0), s2, 0, 0, 0);
      s3 = __builtin_amdgcn_mfma_f32_4x4x4bf16_1k(__builtin_bit_cast(s16x4, t1), __builtin_bit_cast(s16x4, t1), s3, 0, 0, 0);
      s1 = MFMA16(BF8(sel), BF8(xr[ps][i]), s1);
      SCHED();
    });
    if constexpr (ps == 0) {
      dmaW(0, 0); dmaW(0, 1); dmaW(0, 2);
      unsigned id = rg * 16 + (lane >> 2); id = id < 36 ? id : 35;
      glds4(wa[0] + (set ? wa[1] - wa[0] : 0) - 8 * ww * (D * 2) + c * 36 * 128, id * 128 + (lane & 3) * 4, lds0 + JUNK_OFF);
      code_touch(5, 0, lds0 + JUNK_OFF);
    }
    s2 += s3;
    const float e0 = s2[0], e1 = s2[1], e2 = s2[2], e3 = s2[3];
    const float lo2 = (lane & 1) ? e1 : e0, hi2 = (lane & 1) ? e3 : e2;
    float a2 = (lane & 2) ? hi2 : lo2;
    const float f0 = s1[0], f1 = s1[1], f2 = s1[2], f3 = s1[3];
    const float lo1 = (lane & 16) ? f1 : f0, hi1 = (lane & 16) ? f3 : f2;
    float a1 = (lane & 32) ? hi1 : lo1;
    a1 = dpp_add<0xB1>(a1); a2 = dpp_add<0xB1>(a2);
    a1 = dpp_add<0x4E>(a1); a2 = dpp_add<0x4E>(a2);
    a1 = dpp_add<0x141>(a1); a2 = dpp_add<0x141>(a2);
    const float m = a1 * (1.f / D);
    const float var = fmaxf(a2 * (1.f / D) - m * m, 0.f);
    rs[ps] = __builtin_amdgcn_rsqf(var + 1e-5f);
    nb[ps] = -m * rs[ps];
    mn[ps] = m;
    if constexpr (ps == 1) {
      opaque(epoch);
      tag = __builtin_amdgcn_readfirstlane(epoch) + 1u;
      if constexpr (R == 2) {
        gstore_sc1(wc + C_STAT + rb0 * 16, (lane >> 3) * 16, u32x4{__float_as_uint(mn[0]), tag, __float_as_uint(rs[0]), tag});
        gstore_sc1(wc + C_STAT + rb1_ * 16, (lane >> 3) * 16, u32x4{__float_as_uint(mn[1]), tag, __float_as_uint(rs[1]), tag});
      }
    }
#pragma unroll
    for (int i = 0; i < 3; ++i) { xr[ps][i] = norm16(xr[ps][i], rs[ps], nb[ps]); xwrite1(ps, i); }
  };
  if constexpr (!SSM) {
    dmaW(0, 0); dmaW(0, 1); dmaW(0, 2);
    __builtin_amdgcn_s_setprio(0);
    dmaW(1, 0); dmaW(1, 1); dmaW(1, 2);
    STAMP(1); STAMP(2);
  } else {
    stats(IC<0>{});
    STAMP(1);
    stats(IC<1>{});
    STAMP(2);
    dmaW(1, 0); dmaW(1, 1); dmaW(1, 2);
    opaque(aux[0]); opaque(aux[1]); opaque(aux[2]);
    if (w < 3) {
      const float lr = bflo(aux[0]), li = bflo(aux[1]);
      const float dt = __builtin_amdgcn_exp2f(bflo(aux[2]) * LOG2E);
      const float mag = __builtin_amdgcn_exp2f(dt * lr * LOG2E);
      const float rev = __builtin_amdgcn_fractf(dt * li * 0.15915494309189535f);
      f32x2 L = {mag * __builtin_amdgcn_cosf(rev), mag * __builtin_amdgcn_sinf(rev)};
      const float inv = 2.f / (lr * lr + li * li);
      const float nr = L[0] - 1.f, ni = L[1];
      const f32x2 c2 = {(nr * lr + ni * li) * inv, (ni * lr - nr * li) * inv};
      const f32x2 L16 = csq(csq(csq(csq(L))));
      const f32x2 L128 = csq(csq(csq(L16)));
      lds_wr8(lds0 + LT_OFF + tid * 8, L); lds_wr8(lds0 + LT_OFF + 1536 + tid * 8, c2);
      lds_wr8(lds0 + LT_OFF + 2 * 1536 + tid * 8, L16); lds_wr8(lds0 + LT_OFF + 3 * 1536 + tid * 8, L128);
    }
  }
  u32x2 flg;
  const int rperm = 16 * ((r >> 2) & 1) + (r & 3) + 4 * (r >> 3);
  bf16x8 xn[36];
  unsigned xn_ad[4];
  { const int row = 32 * rg + (SSM ? rperm : r); xn_ad[0] = lds0 + X_OFF + row * 128 + ((h ^ ((row >> 1) & 7)) * 16); }
  xn_ad[1] = xn_ad[0] ^ 32; xn_ad[2] = xn_ad[0] ^ 64; xn_ad[3] = xn_ad[0] ^ 96;
  auto xn_read = [&](int rd, int ks) INL { xn[12 * rd + ks] = BF8(lds_rd(xn_ad[ks & 3] + (ks >> 2) * 16384)); };

  const int G1 = (lane >> 4) & 1, q = (lane >> 2) & 3, p4 = lane & 3;
  unsigned rb1[4], rb2[4][2], ab[2];
  if constexpr (SSM) {
    const int row = 32 * set + r;
    rb1[0] = lds0 + row * 128 + ((h ^ ((row >> 1) & 7)) * 16);
    rb1[1] = rb1[0] ^ 32; rb1[2] = rb1[0] ^ 64; rb1[3] = rb1[0] ^ 96;
    opaque(rb1[0]); opaque(rb1[1]); opaque(rb1[2]); opaque(rb1[3]);
  } else {
    rb1[0] = lds0 + (8 * h + q) * 128 + (((set * 4 + G1 * 2 + (p4 >> 1)) ^ (((q >> 1) & 1) << 2)) * 16) + (p4 & 1) * 8;
    rb1[1] = rb1[0] + 2 * SLOT;
    opaque(rb1[0]); opaque(rb1[1]);
  }
  unsigned tabb = lds0 + TAB_OFF + (32 * set + r) * (SSM ? 8 : 16);
  opaque(tabb);
  unsigned tabh = tabb + h * 2048 + rg * 512;
  if constexpr (SSM) opaque(tabh);
  auto wfrag = [&](int n) INL {
    const int s = n / 12, ks = n % 12;
    if constexpr (SSM) {
      if (s < 18) return BF8(lds_rd(rb1[ks & 3] + (s & 1) * SLOT + (ks >> 2) * 8192));
      else return BF8(lds_rd(rb2[p2_k4(n)][(s >> 1) & 1] + (s & 1) * SLOT + p2_cb(n) * 8192));
    } else {
      if (s < 18) { unsigned a = rb1[(s >> 1) & 1] + (s & 1) * SLOT + ks * 2048; return tr_frag(a, a + 512); }
      else { unsigned a = rb2[0][(s >> 1) & 1] + (s & 1) * SLOT + p2_cb(n) * 8192 + p2_k4(n) * 2048; return tr_frag(a, a + 1024); }
    }
  };
  auto afrag = [&](int kq) INL {
    if constexpr (SSM) {
      const int u = kq >> 2, k4 = kq & 3;
      const int v = (k4 < 2) ? u - 1 : u;
      const unsigned off = (v == 17 ? 3 : (v + 1) % 3) * 16384 + ((k4 < 2) ? 8192 : 0) + (k4 & 1) * 256;
      return tr_frag(ab[0] + off, ab[1] + off);
    } else {
      return BF8(lds_rd(ab[0] + ((kq >> 2) & 3) * 16384 + (kq & 3) * 4096));
    }
  };
  WAIT_L0();
  STAMP(3);
  WAIT_V(SSM ? 5 : 3);
  BAR();
  STAMP(4);

  f32x16 acc[4];
  constexpr int NA = KM + 1;
  f32x16 oacc[3];
  u32x4 actp[2], gr[2];
  float tn, tr, tprev, tprev2;
  f32x2 Lam, Lx, ee, cc, cl, cq, g0, g1, g2;
  const unsigned long long gm0 = (c - 1 - 4 * rg >= 0 ? 0xffffffffull : 0ull) | (c - 3 - 4 * rg >= 0 ? 0xffffffff00000000ull : 0ull);
  const unsigned long long gm1 = (c - 2 - 4 * rg >= 0 ? 0xffffffffull : 0ull) | (c - 4 - 4 * rg >= 0 ? 0xffffffff00000000ull : 0ull);
  bf16x8 fr[4], af[4];
  u32x2 fx[3], fdk[3], gc[2];
  u32x4 fst, corrA, corrB;
  float cs[8], rsl;
  const int w67 = (w + 2) >> 3, cu0 = (w << w67) * 512, cu1 = cu0 + (6 - 5 * w67) * 512;
  float sk[12], xf[12];
  unsigned sto;
  const f32x16 zero = {};
  const char* amine = wc + C_ACT + (j * 6) * 16384 + set * 8192 + rg * (SSM ? 2048 : 1024);
  const char* flagb = wc + C_FLAG + (3 * gq) * 128;
  fr[0] = wfrag(0); fr[1] = wfrag(1);
  if constexpr (!SSM || SPD == 3) fr[2] = wfrag(2);

  const char* pbase = wc + C_PART + (rg * 9 + 3 * mi) * 20480 + gq * 4096 + set * 2048;
  auto pstore = [&](int cb, int hh) INL {
    u32x4 v;
#pragma unroll
    for (int e = 0; e < 4; ++e) v[e] = pk2(oacc[cb][8 * hh + 2 * e], oacc[cb][8 * hh + 2 * e + 1]);
    const unsigned o = pvo ^ (set * 128 + ((pvo >> 3) & 64) + hh * 32);
    if (!SSM && rg == gq) lds_wr(lds0 + ARING + 32768 + cb * 4096 + set * 2048 + hh * 1024 + o, v);
    else gstore_sc1(pbase + cb * 20480 + hh * 1024, o, v);
  };
  const int fpk = (w >> 1) < 3 ? (gq + 1 + (w >> 1)) & 3 : 4;
  const char* ffl = wc + C_FLGW + (3 * fpk + mi) * 128 + (gq + 4 * (w & 1)) * 16;
  bool got = false;
  auto fin_ready = [&]() INL { return __builtin_amdgcn_readfirstlane(flg[1]) == tag && __builtin_amdgcn_readfirstlane(flg[0]) >= F_FIN; };
  auto fin_fetch = [&]() INL {
    const char* src = wc + C_PART + (gq * 9 + 3 * mi) * 20480 + fpk * 4096 + (w & 1) * 2048;
#pragma unroll
    for (int i = 0; i < 6; ++i) gldsA(src + (i >> 1) * 20480 + (i & 1) * 1024, pvo, lds0 + (w >> 1) * 12288 + (w & 1) * 2048, (i >> 1) * 4096 + (i & 1) * 1024);
  };
  auto step = [&](auto NI) INL {
    constexpr int n = NI.value;
    constexpr int s = n / 12, ks = n % 12;
    if constexpr (R != 2 && n == 216 - 4) {
      const int lane = lane_now(), r = lane & 31, h = lane >> 5, G1 = (lane >> 4) & 1, q = (lane >> 2) & 3, p4 = lane & 3;
      if constexpr (SSM) {
        const int rperm = 16 * ((r >> 2) & 1) + (r & 3) + 4 * (r >> 3);
        const int row = 32 * set + rperm;
        rb2[0][0] = lds0 + row * 128 + ((h ^ ((row >> 1) & 7)) * 16);
        rb2[1][0] = rb2[0][0] ^ 32; rb2[2][0] = rb2[0][0] ^ 64; rb2[3][0] = rb2[0][0] ^ 96;
#pragma unroll
        for (int k = 0; k < 4; ++k) { rb2[k][1] = rb2[k][0] + 2 * SLOT; opaque(rb2[k][0]); opaque(rb2[k][1]); }
        const int o = p4 >> 1;
        ab[0] = lds0 + ARING + rg * 2048 + o * 1024 + G1 * 512 + (((2 * h) ^ (2 * G1 + o)) * 64) + q * 16 + (p4 & 1) * 8;
        ab[1] = ab[0] ^ 64;
        opaque(ab[0]); opaque(ab[1]);
      } else {
        const int dch = 4 * (p4 & 1) + 2 * G1 + (p4 >> 1);
        rb2[0][0] = lds0 + (4 * h + q) * 128 + (((set * 4 + (dch >> 1)) ^ (((q >> 1) & 1) << 2)) * 16) + (dch & 1) * 8;
        rb2[0][1] = rb2[0][0] + 2 * SLOT;
        ab[0] = lds0 + ARING + rg * 1024 + lane * 16;
        opaque(rb2[0][0]); opaque(rb2[0][1]); opaque(ab[0]);
      }
    }
    if constexpr (SSM && n == 16 * 12) pvo = lane_now() * 16;
    if constexpr (SSM && ks == 12 - SPD && (s & 1) == 1 && s < (R == 1 ? 11 : 17)) {
#pragma unroll
      for (int k = 0; k < 4; ++k) { rb1[k] += (s & 2) ? -2 * SLOT : 2 * SLOT; opaque(rb1[k]); }
    }
    constexpr int ss = (R == 1 && s >= 16) ? s - 4 : s;
    constexpr int pb = (ss >= 6) ? (ss - 6) / 6 : 0, ph = (ss >= 6 && ss < (R == 1 ? 18 : 24)) ? (ss - 6) % 6 : -1;
    constexpr bool mm_on = R == 0 || (R == 1 ? (n < 144 || n >= 216) : n < 216);
    if constexpr (ks == 0) {
      constexpr int pub = pub_at(R, s);
      constexpr bool poll = chk_need(R, s) != 0;
      if constexpr (poll) { if (w == 0) { const int l = lane_now(); flg = gload8_sc1(flagb, (l & 3) * 128); } }
      if constexpr (pub > 0) { if (w == 1) gstore8_sc1(flagb + mi * 128, 0u, u32x2{(unsigned)pub, tag}); }
      if constexpr (SSM && ph == 3) {
        const unsigned go = 3 * tabb - tabh;
        const char* gb = ws + WS_GRAN + ((R == 2 ? pb * 3 + 2 : mi * 3 + pb) * 16 + c - 1 - 4 * rg) * 1024 + rg * 512 - 2 * (lds0 + TAB_OFF);
        gr[0] = gload16_sc1(gb, go);
        gr[1] = gload16_sc1(gb - 1024, go);
      }
    }
    if constexpr (s < 3 * km(R)) {
      constexpr int kt = s / km(R), v = n - 12 * km(R) * kt;
      if constexpr (v == 0) { xn_read(kt, 0); xn_read(kt, 1); xn_read(kt, 2); xn_read(kt, 3); }
      if constexpr (SSM) {
        if constexpr (v >= 1 && v + 3 < 12) xn_read(kt, v + 3);
        constexpr int ch = v == 21 ? 5 : (v >= 12 && v <= 20 && v % 2 == 0) ? (v - 12) / 2 : -1;
        if constexpr (kt < 2 && ch >= 0) { xwrite1(ch / 3, 3 * (kt + 1) + ch % 3); SCHED(); }
      } else if constexpr (v >= 1 && v <= 4) { xn_read(kt, 2 * v + 2); xn_read(kt, 2 * v + 3); }
    }
    if constexpr (SSM && ks == 4 && (s == 8 || s == 11 || (R == 2 && (s == 14 || s == 17)))) {
      const int l = lane_now(), r = l & 31;
      const int row = 32 * rg + 16 * ((r >> 2) & 1) + (r & 3) + 4 * (r >> 3);
      const unsigned a = lds0 + X_OFF + 32768 + row * 128 + (((l >> 5) ^ ((row >> 1) & 7)) * 16);
      xn[32] = BF8(lds_rd(a)); xn[33] = BF8(lds_rd(a ^ 32)); xn[34] = BF8(lds_rd(a ^ 64)); xn[35] = BF8(lds_rd(a ^ 96));
    }
    if constexpr (op_w(R, n)) { if constexpr (s == 0) dmaW(2 + ks / 3, ks % 3); else dmaW(w_slot(R, s), ks / 3); }
    if constexpr (op_x(R, n)) dmaX(n / 36 + 1, x_piece(n));
    if constexpr (op_a(R, n)) {
      if constexpr (SSM) {
        constexpr int pc = a_code(n) & 1, v = (a_code(n) - pc) / 2, u = v + pc;
        gldsA((u_hlp(u) < 0 ? acm[u_hlp(u) < 0 ? u_mem(u) : 0] + (2 * u_blk(u) + (u & 1)) * 16384 : wc + C_ACT + (15 * 6 + 2 * u_hlp(u) + (u & 1)) * 16384 + w * 1024) + (pc ? 0 : 8192), pvo,
              lda, ARING + (v == 17 ? 3 : (v + 1) % 3) * 16384 + pc * 8192);
      } else {
        constexpr int u = s + 3 - 18;
        gldsA(acm[u % 3] + (u / 3) * 16384 + (ks / 3) * 8192, pvo, lda, ARING + (u & 3) * 16384 + (ks / 3) * 8192);
      }
    }
    if constexpr (op_e(R, n)) { if constexpr (n == CT0) code_touch(5, 0, lds0 + JUNK_OFF); else early_touch(n != ET1); }
    if constexpr (op_g(R, n)) {
      if constexpr (n == 79) fst = gload16_sc1(wc + C_STAT, (32 * rg) * 16 + (pvo & 496));
      else gc[n - 80] = gload8_sc1(ws + WS_CSUM + j * 8192 + (n == 80 ? cu0 : cu1), pvo >> 1);
    }
    if constexpr (op_cs(R, n)) {
      constexpr int i = cs_piece(n);
      const char* cb_ = (const char*)P.w1 + (size_t)(288 * ((c / 6) & 1) + 36 * w + 8 * i) * (F * 2) + 768 * j + (c % 6) * 128;
      if constexpr (i < 5) xr[0][i] = gload16<0>(cb_, ((pvo >> 7) & (i < 4 ? 7 : 3)) * (F * 2) + (pvo & 112));
      else if constexpr (i == 5) epoch = gload4_sc1(ws + WS_EPOCH, 0u);
      else aux[0] = gload2((const char*)P.b1 + 768 * j + (w < 6 ? w : 0) * 128, pvo >> 3);
    }
    if constexpr (op_cp(R, n)) code_touch(3, 3072, ldw + ((s + 3) & 3) * SLOT + 2 * 8192);
    if constexpr (op_pf(R, n)) {
      const unsigned l = lane_now();
      constexpr int sl = SSM ? (s < 3 ? s : s - 3) : (s < 6 ? s - 5 : s - 6);
      if constexpr (SSM) {
        constexpr unsigned lim = s < 3 ? 72 : 108;
        unsigned id = sl * 32 + rg * 8 + (l >> 3); id = id < lim ? id : lim - 1;
        const u16* mat = s < 3 ? (set ? P.bim : P.bre) : (set ? P.cim : P.cre);
        glds4((const char*)mat + (size_t)(192 * mi + (s < 3 ? 64 : 0)) * (D * 2) + c * lim * 128, id * 128 + (l & 7) * 4, lds0 + JUNK_OFF);
      } else if constexpr (s < 3) {
        unsigned id = sl * 64 + w * 8 + (l >> 3); id = id < 144 ? id : 143;
        glds4((const char*)P.w1 + (size_t)(36 * c) * (F * 2) + 768 * j + 256, (id >> 2) * (F * 2) + (id & 3) * 128 + (l & 7) * 4, lds0 + JUNK_OFF);
      } else {
        unsigned id = sl * 64 + w * 8 + (l >> 3); id = id < 216 ? id : 215;
        glds4((const char*)P.w2 + (size_t)(384 * j) * (D * 2) + c * 216 * 128, id * 128 + (l & 7) * 4, lds0 + JUNK_OFF);
      }
    }
    if constexpr (SSM && n + SPD < (R == 1 ? 144 : 216)) fr[(n + SPD) & 3] = wfrag(n + SPD);
    if constexpr ((R == 0 || (R == 1 && n >= 213)) && n + 3 < 432) fr[(n + 3) & 3] = wfrag(n + 3);
    if constexpr (R == 2) {
    } else if constexpr (SSM) {
      constexpr int u = s - 18;
      if constexpr (s >= 18 && s < 35 && (ks == 3 || ks == 6)) af[ks / 3 + 1] = afrag(4 * u + ks / 3 + 1);
      if constexpr (s >= 17 && s < 35 && (ks == 9 || ks == 10)) af[ks - 9] = afrag(4 * (u + 1) + ks - 9);
      if constexpr (n == 419 || n == 420) af[n - 417] = afrag(68 + n - 417);
    } else {
      if constexpr (n + 3 >= 216 && n + 3 <= 420 && (n + 3) % 3 == 0) af[((n + 3 - 216) / 3) & 3] = afrag((n + 3 - 216) / 3);
      if constexpr (n >= 418 && n <= 420) af[n - 417] = afrag(68 + n - 417);
      if constexpr (op_fo(R, n)) {
        constexpr int i = s - 25;
        const unsigned rw = w * 4 + (pvo >> 8), co = (pvo >> 1) & 120;
        if constexpr (i < 3) fx[i] = gload8((const char*)P.x + (size_t)(128 * c + 32 * gq) * (D * 2) + 384 * mi + i * 128, rw * (D * 2) + co);
        else if constexpr (i < 6) fdk[i - 3] = gload8((const char*)P.dsk + 384 * mi + (i - 3) * 128, co);
        else fst = gload16_sc1(wc + C_STAT, (32 * gq + rw) * 16);
      }
      if constexpr (n == 408) { pin2(fx[0]); pin2(fx[1]); pin2(fx[2]); pin2(fdk[0]); pin2(fdk[1]); pin2(fdk[2]); wait1<63>(fst); flg = gload8_sc1(ffl, 0u); rsl = __uint_as_float(fst[2]) * LOG2E; tn = __uint_as_float(fst[2]) * (1.f / LOG2E); opaquef(rsl); opaquef(tn); }
      if constexpr (n >= 408 && n < 420) {
        constexpr int e = n - 408;
        const unsigned xv = fx[e >> 2][(e >> 1) & 1], dv = fdk[e >> 2][(e >> 1) & 1];
        xf[e] = (e & 1) ? bfhi(xv) : bflo(xv);
        sk[e] = (xf[e] - __uint_as_float(fst[0])) * tn * ((e & 1) ? bfhi(dv) : bflo(dv));
        opaquef(xf[e]); opaquef(sk[e]);
      }
      if constexpr (n == 422) {
        SCHED();
        wait0_2(flg);
        got = fin_ready();
        if (got) fin_fetch();
      }
    }
    if constexpr (!mm_on) {
    } else if constexpr (s < 18) {
      constexpr int T = p1_tile(R, s), kt = p1_kt(R, s);
      if constexpr (SSM) { acc[T & 3] = MFMA32(xn[12 * kt + ks], fr[n & 3], (kt == 0 && ks == 0) ? zero : acc[T & 3]); opaque16(acc[T & 3]); }
      else { acc[T % NA] = MFMA32(fr[n & 3], xn[12 * kt + ks], (kt == 0 && ks == 0) ? zero : acc[T % NA]); opaque16(acc[T % NA]); }
      if constexpr (SSM && s < 2 * km(R)) {
        constexpr int v = n - 12 * km(R) * kt, q = km(R) == 3 ? 4 : 6;
        constexpr int d0 = (q * v + 4) / 5, d1 = (q * (v + 1) + 4) / 5;
        if constexpr (d0 < d1 && d0 < 24) { constexpr int ch = d0 >> 2; xr[ch / 3][3 * (kt + 1) + ch % 3][d0 & 3] = norm4(xr[ch / 3][3 * (kt + 1) + ch % 3][d0 & 3], rs[ch / 3], nb[ch / 3]); }
        if constexpr (d0 + 1 < d1 && d0 + 1 < 24) { constexpr int d = d0 + 1, ch = d >> 2; xr[ch / 3][3 * (kt + 1) + ch % 3][d & 3] = norm4(xr[ch / 3][3 * (kt + 1) + ch % 3][d & 3], rs[ch / 3], nb[ch / 3]); }
      }
    } else {
      oacc[p2_cb(n)] = MFMA32(fr[n & 3], af[p2_k4(n)], n < 219 ? zero : oacc[p2_cb(n)]);
      opaque16(oacc[p2_cb(n)]);
      if constexpr (n == 425 || n == 426 || n == 429 || n == 430) pstore((n - 425) / 4, (n - 425) & 1);
    }
    if constexpr (!SSM) {
      if constexpr (n >= 36 && n <= 53) {
        if constexpr (n == 36) { SCHED(); wait2<ops_between(0, 26, 36)>(xr[0][4], xr[0][0]); }
        if constexpr (n == 42) { SCHED(); wait1<ops_between(0, 29, 42)>(xr[0][1]); }
        if constexpr (n == 46) { SCHED(); wait1<ops_between(0, 32, 46)>(xr[0][2]); }
        if constexpr (n == 50) { SCHED(); wait1<ops_between(0, 34, 50)>(xr[0][3]); }
        if constexpr (n < 38) {
#pragma unroll
          for (int d = 2 * (n - 36); d < 2 * (n - 35); ++d) { const unsigned v = msel_u(xr[0][4][d], 0xffffffffull); cs[2 * d] = bflo(v); cs[2 * d + 1] = bfhi(v); opaquef(cs[2 * d]); opaquef(cs[2 * d + 1]); }
        } else {
          constexpr int d = n - 38;
          const unsigned v = xr[0][d >> 2][d & 3]; cs[2 * (d & 3)] += bflo(v); opaquef(cs[2 * (d & 3)]); cs[2 * (d & 3) + 1] += bfhi(v); opaquef(cs[2 * (d & 3) + 1]);
        }
      }
      if constexpr (n == 54) {
#pragma unroll
        for (int e = 0; e < 4; ++e) { auto t = __builtin_amdgcn_permlane32_swap(__float_as_uint(cs[e]), __float_as_uint(cs[e + 4]), false, false); cs[e] = __uint_as_float(t[0]) + __uint_as_float(t[1]); opaquef(cs[e]); }
      }
      if constexpr (n == 55) {
#pragma unroll
        for (int e = 0; e < 2; ++e) { auto t = __builtin_amdgcn_permlane16_swap(__float_as_uint(cs[e]), __float_as_uint(cs[e + 2]), false, false); cs[e] = dpp_add<0x128>(__uint_as_float(t[0]) + __uint_as_float(t[1])); opaquef(cs[e]); }
        const unsigned l = pvo >> 4;
        *(LAS float*)(lds0 + TAB_OFF + 8192 + w * 256 + (8 * (l & 7) + (l >> 3)) * 4) = bsel(cs[0], cs[1], 0xff00ff00ff00ff00ull);
      }
      if constexpr (n == 60) {
        opaque(epoch); opaque(aux[0]); tag = __builtin_amdgcn_readfirstlane(epoch) + 1u;
        if (w == 2) {
          float t = 0.f;
#pragma unroll
          for (int k = 0; k < 8; ++k) t += *(LAS float*)(lds0 + TAB_OFF + 8192 + k * 256 + (pvo >> 2));
          gstore8_sc1(ws + WS_CSUM + j * 8192 + c * 512, pvo >> 1, u32x2{__float_as_uint(t), tag});
        }
      }
      if constexpr (n == 91) {
        SCHED();
        waitg<ops_between(0, 82, 91)>(fst, gc[0], gc[1]);
        auto stale = [&]() INL { return __builtin_amdgcn_ballot_w64(((fst[1] ^ tag) | (fst[3] ^ tag) | (gc[0][1] ^ tag) | (gc[1][1] ^ tag)) != 0u) != 0ull; };
        if (__builtin_expect(stale(), 0)) {
          const char* gb = ws + WS_CSUM + j * 8192;
          int spins = 200000;
          do { reloadg(fst, gc[0], gc[1], wc + C_STAT + (32 * rg) * 16, gb + cu0, gb + cu1, pvo & 496, pvo >> 1); } while (stale() && --spins > 0);
        }
        const float Sv = __uint_as_float(gc[0][0]) + __uint_as_float(gc[1][0]);
        const unsigned Sh = pk2(Sv, Sv), Sl = pk2(Sv - bfhi(Sh), 0.f);
        lds_wr(lds0 + TAB_OFF + w * 1024 + pvo, u32x4{Sh, (Sl & 0xffffu) | (aux[0] << 16), aux[0], 0u});
      }
      if constexpr (n == 93) { tn = __builtin_amdgcn_rcpf(__uint_as_float(fst[2])); rsl = __uint_as_float(fst[2]) * LOG2E; opaquef(tn); opaquef(rsl); }
      if constexpr (n == 94) { const float mu = -__uint_as_float(fst[0]); gc[0][0] = pk2(mu, tn); gc[0][1] = pk2(mu - bflo(gc[0][0]), tn - bfhi(gc[0][0])); pin2(gc[0]); }
      if constexpr (n == 95) {
        const unsigned hi = gc[0][0], lo = gc[0][1];
        corrB = u32x4{msel_u((hi & 0xffffu) | (lo << 16), 0xffffffffull), msel_u(hi, 0xffffffffull), msel_u(lo >> 16, 0xffffffffull), msel_u(lo, 0ull)};
        wait1<63>(corrB);
      }
      if constexpr (corr_tile(n + (n == 96 ? 3 : 6)) >= (n == 96 ? 0 : 1)) corrA = lds_rd(tabb + corr_tile(n + (n == 96 ? 3 : 6)) * 1024);
      if constexpr (corr_tile(n) >= 0) { acc[corr_tile(n) % NA] = MFMA32(BF8(corrA), BF8(corrB), acc[corr_tile(n) % NA]); opaque16(acc[corr_tile(n) % NA]); }
      if constexpr (n >= EPI0 && n < EPI0 + 6 * EP) {
        constexpr int T = (n - EPI0) / EP, u = (n - EPI0) % EP, e = u >> 1;
        const f32x16& pa = acc[T % NA];
        if constexpr ((u & 1) == 0) {
          if constexpr (e < 16) { tn = __builtin_amdgcn_exp2f(pa[e] * rsl); opaquef(tn); }
          if constexpr (e >= 1) {
            const float a = __builtin_fmaf(-pa[e - 1], tr, pa[e - 1]);
            if constexpr (((e - 1) & 1) == 0) { tprev = a; opaquef(tprev); }
            else { unsigned pk = pk2(tprev, a); opaque(pk); actp[(e - 1) >> 3][((e - 1) >> 1) & 3] = pk; }
          }
        } else { tr = __builtin_amdgcn_rcpf(__builtin_fmaf(tn, __builtin_fmaf(tn, 0.5f, 1.f), 1.f)); opaquef(tr); }
      }
      if constexpr (op_st(0, n)) { constexpr int id = st_id(n); gstore_sc1(amine + (id >> 1) * 16384 + (id & 1) * 4096, pvo, actp[id & 1]); }
    } else if constexpr (ph >= 0) {
      f32x16& ur = acc[(2 * pb) & 3];
      f32x16& ui = acc[(2 * pb + 1) & 3];
      const unsigned lt = tabb + pb * 512;
      const unsigned wt = tabb + (WT_OFF - TAB_OFF) + 2048, gt = tabb + (GT_OFF - TAB_OFF);
      if constexpr (ph == 0) {
        if constexpr (ks == 0) { ee = cfma(Lam, f32x2{ur[0], ui[0]}, f32x2{ur[1], ui[1]}); opaquec(ee); }
        if constexpr (ks >= 1 && ks <= 9) {
          constexpr int t0 = 2 + ((ks - 1) * 14) / 9, t1 = 2 + (ks * 14) / 9;
          ee = cfma(Lam, ee, f32x2{ur[t0], ui[t0]});
          if constexpr (t0 + 1 < t1) ee = cfma(Lam, ee, f32x2{ur[t0 + 1], ui[t0 + 1]});
          opaquec(ee);
        }
        if constexpr (ks == 7) Lx = lds_rd8(lt + 2 * 1536);
        if constexpr (ks == 10) {
          const f32x2 elo = {up32(ee[0]), up32(ee[1])};
          lds_wr8(tabh + (WT_OFF - TAB_OFF), cfma_s(Lx, elo, ee));
        }
        if constexpr (ks == 11) { Lam = csq(Lx); opaquec(Lam); }
      }
      if constexpr (ph == 1) {
        if constexpr (ks == 0) { g0 = lds_rd8(wt); g1 = lds_rd8(wt + 512); g2 = lds_rd8(wt + 1024); ee = lds_rd8(wt + 1536); }
        if constexpr (ks == 2) {
          cl = f32x2{0.f, 0.f}; cq = f32x2{1.f, 0.f};
          if (rg > 0) { cl = g0; cq = Lam; }
          if (rg > 1) { cl = cfma_s(Lam, cl, g1); cq = cmul_s(Lam, Lam); }
          if (rg > 2) {
            cl = cfma_s(Lam, cl, g2); cq = cmul_s(Lam, cq);
            const f32x2 E = cfma_s(Lam, cl, ee);
            gstore_sc1(ws + WS_GRAN + ((R == 2 ? pb * 3 + 2 : mi * 3 + pb) * 16 + c) * 1024 - 2 * (lds0 + TAB_OFF), 2 * tabb,
                       u32x4{__float_as_uint(E[0]), tag, __float_as_uint(E[1]), tag});
          }
          opaquec(cl); opaquec(cq);
          g0 = lds_rd8(wt - 2048 + rg * 512); cc = lds_rd8(lt + 1536);
        }
        if constexpr (ks == 4) {
          const f32x2 a = cfma_s(Lx, cl, g0);
          cl = f32x2{usel(cl[0], a[0]), usel(cl[1], a[1])};
          opaquec(cl);
        }
        if constexpr (ks == 5) { const f32x2 a = cmul_s(Lx, cq); cq = f32x2{usel(cq[0], a[0]), usel(cq[1], a[1])}; opaquec(cq); }
        if constexpr (ks == 6) { cl = cmul_s(cc, cl); opaquec(cl); }
        if constexpr (ks == 7) { cq = cmul_s(cc, cq); opaquec(cq); }
      }
      if constexpr ((ph == 1 && ks >= 8) || ph == 2) {
        constexpr int t = (ph == 1) ? ks - 8 : ks + 4;
        const float t1 = cc[1] * ui[t], t2 = cc[1] * ur[t];
        float a = __builtin_fmaf(cc[0], ur[t], -t1), b = __builtin_fmaf(-cc[0], ui[t], -t2);
        opaquef(a); opaquef(b);
        ur[t] = a; ui[t] = b;
      }
      if constexpr (ph == 3) {
        if constexpr (ks == 4) cc = lds_rd8(lt + 3 * 1536);
        if constexpr (ks == 7) { Lx = csq(cc); opaquec(Lx); }
        if constexpr (ks == 9) {
          SCHED();
          wait2<ops_between(R, 12 * s, n)>(gr[0], gr[1]);
          auto stale = [&]() INL {
            const unsigned b0 = (gr[0][1] ^ tag) | (gr[0][3] ^ tag), b1 = (gr[1][1] ^ tag) | (gr[1][3] ^ tag);
            return ((__builtin_amdgcn_ballot_w64(b0 != 0u) & gm0) | (__builtin_amdgcn_ballot_w64(b1 != 0u) & gm1)) != 0ull;
          };
          if (__builtin_expect(stale(), 0)) {
            const unsigned go = 3 * tabb - tabh;
            const char* gb = ws + WS_GRAN + ((R == 2 ? pb * 3 + 2 : mi * 3 + pb) * 16 + c - 1 - 4 * rg) * 1024 + rg * 512 - 2 * (lds0 + TAB_OFF);
            int spins = 200000;
            do { reload2(gr[0], gr[1], gb, gb - 1024, go); } while (stale() && --spins > 0);
          }
          const f32x2 a0 = {msel(gr[0][0], gm0), msel(gr[0][2], gm0)}, a1 = {msel(gr[1][0], gm1), msel(gr[1][2], gm1)};
          ee = cfma_s(cc, a1, a0); opaquec(ee);
        }
        if constexpr (ks == 10) {
          const f32x2 lo = {up32(ee[0]), up32(ee[1])};
          lds_wr8(tabh + (GT_OFF - TAB_OFF), cfma_s(Lx, ee, lo));
        }
        if constexpr (ks == 11) { cc = csq(Lx); opaquec(cc); }
      }
      if constexpr (ph == 4 && ks <= 3) {
        if constexpr (ks == 0) { ee = lds_rd8(gt + 2048 + 1536); g0 = lds_rd8(gt + 2048 + 1024); g1 = lds_rd8(gt + 2048 + 512); g2 = lds_rd8(gt + 2048); Lam = lds_rd8(lt); }
        if constexpr (ks == 2) { ee = cfma(cc, cfma(cc, ee, g0), g1); opaquec(ee); }
        if constexpr (ks == 3) { ee = cfma(cq, cfma(cc, ee, g2), cl); ee[1] = -ee[1]; opaquec(ee); }
      }
      if constexpr ((ph == 4 && ks >= 4) || (ph == 5 && ks <= 7)) {
        constexpr int tt = (ph == 4) ? ks - 4 : ks + 8;
        ee = cfma(f32x2{Lam[0], -Lam[1]}, ee, f32x2{ur[tt], ui[tt]});
        opaquec(ee);
        if constexpr ((tt & 1) == 0) { tprev = ee[0]; tprev2 = ee[1]; }
        else {
          unsigned p0 = pk2(tprev, ee[0]), p1 = pk2(tprev2, ee[1]); opaque(p0); opaque(p1);
          if constexpr (tt < 8) { gr[0][tt >> 1] = p0; gr[1][tt >> 1] = p1; }
          else { actp[0][(tt >> 1) & 3] = p0; actp[1][(tt >> 1) & 3] = p1; }
        }
      }
      if constexpr (op_st(R, n)) {
        constexpr int o = (ks - 7) >> 1, part = (ks - 7) & 1;
        if constexpr (ks == 7) { const int l = lane_now(); sto = (l ^ (8 * (l >> 5))) * 16; }
        if constexpr (ks == 9) sto ^= 64;
        gstore_sc1(amine + (2 * pb + part) * 16384 + o * 1024, sto, o ? actp[part] : gr[part]);
      }
    }
    if constexpr (SSM && ks == 9 && (s == 5 || s == 11 || (R == 2 && s == 17))) Lam = lds_rd8(tabb + ((s - 5) / 6) * 512);
    if constexpr (n == 8) { SCHED(); WAIT_V(ops_between(R, 0, 8)); BAR(); }
    if constexpr (R == 1 && n == 212) {
      SCHED(); WAIT_V(0); BAR();
      if (w == 0) { const int l = lane_now(); flg = gload8_sc1(flagb, (l & 3) * 128); }
    }
    if constexpr (R == 1 && n == 221) {
      SCHED();
      if (w == 0) {
        wait0_2(flg);
        const int l = lane_now();
        int spins = 200000;
        while ((__builtin_amdgcn_ballot_w64(flg[1] != tag || flg[0] < 2u) & 7ull) != 0ull && --spins > 0) { flg = gload8_sc1(flagb, (l & 3) * 128); wait0_2(flg); }
      }
      BAR();
    }
    if constexpr (R == 1 && n == 236) { SCHED(); WAIT_V(vm_n(1, 222, 236)); BAR(); }
    if constexpr (!SSM && n == 44) { SCHED(); BAR(); }
    if constexpr (ks == 11 && s < 34) {
      SCHED();
      if constexpr (SSM && (s == 1 || s == 3)) WAIT_L0();
      if constexpr (SSM && (ph == 0 || ph == 3 || s >= 17)) WAIT_L0();
      WAIT_V(drain(R, s) ? 0 : (R == 1 && s == 16) ? ops_between(1, 192, n) : vm_n(R, s == 0 ? 2 : (R == 0 && (s == 1 || s == 4)) ? 12 * s - 4 : 12 * s - 1, n));
      if constexpr (chk_need(R, s) != 0) {
        if (w == 0) {
          constexpr unsigned need = chk_need(R, s);
          constexpr unsigned long long lanes = (R == 1 && s != 26) ? 8ull : 7ull;
          WAIT_V(ops_between(R, 12 * s, n));
          pin2(flg);
          auto late = [&]() INL { return (__builtin_amdgcn_ballot_w64(flg[1] != tag || flg[0] < need) & lanes) != 0ull; };
          if (__builtin_expect(late(), 0)) {
            const int l = lane_now();
            int spins = 200000;
            do { flg = gload8_sc1(flagb, (l & 3) * 128); wait0_2(flg); } while (late() && --spins > 0);
          }
        }
      }
      BAR();
      STAMP(5 + s);
    }
    SCHED();
  };
  run_seq<0>(step, __make_integer_seq<ISeq, int, 144>{});
  if constexpr (R != 1) run_seq<144>(step, __make_integer_seq<ISeq, int, 48>{});
  else { STAMP(17); STAMP(18); STAMP(19); STAMP(20); }
  run_seq<192>(step, __make_integer_seq<ISeq, int, 24>{});
  run_seq<216>(step, __make_integer_seq<ISeq, int, R == 2 ? 71 : 216>{});
  if constexpr (R != 2) { pstore(2, 0); pstore(2, 1); }
  STAMP(39);
  WAIT_V(0);
  if constexpr (R == 2) { BAR(); if (w == 1) gstore8_sc1(flagb + mi * 128, 0u, u32x2{F_FIN, tag}); }
  else gstore8_sc1(wc + C_FLGW + j * 128 + w * 16, 0u, u32x2{F_FIN, tag});
  STAMP(40);
  if constexpr (!SSM) {
    const int l = lane_now(), t = w * 64 + l;
    const int c4 = t & 15, rho = c4 >> 1;
    const unsigned ra = lds0 + (c4 >> 3) * 2048 + (rho & 1) * 1024 + ((c4 >> 2) & 1) * 512 + (((t >> 4) ^ (2 * rho)) * 16) + (c4 & 1) * 8;
    const char* ob = (const char*)(P.out + (size_t)(128 * c + 32 * gq) * D + 192 * mi);
    const unsigned oo = (t >> 4) * (D * 4) + c4 * 16;
    if (!got) {
      int spins = 400000;
      do { flg = gload8_sc1(ffl, 0u); wait0_2(flg); } while (!fin_ready() && --spins > 0);
      fin_fetch();
    }
    STAMP(41);
    STAMP(42);
    WAIT_L0();
    WAIT_V(0);
    STAMP(43);
    BAR();
    STAMP(44);
#pragma unroll
    for (int cb = 0; cb < 3; ++cb) {
      u32x2 pv[4];
#pragma unroll
      for (int k = 0; k < 4; ++k) pv[k] = *(LAS u32x2*)(ra + k * 12288 + cb * 4096);
      const u32x2 ov = *(LAS u32x2*)(ra + ARING + 32768 + cb * 4096);
      f32x4 o;
#pragma unroll
      for (int e = 0; e < 4; ++e) {
        auto up = [&](unsigned v) INL { return (e & 1) ? bfhi(v) : bflo(v); };
        const float z = ((up(ov[e >> 1]) + up(pv[0][e >> 1])) + (up(pv[1][e >> 1]) + up(pv[2][e >> 1]))) * rsl;
        const float gt = z * __builtin_amdgcn_rcpf(1.f + __builtin_amdgcn_exp2f(-z));
        o[e] = __builtin_fmaf(__builtin_fmaf(up(pv[3][e >> 1]), 0.693147180559945f, sk[4 * cb + e]), gt, xf[4 * cb + e]);
      }
      gstore_sc1(ob + 256 * cb, oo, __builtin_bit_cast(u32x4, o));
    }
    if (c == 15 && j == 0 && t == 0) *(unsigned*)(ws + WS_EPOCH) = tag;
  }
  if constexpr (R == 2) asm volatile("fk_hlp_end:"); else if constexpr (R == 1) asm volatile("fk_ssm_end:"); else asm volatile("fk_mlp_end:");
  if constexpr (SSM) { STAMP(41); STAMP(42); STAMP(43); STAMP(44); }
  STAMP(45);
  STAMP(46);
  RSTAMP(61);
  STAMP_OUT();
}

#if defined(__HIP_DEVICE_COMPILE__)
#define NOPK __attribute__((target("no-packed-fp32-ops")))
#else
#define NOPK
#endif
__global__ NOPK __launch_bounds__(512, 2) void main_kernel(const Params P) {
  __shared__ __attribute__((aligned(16))) char smem[163840];
  const unsigned lds0 = (unsigned)(size_t)(LAS char*)smem;
  const int b = blockIdx.x, xcd = b & 7, idx = b >> 3;
#define MAP 2
  int j, c;
  if (MAP == 0) { j = idx >> 1; c = 2 * xcd + (idx & 1); }
  else if (idx >= 24) { j = 12 + ((idx - 24) >> 1); c = 2 * xcd + (idx & 1); }
  else if (MAP == 1) { j = 6 * (xcd & 1) + (idx >> 2); c = 4 * (xcd >> 1) + (idx & 3); }
  else { j = 3 * (xcd & 3) + (idx >> 3); c = 8 * (xcd >> 2) + (idx & 7); }
#if defined(__HIP_DEVICE_COMPILE__)
  if (j < 12) body<0>(P, lds0, c, j);
  else if (j == 15) body<2>(P, lds0, c, j);
  else body<1>(P, lds0, c, j);
#endif
}

extern "C" void kernel_launch(void* const* d_in, const int* in_sizes, int n_in, void* d_out, int out_size, void* d_ws, size_t ws_size,
                              hipStream_t stream) {
  Params P;
  P.x = (const u16*)d_in[0]; P.lre = (const u16*)d_in[1]; P.lim = (const u16*)d_in[2];
  P.bre = (const u16*)d_in[3]; P.bim = (const u16*)d_in[4]; P.cre = (const u16*)d_in[5]; P.cim = (const u16*)d_in[6];
  P.ldt = (const u16*)d_in[7]; P.w1 = (const u16*)d_in[10]; P.b1 = (const u16*)d_in[11]; P.w2 = (const u16*)d_in[12];
  P.dsk = (const u16*)d_in[8]; P.ws = (char*)d_ws; P.out = (float*)d_out;
  main_kernel<<<256, 512, 0, stream>>>(P);
}
```

```cpp
#include <hip/hip_runtime.h>
#include <hip/hip_bf16.h>
#pragma clang diagnostic ignored "-Wint-to-pointer-cast"

typedef unsigned short u16;
typedef __attribute__((ext_vector_type(2))) __bf16 bf16x2;
typedef __attribute__((ext_vector_type(4))) __bf16 bf16x4;
typedef __attribute__((ext_vector_type(8))) __bf16 bf16x8;
typedef __attribute__((ext_vector_type(4))) short s16x4;
typedef __attribute__((ext_vector_type(2))) float f32x2;
typedef __attribute__((ext_vector_type(4))) float f32x4;
typedef __attribute__((ext_vector_type(16))) float f32x16;
typedef __attribute__((ext_vector_type(2))) unsigned u32x2;
typedef __attribute__((ext_vector_type(4))) unsigned u32x4;
#define LAS __attribute__((address_space(3)))
#define GAS __attribute__((address_space(1)))
#define DI __device__ __forceinline__
#define INL __attribute__((always_inline))

constexpr int S = 2048, D = 576, F = 4608;
constexpr float LOG2E = 1.44269504088896f;
constexpr int SLOT = 24576;
constexpr int X_OFF = 4 * SLOT;
constexpr int ARING = X_OFF;
constexpr int TAB_OFF = ARING + 49152;
constexpr int LT_OFF = TAB_OFF;
constexpr int WT_OFF = TAB_OFF + 6144;
constexpr int JUNK_OFF = TAB_OFF + 14336;
constexpr int GT_OFF = TAB_OFF + 10240;
constexpr size_t WS_EPOCH = 0;
constexpr size_t WS_GRAN = 32768;
constexpr size_t WS_CSUM = 196608;
constexpr size_t WS_C0 = 327680, WS_CS = 2334720;
constexpr int C_FLAG = 0;
constexpr int C_FLGW = 8192;
constexpr int C_STAT = 4096;
constexpr int C_ACT = 24576;
constexpr int C_PART = C_ACT + 1572864;

DI float bflo(unsigned v) { return __uint_as_float(v << 16); }
DI float bfhi(unsigned v) { return __uint_as_float(v & 0xffff0000u); }
DI unsigned pk2(float a, float b) { f32x2 v = {a, b}; return __builtin_bit_cast(unsigned, __builtin_convertvector(v, bf16x2)); }
#define WAIT_V(n) asm volatile("s_waitcnt vmcnt(%0)" ::"n"(n) : "memory")
#define WAIT_L0() asm volatile("s_waitcnt lgkmcnt(0)" ::: "memory")
#define BAR() asm volatile("s_barrier" ::: "memory")
#define SCHED() __builtin_amdgcn_sched_barrier(0)
#define BF8(x) __builtin_bit_cast(bf16x8, (x))
#define MFMA16(a, b, c) __builtin_amdgcn_mfma_f32_16x16x32_bf16((a), (b), (c), 0, 0, 0)
#define MFMA32(a, b, c) __builtin_amdgcn_mfma_f32_32x32x16_bf16((a), (b), (c), 0, 0, 0)
template <int I> struct IC { static constexpr int value = I; };
template <int B, int E, class Fn> DI void static_for(Fn&& f) { if constexpr (B < E) { f(IC<B>{}); static_for<B + 1, E>(f); } }

template <class T, T... I> struct ISeq {};
template <int B, class Fn, int... I> DI void run_seq(Fn&& f, ISeq<int, I...>) { (f(IC<B + I>{}), ...); }

DI void gldsW(const void* sbase, unsigned voff, unsigned lds_base, int imm) {
  asm volatile("s_add_u32 m0, %2, %3\n\ts_nop 0\n\tglobal_load_lds_dwordx4 %0, %1" ::"v"(voff), "s"(sbase), "s"(lds_base), "n"(imm) : "memory", "scc");
}
DI void gldsA(const void* sbase, unsigned voff, unsigned lds_base, int imm) {
  asm volatile("s_add_u32 m0, %2, %3\n\ts_nop 0\n\tglobal_load_lds_dwordx4 %0, %1 sc1" ::"v"(voff), "s"(sbase), "s"(lds_base), "n"(imm) : "memory", "scc");
}
DI void glds4(const void* sbase, unsigned voff, unsigned lds_addr) {
  asm volatile("s_mov_b32 m0, %2\n\ts_nop 3\n\tglobal_load_lds_dword %0, %1" ::"v"(voff), "s"(sbase), "s"(lds_addr) : "memory");
}
DI void gstore_sc1(const void* sbase, unsigned voff, u32x4 v) {
  asm volatile("s_nop 4\n\tglobal_store_dwordx4 %0, %1, %2 sc1\n\ts_nop 1" ::"v"(voff), "v"(v), "s"(sbase) : "memory");
}
DI void gstore8_sc1(const void* sbase, unsigned voff, u32x2 v) {
  asm volatile("s_nop 4\n\tglobal_store_dwordx2 %0, %1, %2 sc1\n\ts_nop 1" ::"v"(voff), "v"(v), "s"(sbase) : "memory");
}
template <int OFF> DI u32x4 gload16(const void* sbase, unsigned voff) {
  u32x4 v; asm volatile("global_load_dwordx4 %0, %1, %2 offset:%3" : "=v"(v) : "v"(voff), "s"(sbase), "n"(OFF) : "memory"); return v;
}
DI u32x4 gload16_sc1(const void* sbase, unsigned voff) {
  u32x4 v; asm volatile("s_nop 4\n\tglobal_load_dwordx4 %0, %1, %2 sc1" : "=v"(v) : "v"(voff), "s"(sbase) : "memory"); return v;
}
DI u32x2 gload8_sc1(const void* sbase, unsigned voff) {
  u32x2 v; asm volatile("s_nop 4\n\tglobal_load_dwordx2 %0, %1, %2 sc1" : "=v"(v) : "v"(voff), "s"(sbase) : "memory"); return v;
}
DI u32x2 gload8(const void* sbase, unsigned voff) {
  u32x2 v; asm volatile("s_nop 4\n\tglobal_load_dwordx2 %0, %1, %2" : "=v"(v) : "v"(voff), "s"(sbase) : "memory"); return v;
}
DI unsigned gload4_sc1(const void* sbase, unsigned voff) {
  unsigned v; asm volatile("s_nop 4\n\tglobal_load_dword %0, %1, %2 sc1" : "=v"(v) : "v"(voff), "s"(sbase) : "memory"); return v;
}
DI unsigned gload2(const void* sbase, unsigned voff) {
  unsigned v; asm volatile("s_nop 4\n\tglobal_load_ushort %0, %1, %2" : "=v"(v) : "v"(voff), "s"(sbase) : "memory"); return v;
}
template <int N> DI void wait1(u32x4& a) { asm volatile("s_waitcnt vmcnt(%1)" : "+v"(a) : "n"(N) : "memory"); }
template <int N, int X> DI void wait1x(u32x4& a, unsigned more) {
  asm volatile("s_waitcnt vmcnt(%2)\n\ts_cmp_eq_u32 %1, 0\n\ts_cbranch_scc1 1f\n\ts_waitcnt vmcnt(%3)\n1:" : "+v"(a) : "s"(more), "n"(N + X), "n"(N) : "memory", "scc");
}
template <int N> DI void wait3(u32x4& a, u32x4& b, u32x4& c) { asm volatile("s_waitcnt vmcnt(%3)" : "+v"(a), "+v"(b), "+v"(c) : "n"(N) : "memory"); }
template <int N> DI void wait2(u32x4& a, u32x4& b) { asm volatile("s_waitcnt vmcnt(%2)" : "+v"(a), "+v"(b) : "n"(N) : "memory"); }
template <int N> DI void wait4(u32x2& a, u32x2& b, u32x2& c, u32x2& d) { asm volatile("s_waitcnt vmcnt(%4)" : "+v"(a), "+v"(b), "+v"(c), "+v"(d) : "n"(N) : "memory"); }
template <int N> DI void waitn2(u32x2& a) { asm volatile("s_waitcnt vmcnt(%1)" : "+v"(a) : "n"(N) : "memory"); }
template <int N> DI void wait5(u32x2& a, u32x2& b, u32x2& c, u32x2& d, u32x2& e) { asm volatile("s_waitcnt vmcnt(%5)" : "+v"(a), "+v"(b), "+v"(c), "+v"(d), "+v"(e) : "n"(N) : "memory"); }
DI void reload2(u32x4& a, u32x4& b, const void* pa, const void* pb, unsigned voff) {
  asm volatile("s_nop 4\n\tglobal_load_dwordx4 %0, %2, %3 sc1\n\tglobal_load_dwordx4 %1, %2, %4 sc1\n\ts_waitcnt vmcnt(0)" : "+&v"(a), "+&v"(b) : "v"(voff), "s"(pa), "s"(pb) : "memory");
}
DI float msel(unsigned v, unsigned long long m) { float o; asm("v_cndmask_b32 %0, 0, %1, %2" : "=v"(o) : "v"(v), "s"(m)); return o; }
DI float bsel(float lo, float hi, unsigned long long m) { float o; asm("v_cndmask_b32 %0, %1, %2, %3" : "=v"(o) : "v"(lo), "v"(hi), "s"(m)); return o; }
DI unsigned msel_u(unsigned v, unsigned long long m) { unsigned o; asm("v_cndmask_b32 %0, 0, %1, %2" : "=v"(o) : "v"(v), "s"(m)); return o; }
template <int N> DI void waitg(u32x4& a, u32x2& b, u32x2& c) { asm volatile("s_waitcnt vmcnt(%3)" : "+v"(a), "+v"(b), "+v"(c) : "n"(N) : "memory"); }
DI void reloadg(u32x4& a, u32x2& b, u32x2& c, const void* pa, const void* pb, const void* pc, unsigned oa, unsigned ob) {
  asm volatile("s_nop 4\n\tglobal_load_dwordx4 %0, %3, %5 sc1\n\tglobal_load_dwordx2 %1, %4, %6 sc1\n\tglobal_load_dwordx2 %2, %4, %7 sc1\n\ts_waitcnt vmcnt(0)"
               : "+&v"(a), "+&v"(b), "+&v"(c) : "v"(oa), "v"(ob), "s"(pa), "s"(pb), "s"(pc) : "memory");
}
DI void pin2(u32x2& a) { asm volatile("" : "+v"(a)::"memory"); }
DI void wait0_2(u32x2& a) { asm volatile("s_waitcnt vmcnt(0)" : "+v"(a)::"memory"); }
DI void opaque(unsigned& a) { asm volatile("" : "+v"(a)); }
DI void opaquef(float& a) { asm volatile("" : "+v"(a)); }
DI int lane_now() { int l; asm volatile("v_mbcnt_lo_u32_b32 %0, -1, 0\n\tv_mbcnt_hi_u32_b32 %0, -1, %0" : "=v"(l)); return l; }
DI void opaque16(f32x16& a) { asm volatile("" : "+v"(a)); }
DI void opaquec(f32x2& a) { float p = a[0], q = a[1]; asm volatile("" : "+v"(p), "+v"(q)); a[0] = p; a[1] = q; }
DI bf16x8 tr_frag(unsigned a0, unsigned a1) {
  bf16x4 lo = __builtin_amdgcn_ds_read_tr16_b64_v4bf16((LAS bf16x4*)a0);
  bf16x4 hi = __builtin_amdgcn_ds_read_tr16_b64_v4bf16((LAS bf16x4*)a1);
  return __builtin_shufflevector(lo, hi, 0, 1, 2, 3, 4, 5, 6, 7);
}
DI u32x4 lds_rd(unsigned a) { return *(LAS u32x4*)a; }
DI void lds_wr(unsigned a, u32x4 v) { *(LAS u32x4*)a = v; }
DI f32x2 lds_rd8(unsigned a) { return *(LAS f32x2*)a; }
DI void lds_wr8(unsigned a, f32x2 v) { *(LAS f32x2*)a = v; }
DI u32x4 norm16(u32x4 v, float a, float b) {
  unsigned a0 = v[0], a1 = v[1], a2 = v[2], a3 = v[3], t0, t1, t2, t3;
  asm("v_lshlrev_b32 %4, 16, %0\n\tv_lshlrev_b32 %5, 16, %1\n\tv_lshlrev_b32 %6, 16, %2\n\tv_lshlrev_b32 %7, 16, %3\n\t"
      "v_and_b32 %0, 0xffff0000, %0\n\tv_and_b32 %1, 0xffff0000, %1\n\tv_and_b32 %2, 0xffff0000, %2\n\tv_and_b32 %3, 0xffff0000, %3\n\t"
      "v_fma_f32 %4, %4, %8, %9\n\tv_fma_f32 %5, %5, %8, %9\n\tv_fma_f32 %6, %6, %8, %9\n\tv_fma_f32 %7, %7, %8, %9\n\t"
      "v_fma_f32 %0, %0, %8, %9\n\tv_fma_f32 %1, %1, %8, %9\n\tv_fma_f32 %2, %2, %8, %9\n\tv_fma_f32 %3, %3, %8, %9\n\t"
      "v_cvt_pk_bf16_f32 %0, %4, %0\n\tv_cvt_pk_bf16_f32 %1, %5, %1\n\tv_cvt_pk_bf16_f32 %2, %6, %2\n\tv_cvt_pk_bf16_f32 %3, %7, %3"
      : "+v"(a0), "+v"(a1), "+v"(a2), "+v"(a3), "=&v"(t0), "=&v"(t1), "=&v"(t2), "=&v"(t3) : "v"(a), "v"(b));
  return u32x4{a0, a1, a2, a3};
}
DI unsigned norm4(unsigned a, float sc, float bi) {
  unsigned t;
  asm volatile("v_lshlrev_b32 %1, 16, %0\n\tv_and_b32 %0, 0xffff0000, %0\n\tv_fma_f32 %1, %1, %2, %3\n\tv_fma_f32 %0, %0, %2, %3\n\tv_cvt_pk_bf16_f32 %0, %1, %0"
               : "+v"(a), "=&v"(t) : "v"(sc), "v"(bi));
  return a;
}
template <int CTRL> DI float dpp_add(float a) {
  return a + __builtin_bit_cast(float, __builtin_amdgcn_update_dpp(0, __builtin_bit_cast(int, a), CTRL, 0xf, 0xf, true));
}
DI f32x2 cfma(f32x2 a, f32x2 b, f32x2 c) {
  f32x2 o;
  o[0] = __builtin_fmaf(a[0], b[0], __builtin_fmaf(-a[1], b[1], c[0]));
  o[1] = __builtin_fmaf(a[0], b[1], __builtin_fmaf(a[1], b[0], c[1]));
  return o;
}
DI f32x2 cfma_s(f32x2 a, f32x2 b, f32x2 c) {
  float o0, o1;
  asm("v_fma_f32 %0, -%3, %5, %6\n\tv_fma_f32 %1, %3, %4, %7\n\tv_fmac_f32 %0, %2, %4\n\tv_fmac_f32 %1, %2, %5"
      : "=&v"(o0), "=&v"(o1) : "v"(a[0]), "v"(a[1]), "v"(b[0]), "v"(b[1]), "v"(c[0]), "v"(c[1]));
  return f32x2{o0, o1};
}
DI f32x2 cmul_s(f32x2 a, f32x2 b) {
  float o0, o1;
  asm("v_mul_f32_e64 %0, -%3, %5\n\tv_mul_f32 %1, %3, %4\n\tv_fmac_f32 %0, %2, %4\n\tv_fmac_f32 %1, %2, %5"
      : "=&v"(o0), "=&v"(o1) : "v"(a[0]), "v"(a[1]), "v"(b[0]), "v"(b[1]));
  return f32x2{o0, o1};
}
DI float usel(float lo, float hi) { float o; asm("v_cndmask_b32 %0, %1, %2, %3" : "=v"(o) : "v"(lo), "v"(hi), "s"(0xffffffff00000000ull)); return o; }
DI f32x2 csq(f32x2 a) { f32x2 o; o[0] = __builtin_fmaf(a[0], a[0], -a[1] * a[1]); o[1] = 2.f * a[0] * a[1]; return o; }
DI float sum16(float v) {
  auto t = __builtin_amdgcn_permlane16_swap(__float_as_uint(v), __float_as_uint(v), false, false);
  return __uint_as_float(t[0]) + __uint_as_float(t[1]);
}
DI float up32(float v) {
  auto t = __builtin_amdgcn_permlane32_swap(0u, __float_as_uint(v), false, false);
  return __uint_as_float(t[0]);
}

#define STAMP(i)
#define RSTAMP(i)
#define STAMP_OUT()

constexpr int KM = 3;
#define SPD 2
constexpr int km(int R) { return R == 0 ? KM : 2; }
constexpr int p1_tile(int R, int s) { return s < 3 * km(R) ? s % km(R) : s / 3; }
constexpr int p1_kt(int R, int s) { return s < 3 * km(R) ? s / km(R) : s % 3; }
constexpr int p2_cb(int n) { return n >= 420 ? (n - 420) / 4 : (n % 12) % 3; }
constexpr int p2_k4(int n) { return n >= 420 ? (n - 420) % 4 : (n % 12) / 3; }
constexpr int EPI0 = 101;
constexpr int corr_tile(int n) { return n == 99 ? 0 : n == 106 ? 1 : n == 113 ? 2 : n == 145 ? 3 : n == 181 ? 4 : n == 217 ? 5 : -1; }
constexpr int EP = 33;
#define STV 1
constexpr int st_step(int T, int o) { int r = EPI0 + EP * T + (o ? 33 : 17); while (STV ? (r % 12 != 1 && r % 12 != 4 && r % 12 != 7 && r % 12 != 9) : r % 12 != 7) ++r; return r; }
constexpr int st_id(int n) { for (int i = 0; i < 12; ++i) if (st_step(i >> 1, i & 1) == n) return i; return -1; }
constexpr bool op_w(int R, int n) {
  const int s = n / 12, k = n % 12;
  if ((R == 1 && s >= 12 && s <= 18 && s != 16) || (R == 2 && s >= 15)) return false;
  return s == 0 ? k < 6 : (s <= 32 && k % 3 == 0 && k <= 6);
}
constexpr int w_slot(int R, int s) { return R == 1 && s >= 9 && s <= 11 ? (s == 11 ? 18 : s + 11) : s + 3; }
constexpr int x_piece(int n) { const int t = n % 36; return n >= 72 ? -1 : t == 9 ? 0 : t == 11 ? 1 : t == 14 ? 2 : t == 17 ? 3 : t == 20 ? 4 : t == 22 ? 5 : -1; }
constexpr bool op_x(int R, int n) { return R == 0 && x_piece(n) >= 0; }
constexpr int a_code(int n) {
  const int s = n / 12, k = n % 12;
  if (s == 17) return k < 4 ? k - 1 : -9;
  if (s == 18) return (k >= 6 && k <= 8) ? k - 3 : -9;
  if (n == 12 * 32 + 4) return 34;
  return (s >= 19 && s <= 32 && (k == 2 || k == 5)) ? 2 * (s - 16) + (k == 5) : -9;
}
constexpr bool op_a(int R, int n) {
  const int s = n / 12, k = n % 12;
  if (R == 2) return false;
  if (R == 1) return a_code(n) != -9;
  return (k == 2 || k == 5) && s >= 15 && s <= 32;
}
constexpr bool op_pf(int R, int n) {
  return n % 12 == 8 && (R == 1 ? n / 12 <= 6 : R == 0 && n / 12 >= 5 && n / 12 <= 9 && n / 12 != 6);
}
#define NOCT 0
#define ET1 501
#define CT0 502
constexpr bool op_cp(int R, int n) { const int s = n / 12; return !NOCT && n % 12 == 5 && s >= 7 && s <= 28 && s % 7 == 0 && !(R == 1 && s == 14); }
constexpr bool op_st(int R, int n) {
  const int s = n / 12, k = n % 12;
  if (R) return (s == 11 || (R == 1 ? s == 21 : s == 17 || s == 23)) && k >= 7 && k <= 10;
  return st_id(n) >= 0;
}
constexpr int cs_piece(int n) { return n == 23 ? 4 : n == 25 ? 0 : n == 28 ? 1 : n == 31 ? 2 : n == 33 ? 3 : n == 35 ? 5 : n == 38 ? 6 : -1; }
constexpr int op_g(int R, int n) { return R == 0 && n >= 79 && n <= 81; }
constexpr bool op_cs(int R, int n) { return R == 0 && cs_piece(n) >= 0; }
#define ET2 500
constexpr bool op_e(int R, int n) { return R == 0 && (n == CT0 || n == ET1 || n == ET2); }
constexpr bool op_ft(int R, int n) { return R != 2 && n == 12 * 6 + 10; }
constexpr bool op_fo(int R, int n) { return R == 0 && n % 12 == 8 && n / 12 >= 25 && n / 12 <= 31; }
constexpr bool drain(int R, int s) { return R == 2 && s == 18; }
constexpr int n_fill(int R, int n) { return op_w(R, n) + op_x(R, n) + op_a(R, n); }
constexpr int n_lazy(int R, int n) { return op_pf(R, n) + op_cp(R, n) + op_st(R, n) + op_fo(R, n) + op_cs(R, n) + op_e(R, n) + op_g(R, n) + op_ft(R, n); }
constexpr int last_fill(int R, int L) { while (L >= 0 && n_fill(R, L) == 0) --L; return L; }
constexpr int ops_between(int R, int a, int e) { int n = 0; for (int i = a; i <= e; ++i) n += n_fill(R, i) + n_lazy(R, i); return n; }
constexpr int vm_n(int R, int L, int e) { const int l = last_fill(R, L); return (l >= 0 ? n_lazy(R, l) : 0) + ops_between(R, l + 1, e); }
constexpr int done_slot(int R, int m) { int s = m / 12; while (!drain(R, s) && last_fill(R, 12 * s - 1) <= m && s < 40) ++s; return s + 1; }
constexpr int pub_at(int R, int s) {
  if (s == 6) return 1;
  if (R == 0) { for (int T = 0; T < 6; ++T) if (done_slot(0, st_step(T, 1)) == s) return T + 2; }
  else if (R == 1) { if (s == 17) return 2; if (done_slot(1, 12 * 21 + 10) == s) return 3; }
  else { for (int b = 0; b < 2; ++b) if (done_slot(R, 12 * (11 + 6 * b) + 10) == s) return b + 2; }
  return 0;
}
static_assert(done_slot(0, 12 * 31 + 8) <= 34, "schedule");
static_assert(pub_at(1, 24) == 3 && pub_at(2, 14) == 2 && pub_at(2, 19) == 3, "schedule");
static_assert(done_slot(0, st_step(0, 1)) <= 14 && done_slot(0, st_step(5, 1)) <= 29, "schedule");
constexpr unsigned chk_need(int R, int s) {
  if (R == 0) return (s >= 15 && s <= 30 && (s - 15) % 3 == 0) ? (s - 15) / 3 + 2 : 0;
  if (R == 1) return s == 26 ? 3 : s == 20 ? 2 : s == 22 ? 3 : s == 30 ? 4 : 0;
  return 0;
}
constexpr int u_hlp(int u) { return (u >= 6 && u <= 9) ? (u - 6) >> 1 : u >= 16 ? 2 : -1; }
constexpr int u_mem(int u) { return (u < 6 ? u : u - 10) >> 1; }
constexpr int u_blk(int u) { return u < 6 ? 0 : 1; }
constexpr unsigned F_FIN = 8;

struct Params {
  const u16 *x, *lre, *lim, *bre, *bim, *cre, *cim, *ldt, *dsk, *w1, *b1, *w2;
  char* ws;
  float* out;
};

template <int R>
DI void body(const Params& P, const unsigned lds0, const int c, const int j) {
  constexpr bool SSM = R != 0;
  constexpr int PSTR = R == 2 ? 192 : 64;
  char* const ws = P.ws;
  const int tid = threadIdx.x, lane = tid & 63;
  const int w = __builtin_amdgcn_readfirstlane(tid >> 6);
  const int rg = w & 3, set = w >> 2;
  const int r = lane & 31, h = lane >> 5;
  unsigned stampv = 0; (void)stampv;
  if constexpr (R == 0) asm volatile("fk_mlp_beg:");
  RSTAMP(60); STAMP(0);
  const int gq = SSM ? 4 : j / 3, mi = j - 3 * gq;

  char* const wc = ws + WS_C0 + (size_t)c * WS_CS;
  u32x4 xr[2][9];
  unsigned aux[3], epoch, tag;
  const int ww = (w + (SSM ? 2 * c : c)) & 7;
  const int rb0 = 8 * ((w + (SSM ? 4 * mi : 4 * gq)) & 15), krot = mi;
  const int rb1_ = rb0 ^ 64;
  const char* xb1 = (const char*)P.x + (size_t)(128 * c + rb1_) * (D * 2);
  const unsigned xoff = (lane >> 3) * (D * 2) + (lane & 7) * 16;
  const char* xb0 = (const char*)P.x + (size_t)(128 * c + rb0) * (D * 2);
  unsigned voffx = (lane >> 3) * (D * 2) + (((lane & 7) ^ ((4 * (w & 1) + (lane >> 4)) & 7)) * 16);
  auto dmaX = [&](int third, int i) INL {
    int kb = (i >> 1) + krot; kb = kb >= 3 ? kb - 3 : kb;
    gldsW(((i & 1) ? xb1 : xb0) + (3 * third + kb) * 128, voffx, lds0 + kb * 16384 + ((i & 1) ? rb1_ : rb0) * 128, X_OFF);
  };
  auto early_touch = [&](int half) INL {
    const unsigned l = lane_now();
    unsigned id = w * 16 + (l >> 2); id = id < 108 ? id : 107;
    glds4((const char*)P.w1 + (size_t)(36 * c) * (F * 2) + 768 * j + 384 * half, (id / 3) * (F * 2) + (id % 3) * 128 + (l & 3) * 4, lds0 + JUNK_OFF);
  };
  constexpr int NAUX = 4;
  if constexpr (SSM) {
    __builtin_amdgcn_s_setprio(2);
    static_for<0, 9>([&](auto I) INL { xr[0][I.value] = gload16<I.value * 128>(xb0, xoff); });
    epoch = gload4_sc1(ws + WS_EPOCH, 0u);
    const unsigned o = (R == 2 ? 128 + 192 * (w < 3 ? w : 0) + lane : 192 * mi + (tid < 192 ? tid : 0)) * 2;
    aux[0] = gload2(P.lre, o); aux[1] = gload2(P.lim, o); aux[2] = gload2(P.ldt, o);
  } else {
  }

  unsigned pvo = lane * 16;
  unsigned voff1, voff2;
  const char *wa[2], *wb[3][2], *acm[3];
  if constexpr (SSM) {
    voff1 = voff2 = voffx;
    wa[0] = (const char*)P.bre + (size_t)((R == 2 ? 128 : 192 * mi) + 8 * ww) * (D * 2);
    wa[1] = (const char*)P.bim + (size_t)((R == 2 ? 128 : 192 * mi) + 8 * ww) * (D * 2);
  } else {
    const unsigned swz = ((lane & 7) ^ (((lane >> 4) & 1) << 2)) * 16;
    voff1 = (lane >> 3) * (F * 2) + swz;
    voff2 = (lane >> 3) * (D * 2) + swz;
    wa[0] = wa[1] = (const char*)P.w1 + ((size_t)(8 * ww) * F + 384 * j) * 2;
  }
#pragma unroll
  for (int k = 0; k < 3; ++k) {
    const int mm = (mi + k >= 3) ? mi + k - 3 : mi + k;
    if constexpr (SSM) {
      wb[k][0] = (const char*)P.cre + (size_t)(192 * mi + 8 * ww) * (D * 2) + 384 * mm;
      wb[k][1] = (const char*)P.cim + (size_t)(192 * mi + 8 * ww) * (D * 2) + 384 * mm;
    } else {
      wb[k][0] = wb[k][1] = (const char*)P.w2 + ((size_t)(384 * (3 * gq + mm) + 8 * ww) * D + 192 * mi) * 2;
    }
    acm[k] = wc + C_ACT + ((3 * gq + mm) * 6) * 16384 + w * 1024;
  }
  const unsigned ldw = lds0 + ww * 1024, lda = lds0 + w * 1024;
  auto dmaW = [&](int s, int i) INL {
    const int dimm = (s & 3) * SLOT + i * 8192;
    const int u = s - 18;
    if constexpr (SSM) {
      if (s < 18) gldsW(wa[p1_tile(R, s) & 1] + (PSTR * (p1_tile(R, s) >> 1)) * (D * 2) + (192 * p1_kt(R, s) + 64 * i) * 2, voff1, ldw, dimm);
      else if (u_hlp(u) < 0) gldsW(wb[u_mem(u)][u & 1] + (64 * i) * (D * 2) + 128 * u_blk(u), voff2, ldw, dimm);
      else gldsW(wb[0][u & 1] - 384 * mi + (64 * i) * (D * 2) + 384 * u_hlp(u) + 256, voff2, ldw, dimm);
    } else {
      if (s < 18) gldsW(wa[0] + ((size_t)(192 * p1_kt(R, s) + 64 * i) * F + 64 * p1_tile(R, s)) * 2, voff1, ldw, dimm);
      else gldsW(wb[u % 3][0] + ((size_t)(64 * (u / 3)) * D + 64 * i) * 2, voff2, ldw, dimm);
    }
  };
  auto code_touch = [&](int lines_per_wave_log2, unsigned skip, unsigned dst) INL {
    unsigned long long pc; unsigned len;
    if constexpr (R == 2) asm volatile("s_getpc_b64 %0\n\ts_mov_b32 %1, fk_hlp_end-." : "=s"(pc), "=s"(len));
    else if constexpr (R == 1) asm volatile("s_getpc_b64 %0\n\ts_mov_b32 %1, fk_ssm_end-." : "=s"(pc), "=s"(len));
    else asm volatile("s_getpc_b64 %0\n\ts_mov_b32 %1, fk_mlp_end-." : "=s"(pc), "=s"(len));
    const unsigned co = SSM ? skip + (((unsigned)w << lines_per_wave_log2) + ((unsigned)lane_now() >> (6 - lines_per_wave_log2))) * 128
                            : skip + ((((blockIdx.x >> 3) & ((1u << (lines_per_wave_log2 - 1)) - 1)) * 8 + w) * 2 + ((unsigned)lane_now() >> 5)) * 128;
    glds4((const void*)pc, co < len ? co : 0u, dst);
  };
  const unsigned ximg = (lane >> 3) * 128 + (((lane & 7) ^ ((4 * (w & 1) + (lane >> 4)) & 7)) * 16);
  float rs[2], nb[2], mn[2];
  unsigned xw = lds0 + X_OFF + rb0 * 128 + ximg;
  auto xwrite1 = [&](int ps, int i) INL { lds_wr((ps ? xw ^ 8192 : xw) + (i % 3) * 16384, xr[ps][i]); };
  auto stats = [&](auto PI) INL {
    constexpr int ps = PI.value;
    f32x4 s1 = {}, s2 = {}, s3 = {};
    const unsigned selv = (((lane >> 4) & 3) == (lane & 3)) ? 0x3f803f80u : 0u;
    const u32x4 sel = {selv, selv, selv, selv};
    static_for<0, 9>([&](auto GI) INL {
      constexpr int i = GI.value;
      if constexpr (ps == 0) { xr[1][i] = gload16<i * 128>(xb1, xoff); if constexpr (i == 8) __builtin_amdgcn_s_setprio(0); wait1<9 + NAUX>(xr[0][i]); }
      else wait1<8 - i + 5>(xr[1][i]);
      const u32x2 t0 = {xr[ps][i][0], xr[ps][i][1]}, t1 = {xr[ps][i][2], xr[ps][i][3]};
      s2 = __builtin_amdgcn_mfma_f32_4x4x4bf16_1k(__builtin_bit_cast(s16x4, t0), __builtin_bit_cast(s16x4, t0), s2, 0, 0, 0);
      s3 = __builtin_amdgcn_mfma_f32_4x4x4bf16_1k(__builtin_bit_cast(s16x4, t1), __builtin_bit_cast(s16x4, t1), s3, 0, 0, 0);
      s1 = MFMA16(BF8(sel), BF8(xr[ps][i]), s1);
      SCHED();
    });
    if constexpr (ps == 0) {
      dmaW(0, 0); dmaW(0, 1); dmaW(0, 2);
      unsigned id = rg * 16 + (lane >> 2); id = id < 36 ? id : 35;
      glds4(wa[0] + (set ? wa[1] - wa[0] : 0) - 8 * ww * (D * 2) + c * 36 * 128, id * 128 + (lane & 3) * 4, lds0 + JUNK_OFF);
      code_touch(5, 0, lds0 + JUNK_OFF);
    }
    s2 += s3;
    const float e0 = s2[0], e1 = s2[1], e2 = s2[2], e3 = s2[3];
    const float lo2 = (lane & 1) ? e1 : e0, hi2 = (lane & 1) ? e3 : e2;
    float a2 = (lane & 2) ? hi2 : lo2;
    const float f0 = s1[0], f1 = s1[1], f2 = s1[2], f3 = s1[3];
    const float lo1 = (lane & 16) ? f1 : f0, hi1 = (lane & 16) ? f3 : f2;
    float a1 = (lane & 32) ? hi1 : lo1;
    a1 = dpp_add<0xB1>(a1); a2 = dpp_add<0xB1>(a2);
    a1 = dpp_add<0x4E>(a1); a2 = dpp_add<0x4E>(a2);
    a1 = dpp_add<0x141>(a1); a2 = dpp_add<0x141>(a2);
    const float m = a1 * (1.f / D);
    const float var = fmaxf(a2 * (1.f / D) - m * m, 0.f);
    rs[ps] = __builtin_amdgcn_rsqf(var + 1e-5f);
    nb[ps] = -m * rs[ps];
    mn[ps] = m;
    if constexpr (ps == 1) {
      opaque(epoch);
      tag = __builtin_amdgcn_readfirstlane(epoch) + 1u;
      if constexpr (R == 2) {
        gstore_sc1(wc + C_STAT + rb0 * 16, (lane >> 3) * 16, u32x4{__float_as_uint(mn[0]), tag, __float_as_uint(rs[0]), tag});
        gstore_sc1(wc + C_STAT + rb1_ * 16, (lane >> 3) * 16, u32x4{__float_as_uint(mn[1]), tag, __float_as_uint(rs[1]), tag});
      }
    }
#pragma unroll
    for (int i = 0; i < 3; ++i) { xr[ps][i] = norm16(xr[ps][i], rs[ps], nb[ps]); xwrite1(ps, i); }
  };
  if constexpr (!SSM) {
    __builtin_amdgcn_s_setprio(0);
    dmaW(1, 0); dmaW(1, 1); dmaW(1, 2);
    STAMP(1); STAMP(2);
  } else {
    stats(IC<0>{});
    STAMP(1);
    stats(IC<1>{});
    STAMP(2);
    dmaW(1, 0); dmaW(1, 1); dmaW(1, 2);
    opaque(aux[0]); opaque(aux[1]); opaque(aux[2]);
    if (w < 3) {
      const float lr = bflo(aux[0]), li = bflo(aux[1]);
      const float dt = __builtin_amdgcn_exp2f(bflo(aux[2]) * LOG2E);
      const float mag = __builtin_amdgcn_exp2f(dt * lr * LOG2E);
      const float rev = __builtin_amdgcn_fractf(dt * li * 0.15915494309189535f);
      f32x2 L = {mag * __builtin_amdgcn_cosf(rev), mag * __builtin_amdgcn_sinf(rev)};
      const float inv = 2.f / (lr * lr + li * li);
      const float nr = L[0] - 1.f, ni = L[1];
      const f32x2 c2 = {(nr * lr + ni * li) * inv, (ni * lr - nr * li) * inv};
      const f32x2 L16 = csq(csq(csq(csq(L))));
      const f32x2 L128 = csq(csq(csq(L16)));
      lds_wr8(lds0 + LT_OFF + tid * 8, L); lds_wr8(lds0 + LT_OFF + 1536 + tid * 8, c2);
      lds_wr8(lds0 + LT_OFF + 2 * 1536 + tid * 8, L16); lds_wr8(lds0 + LT_OFF + 3 * 1536 + tid * 8, L128);
    }
  }
  u32x2 flg;
  const int rperm = 16 * ((r >> 2) & 1) + (r & 3) + 4 * (r >> 3);
  bf16x8 xn[36];
  unsigned xn_ad[4];
  { const int row = 32 * rg + (SSM ? rperm : r); xn_ad[0] = lds0 + X_OFF + row * 128 + ((h ^ ((row >> 1) & 7)) * 16); }
  xn_ad[1] = xn_ad[0] ^ 32; xn_ad[2] = xn_ad[0] ^ 64; xn_ad[3] = xn_ad[0] ^ 96;
  auto xn_read = [&](int rd, int ks) INL { xn[12 * rd + ks] = BF8(lds_rd(xn_ad[ks & 3] + (ks >> 2) * 16384)); };

  const int G1 = (lane >> 4) & 1, q = (lane >> 2) & 3, p4 = lane & 3;
  unsigned rb1[4], rb2[4][2], ab[2];
  if constexpr (SSM) {
    const int row = 32 * set + r;
    rb1[0] = lds0 + row * 128 + ((h ^ ((row >> 1) & 7)) * 16);
    rb1[1] = rb1[0] ^ 32; rb1[2] = rb1[0] ^ 64; rb1[3] = rb1[0] ^ 96;
    opaque(rb1[0]); opaque(rb1[1]); opaque(rb1[2]); opaque(rb1[3]);
  } else {
    rb1[0] = lds0 + (8 * h + q) * 128 + (((set * 4 + G1 * 2 + (p4 >> 1)) ^ (((q >> 1) & 1) << 2)) * 16) + (p4 & 1) * 8;
    rb1[1] = rb1[0] + 2 * SLOT;
    opaque(rb1[0]); opaque(rb1[1]);
  }
  unsigned tabb = lds0 + TAB_OFF + (32 * set + r) * (SSM ? 8 : 16);
  opaque(tabb);
  unsigned tabh = tabb + h * 2048 + rg * 512;
  if constexpr (SSM) opaque(tabh);
  auto wfrag = [&](int n) INL {
    const int s = n / 12, ks = n % 12;
    if constexpr (SSM) {
      if (s < 18) return BF8(lds_rd(rb1[ks & 3] + (s & 1) * SLOT + (ks >> 2) * 8192));
      else return BF8(lds_rd(rb2[p2_k4(n)][(s >> 1) & 1] + (s & 1) * SLOT + p2_cb(n) * 8192));
    } else {
      if (s < 18) { unsigned a = rb1[(s >> 1) & 1] + (s & 1) * SLOT + ks * 2048; return tr_frag(a, a + 512); }
      else { unsigned a = rb2[0][(s >> 1) & 1] + (s & 1) * SLOT + p2_cb(n) * 8192 + p2_k4(n) * 2048; return tr_frag(a, a + 1024); }
    }
  };
  auto afrag = [&](int kq) INL {
    if constexpr (SSM) {
      const int u = kq >> 2, k4 = kq & 3;
      const int v = (k4 < 2) ? u - 1 : u;
      const unsigned off = (v == 17 ? 3 : (v + 1) % 3) * 16384 + ((k4 < 2) ? 8192 : 0) + (k4 & 1) * 256;
      return tr_frag(ab[0] + off, ab[1] + off);
    } else {
      return BF8(lds_rd(ab[0] + ((kq >> 2) & 3) * 16384 + (kq & 3) * 4096));
    }
  };
  WAIT_L0();
  STAMP(3);
  WAIT_V(SSM ? 5 : 4);
  BAR();
  STAMP(4);

  f32x16 acc[4];
  constexpr int NA = KM + 1;
  f32x16 oacc[3];
  u32x4 actp[2], gr[2];
  float tn, tr, tprev, tprev2;
  f32x2 Lam, Lx, ee, cc, cl, cq, g0, g1, g2;
  const unsigned long long gm0 = (c - 1 - 4 * rg >= 0 ? 0xffffffffull : 0ull) | (c - 3 - 4 * rg >= 0 ? 0xffffffff00000000ull : 0ull);
  const unsigned long long gm1 = (c - 2 - 4 * rg >= 0 ? 0xffffffffull : 0ull) | (c - 4 - 4 * rg >= 0 ? 0xffffffff00000000ull : 0ull);
  bf16x8 fr[4], af[4];
  u32x2 fx[3], fdk[3], gc[2];
  u32x4 fst, corrA, corrB;
  float cs[8], rsl;
  const int w67 = (w + 2) >> 3, cu0 = (w << w67) * 512, cu1 = cu0 + (6 - 5 * w67) * 512;
  float sk[12], xf[12];
  unsigned sto;
  const f32x16 zero = {};
  const char* amine = wc + C_ACT + (j * 6) * 16384 + set * 8192 + rg * (SSM ? 2048 : 1024);
  const char* flagb = wc + C_FLAG + (3 * gq) * 128;
  fr[0] = wfrag(0); fr[1] = wfrag(1);
  if constexpr (!SSM || SPD == 3) fr[2] = wfrag(2);

  const char* pbase = wc + C_PART + (rg * 9 + 3 * mi) * 20480 + gq * 4096 + set * 2048;
  auto pstore = [&](int cb, int hh) INL {
    u32x4 v;
#pragma unroll
    for (int e = 0; e < 4; ++e) v[e] = pk2(oacc[cb][8 * hh + 2 * e], oacc[cb][8 * hh + 2 * e + 1]);
    const unsigned o = pvo ^ (set * 128 + ((pvo >> 3) & 64) + hh * 32);
    if (!SSM && rg == gq) lds_wr(lds0 + ARING + 32768 + cb * 4096 + set * 2048 + hh * 1024 + o, v);
    else gstore_sc1(pbase + cb * 20480 + hh * 1024, o, v);
  };
  const int fpk = (w >> 1) < 3 ? (gq + 1 + (w >> 1)) & 3 : 4;
  const char* ffl = wc + C_FLGW + (3 * fpk + mi) * 1024 + (gq + 4 * (w & 1)) * 128;
  bool got = false;
  auto fin_ready = [&]() INL { return __builtin_amdgcn_readfirstlane(flg[1]) == tag && __builtin_amdgcn_readfirstlane(flg[0]) >= F_FIN; };
  auto fin_fetch = [&]() INL {
    const char* src = wc + C_PART + (gq * 9 + 3 * mi) * 20480 + fpk * 4096 + (w & 1) * 2048;
#pragma unroll
    for (int i = 0; i < 6; ++i) gldsA(src + (i >> 1) * 20480 + (i & 1) * 1024, pvo, lds0 + (w >> 1) * 12288 + (w & 1) * 2048, (i >> 1) * 4096 + (i & 1) * 1024);
  };
  auto step = [&](auto NI) INL {
    constexpr int n = NI.value;
    constexpr int s = n / 12, ks = n % 12;
    if constexpr (R != 2 && n == 216 - 4) {
      const int lane = lane_now(), r = lane & 31, h = lane >> 5, G1 = (lane >> 4) & 1, q = (lane >> 2) & 3, p4 = lane & 3;
      if constexpr (SSM) {
        const int rperm = 16 * ((r >> 2) & 1) + (r & 3) + 4 * (r >> 3);
        const int row = 32 * set + rperm;
        rb2[0][0] = lds0 + row * 128 + ((h ^ ((row >> 1) & 7)) * 16);
        rb2[1][0] = rb2[0][0] ^ 32; rb2[2][0] = rb2[0][0] ^ 64; rb2[3][0] = rb2[0][0] ^ 96;
#pragma unroll
        for (int k = 0; k < 4; ++k) { rb2[k][1] = rb2[k][0] + 2 * SLOT; opaque(rb2[k][0]); opaque(rb2[k][1]); }
        const int o = p4 >> 1;
        ab[0] = lds0 + ARING + rg * 2048 + o * 1024 + G1 * 512 + (((2 * h) ^ (2 * G1 + o)) * 64) + q * 16 + (p4 & 1) * 8;
        ab[1] = ab[0] ^ 64;
        opaque(ab[0]); opaque(ab[1]);
      } else {
        const int dch = 4 * (p4 & 1) + 2 * G1 + (p4 >> 1);
        rb2[0][0] = lds0 + (4 * h + q) * 128 + (((set * 4 + (dch >> 1)) ^ (((q >> 1) & 1) << 2)) * 16) + (dch & 1) * 8;
        rb2[0][1] = rb2[0][0] + 2 * SLOT;
        ab[0] = lds0 + ARING + rg * 1024 + lane * 16;
        opaque(rb2[0][0]); opaque(rb2[0][1]); opaque(ab[0]);
      }
    }
    if constexpr (SSM && n == 16 * 12) pvo = lane_now() * 16;
    if constexpr (SSM && ks == 12 - SPD && (s & 1) == 1 && s < (R == 1 ? 11 : 17)) {
#pragma unroll
      for (int k = 0; k < 4; ++k) { rb1[k] += (s & 2) ? -2 * SLOT : 2 * SLOT; opaque(rb1[k]); }
    }
    constexpr int ss = (R == 1 && s >= 16) ? s - 4 : s;
    constexpr int pb = (ss >= 6) ? (ss - 6) / 6 : 0, ph = (ss >= 6 && ss < (R == 1 ? 18 : 24)) ? (ss - 6) % 6 : -1;
    constexpr bool mm_on = R == 0 || (R == 1 ? (n < 144 || n >= 216) : n < 216);
    if constexpr (ks == 0) {
      constexpr int pub = pub_at(R, s);
#define EPOLL 0
      constexpr bool poll = chk_need(R, (SSM || !EPOLL) ? s : s + 1) != 0;
      if constexpr (poll) { if (w == 0) { const int l = lane_now(); flg = gload8_sc1(flagb, (l & 3) * 128); } }
      if constexpr (pub > 0) { if (w == 1) gstore8_sc1(flagb + mi * 128, (lane_now() & 15) * 8, u32x2{(unsigned)pub, tag}); }
      if constexpr (SSM && ph == 3) {
        const unsigned go = 3 * tabb - tabh;
        const char* gb = ws + WS_GRAN + ((R == 2 ? pb * 3 + 2 : mi * 3 + pb) * 16 + c - 1 - 4 * rg) * 1024 + rg * 512 - 2 * (lds0 + TAB_OFF);
        gr[0] = gload16_sc1(gb, go);
        gr[1] = gload16_sc1(gb - 1024, go);
      }
    }
    if constexpr (s < 3 * km(R)) {
      constexpr int kt = s / km(R), v = n - 12 * km(R) * kt;
      if constexpr (v == 0) { xn_read(kt, 0); xn_read(kt, 1); xn_read(kt, 2); xn_read(kt, 3); }
      if constexpr (SSM) {
        if constexpr (v >= 1 && v + 3 < 12) xn_read(kt, v + 3);
        constexpr int ch = v == 21 ? 5 : (v >= 12 && v <= 20 && v % 2 == 0) ? (v - 12) / 2 : -1;
        if constexpr (kt < 2 && ch >= 0) { xwrite1(ch / 3, 3 * (kt + 1) + ch % 3); SCHED(); }
      } else if constexpr (v >= 1 && v <= 4) { xn_read(kt, 2 * v + 2); xn_read(kt, 2 * v + 3); }
    }
    if constexpr (SSM && ks == 4 && (s == 8 || s == 11 || (R == 2 && (s == 14 || s == 17)))) {
      const int l = lane_now(), r = l & 31;
      const int row = 32 * rg + 16 * ((r >> 2) & 1) + (r & 3) + 4 * (r >> 3);
      const unsigned a = lds0 + X_OFF + 32768 + row * 128 + (((l >> 5) ^ ((row >> 1) & 7)) * 16);
      xn[32] = BF8(lds_rd(a)); xn[33] = BF8(lds_rd(a ^ 32)); xn[34] = BF8(lds_rd(a ^ 64)); xn[35] = BF8(lds_rd(a ^ 96));
    }
    if constexpr (op_w(R, n)) { if constexpr (s == 0) dmaW(2 + ks / 3, ks % 3); else dmaW(w_slot(R, s), ks / 3); }
    if constexpr (op_x(R, n)) dmaX(n / 36 + 1, x_piece(n));
    if constexpr (op_a(R, n)) {
      if constexpr (SSM) {
        constexpr int pc = a_code(n) & 1, v = (a_code(n) - pc) / 2, u = v + pc;
        gldsA((u_hlp(u) < 0 ? acm[u_hlp(u) < 0 ? u_mem(u) : 0] + (2 * u_blk(u) + (u & 1)) * 16384 : wc + C_ACT + (15 * 6 + 2 * u_hlp(u) + (u & 1)) * 16384 + w * 1024) + (pc ? 0 : 8192), pvo,
              lda, ARING + (v == 17 ? 3 : (v + 1) % 3) * 16384 + pc * 8192);
      } else {
        constexpr int u = s + 3 - 18;
        gldsA(acm[u % 3] + (u / 3) * 16384 + (ks / 3) * 8192, pvo, lda, ARING + (u & 3) * 16384 + (ks / 3) * 8192);
      }
    }
    if constexpr (op_e(R, n)) { if constexpr (n == CT0) code_touch(5, 0, lds0 + JUNK_OFF); else early_touch(n != ET1); }
    if constexpr (op_g(R, n)) {
      if constexpr (n == 79) fst = gload16_sc1(wc + C_STAT, (32 * rg) * 16 + (pvo & 496));
      else gc[n - 80] = gload8_sc1(ws + WS_CSUM + j * 8192 + (n == 80 ? cu0 : cu1), pvo >> 1);
    }
    if constexpr (op_cs(R, n)) {
      constexpr int i = cs_piece(n);
      const char* cb_ = (const char*)P.w1 + (size_t)(288 * ((c / 6) & 1) + 36 * w + 8 * i) * (F * 2) + 768 * j + (c % 6) * 128;
      if constexpr (i < 5) xr[0][i] = gload16<0>(cb_, ((pvo >> 7) & (i < 4 ? 7 : 3)) * (F * 2) + (pvo & 112));
      else if constexpr (i == 5) epoch = gload4_sc1(ws + WS_EPOCH, 0u);
      else aux[0] = gload2((const char*)P.b1 + 768 * j + (w < 6 ? w : 0) * 128, pvo >> 3);
    }
    if constexpr (op_ft(R, n)) gstore8_sc1(wc + C_FLGW + j * 1024 + w * 128, (lane_now() & 15) * 8, u32x2{0u, tag});
    if constexpr (op_cp(R, n)) code_touch(3, 3072, ldw + ((s + 3) & 3) * SLOT + 2 * 8192);
    if constexpr (op_pf(R, n)) {
      const unsigned l = lane_now();
      constexpr int sl = SSM ? (s < 3 ? s : s - 3) : (s < 6 ? s - 5 : s - 6);
      if constexpr (SSM) {
        constexpr unsigned lim = s < 3 ? 72 : 108;
        unsigned id = sl * 32 + rg * 8 + (l >> 3); id = id < lim ? id : lim - 1;
        const u16* mat = s < 3 ? (set ? P.bim : P.bre) : (set ? P.cim : P.cre);
        glds4((const char*)mat + (size_t)(192 * mi + (s < 3 ? 64 : 0)) * (D * 2) + c * lim * 128, id * 128 + (l & 7) * 4, lds0 + JUNK_OFF);
      } else if constexpr (s < 3) {
        unsigned id = sl * 64 + w * 8 + (l >> 3); id = id < 144 ? id : 143;
        glds4((const char*)P.w1 + (size_t)(36 * c) * (F * 2) + 768 * j + 256, (id >> 2) * (F * 2) + (id & 3) * 128 + (l & 7) * 4, lds0 + JUNK_OFF);
      } else {
        unsigned id = sl * 64 + w * 8 + (l >> 3); id = id < 216 ? id : 215;
        glds4((const char*)P.w2 + (size_t)(384 * j) * (D * 2) + c * 216 * 128, id * 128 + (l & 7) * 4, lds0 + JUNK_OFF);
      }
    }
    if constexpr (SSM && n + SPD < (R == 1 ? 144 : 216)) fr[(n + SPD) & 3] = wfrag(n + SPD);
    if constexpr ((R == 0 || (R == 1 && n >= 213)) && n + 3 < 432) fr[(n + 3) & 3] = wfrag(n + 3);
    if constexpr (R == 2) {
    } else if constexpr (SSM) {
      constexpr int u = s - 18;
      if constexpr (s >= 18 && s < 35 && (ks == 3 || ks == 6)) af[ks / 3 + 1] = afrag(4 * u + ks / 3 + 1);
      if constexpr (s >= 17 && s < 35 && (ks == 9 || ks == 10)) af[ks - 9] = afrag(4 * (u + 1) + ks - 9);
      if constexpr (n == 419 || n == 420) af[n - 417] = afrag(68 + n - 417);
    } else {
      if constexpr (n + 3 >= 216 && n + 3 <= 420 && (n + 3) % 3 == 0) af[((n + 3 - 216) / 3) & 3] = afrag((n + 3 - 216) / 3);
      if constexpr (n >= 418 && n <= 420) af[n - 417] = afrag(68 + n - 417);
      if constexpr (op_fo(R, n)) {
        constexpr int i = s - 25;
        const unsigned rw = w * 4 + (pvo >> 8), co = (pvo >> 1) & 120;
        if constexpr (i < 3) fx[i] = gload8((const char*)P.x + (size_t)(128 * c + 32 * gq) * (D * 2) + 384 * mi + i * 128, rw * (D * 2) + co);
        else if constexpr (i < 6) fdk[i - 3] = gload8((const char*)P.dsk + 384 * mi + (i - 3) * 128, co);
        else fst = gload16_sc1(wc + C_STAT, (32 * gq + rw) * 16);
      }
      if constexpr (n == 408) { pin2(fx[0]); pin2(fx[1]); pin2(fx[2]); pin2(fdk[0]); pin2(fdk[1]); pin2(fdk[2]); wait1<63>(fst); flg = gload8_sc1(ffl, 0u); rsl = __uint_as_float(fst[2]) * LOG2E; tn = __uint_as_float(fst[2]) * (1.f / LOG2E); opaquef(rsl); opaquef(tn); }
      if constexpr (n >= 408 && n < 420) {
        constexpr int e = n - 408;
        const unsigned xv = fx[e >> 2][(e >> 1) & 1], dv = fdk[e >> 2][(e >> 1) & 1];
        xf[e] = (e & 1) ? bfhi(xv) : bflo(xv);
        sk[e] = (xf[e] - __uint_as_float(fst[0])) * tn * ((e & 1) ? bfhi(dv) : bflo(dv));
        opaquef(xf[e]); opaquef(sk[e]);
      }
#define FCHK 422
      if constexpr (n == FCHK) {
        SCHED();
        wait0_2(flg);
        got = fin_ready();
        if (got) fin_fetch();
      }
    }
    if constexpr (!mm_on) {
    } else if constexpr (s < 18) {
      constexpr int T = p1_tile(R, s), kt = p1_kt(R, s);
      if constexpr (SSM) { acc[T & 3] = MFMA32(xn[12 * kt + ks], fr[n & 3], (kt == 0 && ks == 0) ? zero : acc[T & 3]); opaque16(acc[T & 3]); }
      else { acc[T % NA] = MFMA32(fr[n & 3], xn[12 * kt + ks], (kt == 0 && ks == 0) ? zero : acc[T % NA]); opaque16(acc[T % NA]); }
      if constexpr (SSM && s < 2 * km(R)) {
        constexpr int v = n - 12 * km(R) * kt, q = km(R) == 3 ? 4 : 6;
        constexpr int d0 = (q * v + 4) / 5, d1 = (q * (v + 1) + 4) / 5;
        if constexpr (d0 < d1 && d0 < 24) { constexpr int ch = d0 >> 2; xr[ch / 3][3 * (kt + 1) + ch % 3][d0 & 3] = norm4(xr[ch / 3][3 * (kt + 1) + ch % 3][d0 & 3], rs[ch / 3], nb[ch / 3]); }
        if constexpr (d0 + 1 < d1 && d0 + 1 < 24) { constexpr int d = d0 + 1, ch = d >> 2; xr[ch / 3][3 * (kt + 1) + ch % 3][d & 3] = norm4(xr[ch / 3][3 * (kt + 1) + ch % 3][d & 3], rs[ch / 3], nb[ch / 3]); }
      }
    } else {
      oacc[p2_cb(n)] = MFMA32(fr[n & 3], af[p2_k4(n)], n < 219 ? zero : oacc[p2_cb(n)]);
      opaque16(oacc[p2_cb(n)]);
      if constexpr (n == 425 || n == 426 || n == 429 || n == 430) pstore((n - 425) / 4, (n - 425) & 1);
    }
    if constexpr (!SSM) {
      if constexpr (n >= 36 && n <= 53) {
        if constexpr (n == 36) { SCHED(); wait2<ops_between(0, 26, 36)>(xr[0][4], xr[0][0]); }
        if constexpr (n == 42) { SCHED(); wait1<ops_between(0, 29, 42)>(xr[0][1]); }
        if constexpr (n == 46) { SCHED(); wait1<ops_between(0, 32, 46)>(xr[0][2]); }
        if constexpr (n == 50) { SCHED(); wait1<ops_between(0, 34, 50)>(xr[0][3]); }
        if constexpr (n < 38) {
#pragma unroll
          for (int d = 2 * (n - 36); d < 2 * (n - 35); ++d) { const unsigned v = msel_u(xr[0][4][d], 0xffffffffull); cs[2 * d] = bflo(v); cs[2 * d + 1] = bfhi(v); opaquef(cs[2 * d]); opaquef(cs[2 * d + 1]); }
        } else {
          constexpr int d = n - 38;
          const unsigned v = xr[0][d >> 2][d & 3]; cs[2 * (d & 3)] += bflo(v); opaquef(cs[2 * (d & 3)]); cs[2 * (d & 3) + 1] += bfhi(v); opaquef(cs[2 * (d & 3) + 1]);
        }
      }
      if constexpr (n == 54) {
#pragma unroll
        for (int e = 0; e < 4; ++e) { auto t = __builtin_amdgcn_permlane32_swap(__float_as_uint(cs[e]), __float_as_uint(cs[e + 4]), false, false); cs[e] = __uint_as_float(t[0]) + __uint_as_float(t[1]); opaquef(cs[e]); }
      }
      if constexpr (n == 55) {
#pragma unroll
        for (int e = 0; e < 2; ++e) { auto t = __builtin_amdgcn_permlane16_swap(__float_as_uint(cs[e]), __float_as_uint(cs[e + 2]), false, false); cs[e] = dpp_add<0x128>(__uint_as_float(t[0]) + __uint_as_float(t[1])); opaquef(cs[e]); }
        const unsigned l = pvo >> 4;
        *(LAS float*)(lds0 + TAB_OFF + 8192 + w * 256 + (8 * (l & 7) + (l >> 3)) * 4) = bsel(cs[0], cs[1], 0xff00ff00ff00ff00ull);
      }
      if constexpr (n == 60) {
        opaque(epoch); opaque(aux[0]); tag = __builtin_amdgcn_readfirstlane(epoch) + 1u;
        if (w == 2) {
          float t = 0.f;
#pragma unroll
          for (int k = 0; k < 8; ++k) t += *(LAS float*)(lds0 + TAB_OFF + 8192 + k * 256 + (pvo >> 2));
          gstore8_sc1(ws + WS_CSUM + j * 8192 + c * 512, pvo >> 1, u32x2{__float_as_uint(t), tag});
        }
      }
      if constexpr (n == 91) {
        SCHED();
        waitg<ops_between(0, 82, 91)>(fst, gc[0], gc[1]);
        auto stale = [&]() INL { return __builtin_amdgcn_ballot_w64(((fst[1] ^ tag) | (fst[3] ^ tag) | (gc[0][1] ^ tag) | (gc[1][1] ^ tag)) != 0u) != 0ull; };
        if (__builtin_expect(stale(), 0)) {
          const char* gb = ws + WS_CSUM + j * 8192;
          int spins = 200000;
          do { reloadg(fst, gc[0], gc[1], wc + C_STAT + (32 * rg) * 16, gb + cu0, gb + cu1, pvo & 496, pvo >> 1); } while (stale() && --spins > 0);
        }
        const float Sv = __uint_as_float(gc[0][0]) + __uint_as_float(gc[1][0]);
        const unsigned Sh = pk2(Sv, Sv), Sl = pk2(Sv - bfhi(Sh), 0.f);
        lds_wr(lds0 + TAB_OFF + w * 1024 + pvo, u32x4{Sh, (Sl & 0xffffu) | (aux[0] << 16), aux[0], 0u});
      }
      if constexpr (n == 93) { tn = __builtin_amdgcn_rcpf(__uint_as_float(fst[2])); rsl = __uint_as_float(fst[2]) * LOG2E; opaquef(tn); opaquef(rsl); }
      if constexpr (n == 94) { const float mu = -__uint_as_float(fst[0]); gc[0][0] = pk2(mu, tn); gc[0][1] = pk2(mu - bflo(gc[0][0]), tn - bfhi(gc[0][0])); pin2(gc[0]); }
      if constexpr (n == 95) {
        const unsigned hi = gc[0][0], lo = gc[0][1];
        corrB = u32x4{msel_u((hi & 0xffffu) | (lo << 16), 0xffffffffull), msel_u(hi, 0xffffffffull), msel_u(lo >> 16, 0xffffffffull), msel_u(lo, 0ull)};
        wait1<63>(corrB);
      }
      if constexpr (corr_tile(n + (n == 96 ? 3 : 6)) >= (n == 96 ? 0 : 1)) corrA = lds_rd(tabb + corr_tile(n + (n == 96 ? 3 : 6)) * 1024);
      if constexpr (corr_tile(n) >= 0) { acc[corr_tile(n) % NA] = MFMA32(BF8(corrA), BF8(corrB), acc[corr_tile(n) % NA]); opaque16(acc[corr_tile(n) % NA]); }
      if constexpr (n >= EPI0 && n < EPI0 + 6 * EP) {
        constexpr int T = (n - EPI0) / EP, u = (n - EPI0) % EP, e = u >> 1;
        const f32x16& pa = acc[T % NA];
        if constexpr ((u & 1) == 0) {
          if constexpr (e < 16) { tn = __builtin_amdgcn_exp2f(pa[e] * rsl); opaquef(tn); }
          if constexpr (e >= 1) {
            const float a = __builtin_fmaf(-pa[e - 1], tr, pa[e - 1]);
            if constexpr (((e - 1) & 1) == 0) { tprev = a; opaquef(tprev); }
            else { unsigned pk = pk2(tprev, a); opaque(pk); actp[(e - 1) >> 3][((e - 1) >> 1) & 3] = pk; }
          }
        } else { tr = __builtin_amdgcn_rcpf(__builtin_fmaf(tn, __builtin_fmaf(tn, 0.5f, 1.f), 1.f)); opaquef(tr); }
      }
      if constexpr (op_st(0, n)) { constexpr int id = st_id(n); gstore_sc1(amine + (id >> 1) * 16384 + (id & 1) * 4096, pvo, actp[id & 1]); }
    } else if constexpr (ph >= 0) {
      f32x16& ur = acc[(2 * pb) & 3];
      f32x16& ui = acc[(2 * pb + 1) & 3];
      const unsigned lt = tabb + pb * 512;
      const unsigned wt = tabb + (WT_OFF - TAB_OFF) + 2048, gt = tabb + (GT_OFF - TAB_OFF);
      if constexpr (ph == 0) {
        if constexpr (ks == 0) { ee = cfma(Lam, f32x2{ur[0], ui[0]}, f32x2{ur[1], ui[1]}); opaquec(ee); }
        if constexpr (ks >= 1 && ks <= 9) {
          constexpr int t0 = 2 + ((ks - 1) * 14) / 9, t1 = 2 + (ks * 14) / 9;
          ee = cfma(Lam, ee, f32x2{ur[t0], ui[t0]});
          if constexpr (t0 + 1 < t1) ee = cfma(Lam, ee, f32x2{ur[t0 + 1], ui[t0 + 1]});
          opaquec(ee);
        }
        if constexpr (ks == 7) Lx = lds_rd8(lt + 2 * 1536);
        if constexpr (ks == 10) {
          const f32x2 elo = {up32(ee[0]), up32(ee[1])};
          lds_wr8(tabh + (WT_OFF - TAB_OFF), cfma_s(Lx, elo, ee));
        }
        if constexpr (ks == 11) { Lam = csq(Lx); opaquec(Lam); }
      }
      if constexpr (ph == 1) {
        if constexpr (ks == 0) { g0 = lds_rd8(wt); g1 = lds_rd8(wt + 512); g2 = lds_rd8(wt + 1024); ee = lds_rd8(wt + 1536); }
        if constexpr (ks == 2) {
          cl = f32x2{0.f, 0.f}; cq = f32x2{1.f, 0.f};
          if (rg > 0) { cl = g0; cq = Lam; }
          if (rg > 1) { cl = cfma_s(Lam, cl, g1); cq = cmul_s(Lam, Lam); }
          if (rg > 2) {
            cl = cfma_s(Lam, cl, g2); cq = cmul_s(Lam, cq);
            const f32x2 E = cfma_s(Lam, cl, ee);
            gstore_sc1(ws + WS_GRAN + ((R == 2 ? pb * 3 + 2 : mi * 3 + pb) * 16 + c) * 1024 - 2 * (lds0 + TAB_OFF), 2 * tabb,
                       u32x4{__float_as_uint(E[0]), tag, __float_as_uint(E[1]), tag});
          }
          opaquec(cl); opaquec(cq);
          g0 = lds_rd8(wt - 2048 + rg * 512); cc = lds_rd8(lt + 1536);
        }
        if constexpr (ks == 4) {
          const f32x2 a = cfma_s(Lx, cl, g0);
          cl = f32x2{usel(cl[0], a[0]), usel(cl[1], a[1])};
          opaquec(cl);
        }
        if constexpr (ks == 5) { const f32x2 a = cmul_s(Lx, cq); cq = f32x2{usel(cq[0], a[0]), usel(cq[1], a[1])}; opaquec(cq); }
        if constexpr (ks == 6) { cl = cmul_s(cc, cl); opaquec(cl); }
        if constexpr (ks == 7) { cq = cmul_s(cc, cq); opaquec(cq); }
      }
      if constexpr ((ph == 1 && ks >= 8) || ph == 2) {
        constexpr int t = (ph == 1) ? ks - 8 : ks + 4;
        const float t1 = cc[1] * ui[t], t2 = cc[1] * ur[t];
        float a = __builtin_fmaf(cc[0], ur[t], -t1), b = __builtin_fmaf(-cc[0], ui[t], -t2);
        opaquef(a); opaquef(b);
        ur[t] = a; ui[t] = b;
      }
      if constexpr (ph == 3) {
        if constexpr (ks == 4) cc = lds_rd8(lt + 3 * 1536);
        if constexpr (ks == 7) { Lx = csq(cc); opaquec(Lx); }
        if constexpr (ks == 9) {
          SCHED();
          wait2<ops_between(R, 12 * s, n)>(gr[0], gr[1]);
          auto stale = [&]() INL {
            const unsigned b0 = (gr[0][1] ^ tag) | (gr[0][3] ^ tag), b1 = (gr[1][1] ^ tag) | (gr[1][3] ^ tag);
            return ((__builtin_amdgcn_ballot_w64(b0 != 0u) & gm0) | (__builtin_amdgcn_ballot_w64(b1 != 0u) & gm1)) != 0ull;
          };
          if (__builtin_expect(stale(), 0)) {
            const unsigned go = 3 * tabb - tabh;
            const char* gb = ws + WS_GRAN + ((R == 2 ? pb * 3 + 2 : mi * 3 + pb) * 16 + c - 1 - 4 * rg) * 1024 + rg * 512 - 2 * (lds0 + TAB_OFF);
            int spins = 200000;
            do { reload2(gr[0], gr[1], gb, gb - 1024, go); } while (stale() && --spins > 0);
          }
          const f32x2 a0 = {msel(gr[0][0], gm0), msel(gr[0][2], gm0)}, a1 = {msel(gr[1][0], gm1), msel(gr[1][2], gm1)};
          ee = cfma_s(cc, a1, a0); opaquec(ee);
        }
        if constexpr (ks == 10) {
          const f32x2 lo = {up32(ee[0]), up32(ee[1])};
          lds_wr8(tabh + (GT_OFF - TAB_OFF), cfma_s(Lx, ee, lo));
        }
        if constexpr (ks == 11) { cc = csq(Lx); opaquec(cc); }
      }
      if constexpr (ph == 4 && ks <= 3) {
        if constexpr (ks == 0) { ee = lds_rd8(gt + 2048 + 1536); g0 = lds_rd8(gt + 2048 + 1024); g1 = lds_rd8(gt + 2048 + 512); g2 = lds_rd8(gt + 2048); Lam = lds_rd8(lt); }
        if constexpr (ks == 2) { ee = cfma(cc, cfma(cc, ee, g0), g1); opaquec(ee); }
        if constexpr (ks == 3) { ee = cfma(cq, cfma(cc, ee, g2), cl); ee[1] = -ee[1]; opaquec(ee); }
      }
      if constexpr ((ph == 4 && ks >= 4) || (ph == 5 && ks <= 7)) {
        constexpr int tt = (ph == 4) ? ks - 4 : ks + 8;
        ee = cfma(f32x2{Lam[0], -Lam[1]}, ee, f32x2{ur[tt], ui[tt]});
        opaquec(ee);
        if constexpr ((tt & 1) == 0) { tprev = ee[0]; tprev2 = ee[1]; }
        else {
          unsigned p0 = pk2(tprev, ee[0]), p1 = pk2(tprev2, ee[1]); opaque(p0); opaque(p1);
          if constexpr (tt < 8) { gr[0][tt >> 1] = p0; gr[1][tt >> 1] = p1; }
          else { actp[0][(tt >> 1) & 3] = p0; actp[1][(tt >> 1) & 3] = p1; }
        }
      }
      if constexpr (op_st(R, n)) {
        constexpr int o = (ks - 7) >> 1, part = (ks - 7) & 1;
        if constexpr (ks == 7) { const int l = lane_now(); sto = (l ^ (8 * (l >> 5))) * 16; }
        if constexpr (ks == 9) sto ^= 64;
        gstore_sc1(amine + (2 * pb + part) * 16384 + o * 1024, sto, o ? actp[part] : gr[part]);
      }
    }
    if constexpr (SSM && ks == 9 && (s == 5 || s == 11 || (R == 2 && s == 17))) Lam = lds_rd8(tabb + ((s - 5) / 6) * 512);
    if constexpr (n == 8) { SCHED(); WAIT_V(ops_between(R, 0, 8)); BAR(); }
    if constexpr (R == 1 && n == 212) {
      SCHED(); WAIT_V(0); BAR();
      if (w == 0) { const int l = lane_now(); flg = gload8_sc1(flagb, (l & 3) * 128); }
    }
    if constexpr (R == 1 && n == 221) {
      SCHED();
      if (w == 0) {
        wait0_2(flg);
        const int l = lane_now();
        int spins = 200000;
        while ((__builtin_amdgcn_ballot_w64(flg[1] != tag || flg[0] < 2u) & 7ull) != 0ull && --spins > 0) { flg = gload8_sc1(flagb, (l & 3) * 128); wait0_2(flg); }
      }
      BAR();
    }
    if constexpr (R == 1 && n == 236) { SCHED(); WAIT_V(vm_n(1, 222, 236)); BAR(); }
    if constexpr (!SSM && n == 44) { SCHED(); BAR(); }
    if constexpr (ks == 11 && s < 34) {
      SCHED();
      if constexpr (SSM && (s == 1 || s == 3)) WAIT_L0();
      if constexpr (SSM && (ph == 0 || ph == 3 || s >= 17)) WAIT_L0();
      WAIT_V(drain(R, s) ? 0 : (R == 1 && s == 16) ? ops_between(1, 192, n) : vm_n(R, s == 0 ? 2 : (R == 0 && (s == 1 || s == 4)) ? 12 * s - 4 : 12 * s - 1, n));
      if constexpr (chk_need(R, s) != 0) {
        if (w == 0) {
          constexpr unsigned need = chk_need(R, s);
          constexpr unsigned long long lanes = (R == 1 && s != 26) ? 8ull : 7ull;
          if constexpr (SSM || !EPOLL) WAIT_V(ops_between(R, 12 * s, n));
          pin2(flg);
          auto late = [&]() INL { return (__builtin_amdgcn_ballot_w64(flg[1] != tag || flg[0] < need) & lanes) != 0ull; };
          if (__builtin_expect(late(), 0)) {
            const int l = lane_now();
            int spins = 200000;
            do { flg = gload8_sc1(flagb, (l & 3) * 128); wait0_2(flg); } while (late() && --spins > 0);
          }
        }
      }
      BAR();
      STAMP(5 + s);
    }
    SCHED();
  };
  run_seq<0>(step, __make_integer_seq<ISeq, int, 144>{});
  if constexpr (R != 1) run_seq<144>(step, __make_integer_seq<ISeq, int, 48>{});
  else { STAMP(17); STAMP(18); STAMP(19); STAMP(20); }
  run_seq<192>(step, __make_integer_seq<ISeq, int, 24>{});
  run_seq<216>(step, __make_integer_seq<ISeq, int, R == 2 ? 71 : 216>{});
  if constexpr (R != 2) { pstore(2, 0); pstore(2, 1); }
  STAMP(39);
  WAIT_V(0);
  if constexpr (R == 2) { BAR(); if (w == 1) gstore8_sc1(flagb + mi * 128, (lane_now() & 15) * 8, u32x2{F_FIN, tag}); }
  else gstore8_sc1(wc + C_FLGW + j * 1024 + w * 128, (lane_now() & 15) * 8, u32x2{F_FIN, tag});
  STAMP(40);
  if constexpr (!SSM) {
    const int l = lane_now(), t = w * 64 + l;
    const int c4 = t & 15, rho = c4 >> 1;
    const unsigned ra = lds0 + (c4 >> 3) * 2048 + (rho & 1) * 1024 + ((c4 >> 2) & 1) * 512 + (((t >> 4) ^ (2 * rho)) * 16) + (c4 & 1) * 8;
    const char* ob = (const char*)(P.out + (size_t)(128 * c + 32 * gq) * D + 192 * mi);
    const unsigned oo = (t >> 4) * (D * 4) + c4 * 16;
    if (!got) {
      int spins = 400000;
      do { flg = gload8_sc1(ffl, 0u); wait0_2(flg); } while (!fin_ready() && --spins > 0);
      fin_fetch();
    }
    STAMP(41);
    STAMP(42);
    WAIT_L0();
    WAIT_V(0);
    STAMP(43);
    BAR();
    STAMP(44);
#pragma unroll
    for (int cb = 0; cb < 3; ++cb) {
      u32x2 pv[4];
#pragma unroll
      for (int k = 0; k < 4; ++k) pv[k] = *(LAS u32x2*)(ra + k * 12288 + cb * 4096);
      const u32x2 ov = *(LAS u32x2*)(ra + ARING + 32768 + cb * 4096);
      f32x4 o;
#pragma unroll
      for (int e = 0; e < 4; ++e) {
        auto up = [&](unsigned v) INL { return (e & 1) ? bfhi(v) : bflo(v); };
        const float z = ((up(ov[e >> 1]) + up(pv[0][e >> 1])) + (up(pv[1][e >> 1]) + up(pv[2][e >> 1]))) * rsl;
        const float gt = z * __builtin_amdgcn_rcpf(1.f + __builtin_amdgcn_exp2f(-z));
        o[e] = __builtin_fmaf(__builtin_fmaf(up(pv[3][e >> 1]), 0.693147180559945f, sk[4 * cb + e]), gt, xf[4 * cb + e]);
      }
      gstore_sc1(ob + 256 * cb, oo, __builtin_bit_cast(u32x4, o));
    }
    if (c == 15 && j == 0 && t == 0) *(unsigned*)(ws + WS_EPOCH) = tag;
  }
  if constexpr (R == 2) asm volatile("fk_hlp_end:"); else if constexpr (R == 1) asm volatile("fk_ssm_end:"); else asm volatile("fk_mlp_end:");
  if constexpr (SSM) { STAMP(41); STAMP(42); STAMP(43); STAMP(44); }
  STAMP(45);
  STAMP(46);
  RSTAMP(61);
  STAMP_OUT();
}

#if defined(__HIP_DEVICE_COMPILE__)
#define NOPK __attribute__((target("no-packed-fp32-ops")))
#else
#define NOPK
#endif
__global__ NOPK __launch_bounds__(512, 2) void main_kernel(const Params P) {
  __shared__ __attribute__((aligned(16))) char smem[163840];
  const unsigned lds0 = (unsigned)(size_t)(LAS char*)smem;
  const int b = blockIdx.x, xcd = b & 7, idx = b >> 3;
#define MAP 2
  int j, c;
  if (MAP == 0) { j = idx >> 1; c = 2 * xcd + (idx & 1); }
  else if (idx >= 24) { j = 12 + ((idx - 24) >> 1); c = 2 * xcd + (idx & 1); }
  else if (MAP == 1) { j = 6 * (xcd & 1) + (idx >> 2); c = 4 * (xcd >> 1) + (idx & 3); }
  else { j = 3 * (xcd & 3) + (idx >> 3); c = 8 * (xcd >> 2) + (idx & 7); }
#if defined(__HIP_DEVICE_COMPILE__)
  int jj = j;
  asm volatile("" : "+s"(jj));
  if (jj < 12) {
    const int lane = threadIdx.x & 63, w = __builtin_amdgcn_readfirstlane(threadIdx.x >> 6);
    const int gq = xcd & 3, mi = idx >> 3, rb0 = 8 * ((w + 4 * gq) & 15), ww = (w + c) & 7;
    const unsigned voffx = (lane >> 3) * (D * 2) + (((lane & 7) ^ ((4 * (w & 1) + (lane >> 4)) & 7)) * 16);
    const unsigned voff1 = (lane >> 3) * (F * 2) + (((lane & 7) ^ (((lane >> 4) & 1) << 2)) * 16);
    __builtin_amdgcn_s_setprio(2);
#pragma unroll
    for (int i = 0; i < 6; ++i) {
      int kb = (i >> 1) + mi; kb = kb >= 3 ? kb - 3 : kb;
      const int rb = (i & 1) ? rb0 ^ 64 : rb0;
      gldsW((const char*)P.x + (size_t)(128 * c + rb) * (D * 2) + kb * 128, voffx, lds0 + kb * 16384 + rb * 128, X_OFF);
    }
#pragma unroll
    for (int i = 0; i < 3; ++i) gldsW((const char*)P.w1 + ((size_t)(8 * ww + 64 * i) * F + 384 * j) * 2, voff1, lds0 + ww * 1024, i * 8192);
    unsigned long long pc; unsigned beg, end;
    asm volatile("s_getpc_b64 %0\nfk_entry:\n\ts_mov_b32 %1, fk_mlp_beg-fk_entry\n\ts_mov_b32 %2, fk_mlp_end-fk_entry" : "=s"(pc), "=s"(beg), "=s"(end));
    const unsigned co = beg + (((idx & 15) * 8 + w) * 2 + (lane >> 5)) * 128;
    glds4((const void*)pc, co < end ? co : 0u, lds0 + JUNK_OFF);
  }
  if (j < 12) body<0>(P, lds0, c, j);
  else if (j == 15) body<2>(P, lds0, c, j);
  else body<1>(P, lds0, c, j);
#endif
}

extern "C" void kernel_launch(void* const* d_in, const int* in_sizes, int n_in, void* d_out, int out_size, void* d_ws, size_t ws_size,
                              hipStream_t stream) {
  Params P;
  P.x = (const u16*)d_in[0]; P.lre = (const u16*)d_in[1]; P.lim = (const u16*)d_in[2];
  P.bre = (const u16*)d_in[3]; P.bim = (const u16*)d_in[4]; P.cre = (const u16*)d_in[5]; P.cim = (const u16*)d_in[6];
  P.ldt = (const u16*)d_in[7]; P.w1 = (const u16*)d_in[10]; P.b1 = (const u16*)d_in[11]; P.w2 = (const u16*)d_in[12];
  P.dsk = (const u16*)d_in[8]; P.ws = (char*)d_ws; P.out = (float*)d_out;
  main_kernel<<<256, 512, 0, stream>>>(P);
}
```

```cpp
#include <hip/hip_runtime.h>
#include <hip/hip_bf16.h>
#pragma clang diagnostic ignored "-Wint-to-pointer-cast"

typedef unsigned short u16;
typedef __attribute__((ext_vector_type(2))) __bf16 bf16x2;
typedef __attribute__((ext_vector_type(4))) __bf16 bf16x4;
typedef __attribute__((ext_vector_type(8))) __bf16 bf16x8;
typedef __attribute__((ext_vector_type(4))) short s16x4;
typedef __attribute__((ext_vector_type(2))) float f32x2;
typedef __attribute__((ext_vector_type(4))) float f32x4;
typedef __attribute__((ext_vector_type(16))) float f32x16;
typedef __attribute__((ext_vector_type(2))) unsigned u32x2;
typedef __attribute__((ext_vector_type(4))) unsigned u32x4;
#define LAS __attribute__((address_space(3)))
#define GAS __attribute__((address_space(1)))
#define DI __device__ __forceinline__
#define INL __attribute__((always_inline))

constexpr int S = 2048, D = 576, F = 4608;
constexpr float LOG2E = 1.44269504088896f;
constexpr int SLOT = 24576;
constexpr int X_OFF = 4 * SLOT;
constexpr int ARING = X_OFF;
constexpr int TAB_OFF = ARING + 49152;
constexpr int LT_OFF = TAB_OFF;
constexpr int WT_OFF = TAB_OFF + 6144;
constexpr int JUNK_OFF = TAB_OFF + 14336;
constexpr int GT_OFF = TAB_OFF + 10240;
constexpr size_t WS_EPOCH = 0;
constexpr size_t WS_GRAN = 32768;
constexpr size_t WS_CSUM = 196608;
constexpr size_t WS_C0 = 327680, WS_CS = 2334720;
constexpr int C_FLAG = 0;
constexpr int C_FLGW = 8192;
constexpr int C_STAT = 4096;
constexpr int C_ACT = 24576;
constexpr int C_PART = C_ACT + 1572864;

DI float bflo(unsigned v) { return __uint_as_float(v << 16); }
DI float bfhi(unsigned v) { return __uint_as_float(v & 0xffff0000u); }
DI unsigned pk2(float a, float b) { f32x2 v = {a, b}; return __builtin_bit_cast(unsigned, __builtin_convertvector(v, bf16x2)); }
#define WAIT_V(n) asm volatile("s_waitcnt vmcnt(%0)" ::"n"(n) : "memory")
#define WAIT_L0() asm volatile("s_waitcnt lgkmcnt(0)" ::: "memory")
#define BAR() asm volatile("s_barrier" ::: "memory")
#define SCHED() __builtin_amdgcn_sched_barrier(0)
#define BF8(x) __builtin_bit_cast(bf16x8, (x))
#define MFMA16(a, b, c) __builtin_amdgcn_mfma_f32_16x16x32_bf16((a), (b), (c), 0, 0, 0)
#define MFMA32(a, b, c) __builtin_amdgcn_mfma_f32_32x32x16_bf16((a), (b), (c), 0, 0, 0)
template <int I> struct IC { static constexpr int value = I; };
template <int B, int E, class Fn> DI void static_for(Fn&& f) { if constexpr (B < E) { f(IC<B>{}); static_for<B + 1, E>(f); } }

template <class T, T... I> struct ISeq {};
template <int B, class Fn, int... I> DI void run_seq(Fn&& f, ISeq<int, I...>) { (f(IC<B + I>{}), ...); }

DI void gldsW(const void* sbase, unsigned voff, unsigned lds_base, int imm) {
  asm volatile("s_add_u32 m0, %2, %3\n\ts_nop 0\n\tglobal_load_lds_dwordx4 %0, %1" ::"v"(voff), "s"(sbase), "s"(lds_base), "n"(imm) : "memory", "scc");
}
DI void gldsA(const void* sbase, unsigned voff, unsigned lds_base, int imm) {
  asm volatile("s_add_u32 m0, %2, %3\n\ts_nop 0\n\tglobal_load_lds_dwordx4 %0, %1 sc1" ::"v"(voff), "s"(sbase), "s"(lds_base), "n"(imm) : "memory", "scc");
}
DI void glds4(const void* sbase, unsigned voff, unsigned lds_addr) {
  asm volatile("s_mov_b32 m0, %2\n\ts_nop 3\n\tglobal_load_lds_dword %0, %1" ::"v"(voff), "s"(sbase), "s"(lds_addr) : "memory");
}
DI void gstore_sc1(const void* sbase, unsigned voff, u32x4 v) {
  asm volatile("s_nop 4\n\tglobal_store_dwordx4 %0, %1, %2 sc1\n\ts_nop 1" ::"v"(voff), "v"(v), "s"(sbase) : "memory");
}
DI void gstore8_sc1(const void* sbase, unsigned voff, u32x2 v) {
  asm volatile("s_nop 4\n\tglobal_store_dwordx2 %0, %1, %2 sc1\n\ts_nop 1" ::"v"(voff), "v"(v), "s"(sbase) : "memory");
}
template <int OFF> DI u32x4 gload16(const void* sbase, unsigned voff) {
  u32x4 v; asm volatile("global_load_dwordx4 %0, %1, %2 offset:%3" : "=v"(v) : "v"(voff), "s"(sbase), "n"(OFF) : "memory"); return v;
}
DI u32x4 gload16_sc1(const void* sbase, unsigned voff) {
  u32x4 v; asm volatile("s_nop 4\n\tglobal_load_dwordx4 %0, %1, %2 sc1" : "=v"(v) : "v"(voff), "s"(sbase) : "memory"); return v;
}
DI u32x2 gload8_sc1(const void* sbase, unsigned voff) {
  u32x2 v; asm volatile("s_nop 4\n\tglobal_load_dwordx2 %0, %1, %2 sc1" : "=v"(v) : "v"(voff), "s"(sbase) : "memory"); return v;
}
DI u32x2 gload8(const void* sbase, unsigned voff) {
  u32x2 v; asm volatile("s_nop 4\n\tglobal_load_dwordx2 %0, %1, %2" : "=v"(v) : "v"(voff), "s"(sbase) : "memory"); return v;
}
DI unsigned gload4_sc1(const void* sbase, unsigned voff) {
  unsigned v; asm volatile("s_nop 4\n\tglobal_load_dword %0, %1, %2 sc1" : "=v"(v) : "v"(voff), "s"(sbase) : "memory"); return v;
}
DI unsigned gload2(const void* sbase, unsigned voff) {
  unsigned v; asm volatile("s_nop 4\n\tglobal_load_ushort %0, %1, %2" : "=v"(v) : "v"(voff), "s"(sbase) : "memory"); return v;
}
template <int N> DI void wait1(u32x4& a) { asm volatile("s_waitcnt vmcnt(%1)" : "+v"(a) : "n"(N) : "memory"); }
template <int N, int X> DI void wait1x(u32x4& a, unsigned more) {
  asm volatile("s_waitcnt vmcnt(%2)\n\ts_cmp_eq_u32 %1, 0\n\ts_cbranch_scc1 1f\n\ts_waitcnt vmcnt(%3)\n1:" : "+v"(a) : "s"(more), "n"(N + X), "n"(N) : "memory", "scc");
}
template <int N> DI void wait3(u32x4& a, u32x4& b, u32x4& c) { asm volatile("s_waitcnt vmcnt(%3)" : "+v"(a), "+v"(b), "+v"(c) : "n"(N) : "memory"); }
template <int N> DI void wait2(u32x4& a, u32x4& b) { asm volatile("s_waitcnt vmcnt(%2)" : "+v"(a), "+v"(b) : "n"(N) : "memory"); }
template <int N> DI void wait4(u32x2& a, u32x2& b, u32x2& c, u32x2& d) { asm volatile("s_waitcnt vmcnt(%4)" : "+v"(a), "+v"(b), "+v"(c), "+v"(d) : "n"(N) : "memory"); }
template <int N> DI void waitn2(u32x2& a) { asm volatile("s_waitcnt vmcnt(%1)" : "+v"(a) : "n"(N) : "memory"); }
template <int N> DI void wait5(u32x2& a, u32x2& b, u32x2& c, u32x2& d, u32x2& e) { asm volatile("s_waitcnt vmcnt(%5)" : "+v"(a), "+v"(b), "+v"(c), "+v"(d), "+v"(e) : "n"(N) : "memory"); }
DI void reload2(u32x4& a, u32x4& b, const void* pa, const void* pb, unsigned voff) {
  asm volatile("s_nop 4\n\tglobal_load_dwordx4 %0, %2, %3 sc1\n\tglobal_load_dwordx4 %1, %2, %4 sc1\n\ts_waitcnt vmcnt(0)" : "+&v"(a), "+&v"(b) : "v"(voff), "s"(pa), "s"(pb) : "memory");
}
DI float msel(unsigned v, unsigned long long m) { float o; asm("v_cndmask_b32 %0, 0, %1, %2" : "=v"(o) : "v"(v), "s"(m)); return o; }
DI float bsel(float lo, float hi, unsigned long long m) { float o; asm("v_cndmask_b32 %0, %1, %2, %3" : "=v"(o) : "v"(lo), "v"(hi), "s"(m)); return o; }
DI unsigned msel_u(unsigned v, unsigned long long m) { unsigned o; asm("v_cndmask_b32 %0, 0, %1, %2" : "=v"(o) : "v"(v), "s"(m)); return o; }
template <int N> DI void waitg(u32x4& a, u32x2& b, u32x2& c) { asm volatile("s_waitcnt vmcnt(%3)" : "+v"(a), "+v"(b), "+v"(c) : "n"(N) : "memory"); }
DI void reloadg(u32x4& a, u32x2& b, u32x2& c, const void* pa, const void* pb, const void* pc, unsigned oa, unsigned ob) {
  asm volatile("s_nop 4\n\tglobal_load_dwordx4 %0, %3, %5 sc1\n\tglobal_load_dwordx2 %1, %4, %6 sc1\n\tglobal_load_dwordx2 %2, %4, %7 sc1\n\ts_waitcnt vmcnt(0)"
               : "+&v"(a), "+&v"(b), "+&v"(c) : "v"(oa), "v"(ob), "s"(pa), "s"(pb), "s"(pc) : "memory");
}
DI void pin2(u32x2& a) { asm volatile("" : "+v"(a)::"memory"); }
DI void wait0_2(u32x2& a) { asm volatile("s_waitcnt vmcnt(0)" : "+v"(a)::"memory"); }
DI void opaque(unsigned& a) { asm volatile("" : "+v"(a)); }
DI void opaquef(float& a) { asm volatile("" : "+v"(a)); }
DI int lane_now() { int l; asm volatile("v_mbcnt_lo_u32_b32 %0, -1, 0\n\tv_mbcnt_hi_u32_b32 %0, -1, %0" : "=v"(l)); return l; }
DI void opaque4(f32x4& a) { asm volatile("" : "+v"(a)); }
DI void opaque16(f32x16& a) { asm volatile("" : "+v"(a)); }
DI void opaquec(f32x2& a) { float p = a[0], q = a[1]; asm volatile("" : "+v"(p), "+v"(q)); a[0] = p; a[1] = q; }
DI bf16x8 tr_frag(unsigned a0, unsigned a1) {
  bf16x4 lo = __builtin_amdgcn_ds_read_tr16_b64_v4bf16((LAS bf16x4*)a0);
  bf16x4 hi = __builtin_amdgcn_ds_read_tr16_b64_v4bf16((LAS bf16x4*)a1);
  return __builtin_shufflevector(lo, hi, 0, 1, 2, 3, 4, 5, 6, 7);
}
DI u32x4 lds_rd(unsigned a) { return *(LAS u32x4*)a; }
DI void lds_wr(unsigned a, u32x4 v) { *(LAS u32x4*)a = v; }
DI f32x2 lds_rd8(unsigned a) { return *(LAS f32x2*)a; }
DI void lds_wr8(unsigned a, f32x2 v) { *(LAS f32x2*)a = v; }
DI u32x4 norm16(u32x4 v, float a, float b) {
  unsigned a0 = v[0], a1 = v[1], a2 = v[2], a3 = v[3], t0, t1, t2, t3;
  asm("v_lshlrev_b32 %4, 16, %0\n\tv_lshlrev_b32 %5, 16, %1\n\tv_lshlrev_b32 %6, 16, %2\n\tv_lshlrev_b32 %7, 16, %3\n\t"
      "v_and_b32 %0, 0xffff0000, %0\n\tv_and_b32 %1, 0xffff0000, %1\n\tv_and_b32 %2, 0xffff0000, %2\n\tv_and_b32 %3, 0xffff0000, %3\n\t"
      "v_fma_f32 %4, %4, %8, %9\n\tv_fma_f32 %5, %5, %8, %9\n\tv_fma_f32 %6, %6, %8, %9\n\tv_fma_f32 %7, %7, %8, %9\n\t"
      "v_fma_f32 %0, %0, %8, %9\n\tv_fma_f32 %1, %1, %8, %9\n\tv_fma_f32 %2, %2, %8, %9\n\tv_fma_f32 %3, %3, %8, %9\n\t"
      "v_cvt_pk_bf16_f32 %0, %4, %0\n\tv_cvt_pk_bf16_f32 %1, %5, %1\n\tv_cvt_pk_bf16_f32 %2, %6, %2\n\tv_cvt_pk_bf16_f32 %3, %7, %3"
      : "+v"(a0), "+v"(a1), "+v"(a2), "+v"(a3), "=&v"(t0), "=&v"(t1), "=&v"(t2), "=&v"(t3) : "v"(a), "v"(b));
  return u32x4{a0, a1, a2, a3};
}
DI unsigned norm4(unsigned a, float sc, float bi) {
  unsigned t;
  asm volatile("v_lshlrev_b32 %1, 16, %0\n\tv_and_b32 %0, 0xffff0000, %0\n\tv_fma_f32 %1, %1, %2, %3\n\tv_fma_f32 %0, %0, %2, %3\n\tv_cvt_pk_bf16_f32 %0, %1, %0"
               : "+v"(a), "=&v"(t) : "v"(sc), "v"(bi));
  return a;
}
template <int CTRL> DI float dpp_add(float a) {
  return a + __builtin_bit_cast(float, __builtin_amdgcn_update_dpp(0, __builtin_bit_cast(int, a), CTRL, 0xf, 0xf, true));
}
DI f32x2 cfma(f32x2 a, f32x2 b, f32x2 c) {
  f32x2 o;
  o[0] = __builtin_fmaf(a[0], b[0], __builtin_fmaf(-a[1], b[1], c[0]));
  o[1] = __builtin_fmaf(a[0], b[1], __builtin_fmaf(a[1], b[0], c[1]));
  return o;
}
DI f32x2 cfma_s(f32x2 a, f32x2 b, f32x2 c) {
  float o0, o1;
  asm("v_fma_f32 %0, -%3, %5, %6\n\tv_fma_f32 %1, %3, %4, %7\n\tv_fmac_f32 %0, %2, %4\n\tv_fmac_f32 %1, %2, %5"
      : "=&v"(o0), "=&v"(o1) : "v"(a[0]), "v"(a[1]), "v"(b[0]), "v"(b[1]), "v"(c[0]), "v"(c[1]));
  return f32x2{o0, o1};
}
DI f32x2 cmul_s(f32x2 a, f32x2 b) {
  float o0, o1;
  asm("v_mul_f32_e64 %0, -%3, %5\n\tv_mul_f32 %1, %3, %4\n\tv_fmac_f32 %0, %2, %4\n\tv_fmac_f32 %1, %2, %5"
      : "=&v"(o0), "=&v"(o1) : "v"(a[0]), "v"(a[1]), "v"(b[0]), "v"(b[1]));
  return f32x2{o0, o1};
}
DI float usel(float lo, float hi) { float o; asm("v_cndmask_b32 %0, %1, %2, %3" : "=v"(o) : "v"(lo), "v"(hi), "s"(0xffffffff00000000ull)); return o; }
DI f32x2 csq(f32x2 a) { f32x2 o; o[0] = __builtin_fmaf(a[0], a[0], -a[1] * a[1]); o[1] = 2.f * a[0] * a[1]; return o; }
DI float sum16(float v) {
  auto t = __builtin_amdgcn_permlane16_swap(__float_as_uint(v), __float_as_uint(v), false, false);
  return __uint_as_float(t[0]) + __uint_as_float(t[1]);
}
DI float up32(float v) {
  auto t = __builtin_amdgcn_permlane32_swap(0u, __float_as_uint(v), false, false);
  return __uint_as_float(t[0]);
}

#define STAMP(i)
#define RSTAMP(i)
#define STAMP_OUT()

constexpr int KM = 3;
#define SPD 2
constexpr int km(int R) { return R == 0 ? KM : 2; }
constexpr int p1_tile(int R, int s) { return s < 3 * km(R) ? s % km(R) : s / 3; }
constexpr int p1_kt(int R, int s) { return s < 3 * km(R) ? s / km(R) : s % 3; }
constexpr int g_cb(int g) { return g >= 102 ? (g - 102) / 2 : g % 3; }
constexpr int g_kk(int g) { return g >= 102 ? g & 1 : (g % 6) / 3; }
constexpr int e_buf(int e) { return e == 35 ? 2 : e & 1; }
constexpr int EPI0 = 101;
constexpr int corr_tile(int n) { return n == 99 ? 0 : n == 106 ? 1 : n == 113 ? 2 : n == 145 ? 3 : n == 181 ? 4 : n == 217 ? 5 : -1; }
constexpr int EP = 33;
#define STV 1
constexpr int st_step(int T, int o) { int r = EPI0 + EP * T + (o ? 33 : 17); while (STV ? (r % 12 != 1 && r % 12 != 4 && r % 12 != 7 && r % 12 != 9) : r % 12 != 7) ++r; return r; }
constexpr int st_id(int n) { for (int i = 0; i < 12; ++i) if (st_step(i >> 1, i & 1) == n) return i; return -1; }
constexpr bool op_w(int R, int n) {
  const int s = n / 12, k = n % 12;
  if ((R == 1 && s >= 12 && s <= 18 && s != 16) || (R == 2 && s >= 15)) return false;
  return s == 0 ? k < 6 : (s <= 32 && k % 3 == 0 && k <= 6);
}
constexpr int w_slot(int R, int s) { return R == 1 && s >= 9 && s <= 11 ? (s == 11 ? 18 : s + 11) : s + 3; }
constexpr int x_piece(int n) { const int t = n % 36; return n >= 72 ? -1 : t == 9 ? 0 : t == 11 ? 1 : t == 14 ? 2 : t == 17 ? 3 : t == 20 ? 4 : t == 22 ? 5 : -1; }
constexpr bool op_x(int R, int n) { return R == 0 && x_piece(n) >= 0; }
constexpr int a_code(int n) {
  const int s = n / 12, k = n % 12;
  if (s == 17) return k < 4 ? k - 1 : -9;
  if (s == 18) return (k >= 6 && k <= 8) ? k - 3 : -9;
  if (n == 12 * 32 + 4) return 34;
  return (s >= 19 && s <= 32 && (k == 2 || k == 5)) ? 2 * (s - 16) + (k == 5) : -9;
}
constexpr bool op_a(int R, int n) {
  const int s = n / 12, k = n % 12;
  if (R == 2) return false;
  if (R == 1) return a_code(n) != -9;
  return (k == 2 || k == 5) && s >= 15 && s <= 32;
}
constexpr bool op_pf(int R, int n) {
  return n % 12 == 8 && (R == 1 ? n / 12 <= 6 : R == 0 && n / 12 >= 5 && n / 12 <= 9 && n / 12 != 6);
}
#define NOCT 0
#define ET1 501
#define CT0 502
constexpr bool op_cp(int R, int n) { const int s = n / 12; return !NOCT && n % 12 == 5 && s >= 7 && s <= 28 && s % 7 == 0 && !(R == 1 && s == 14); }
constexpr bool op_st(int R, int n) {
  const int s = n / 12, k = n % 12;
  if (R) return (s == 11 || (R == 1 ? s == 21 : s == 17 || s == 23)) && k >= 7 && k <= 10;
  return st_id(n) >= 0;
}
constexpr int cs_piece(int n) { return n == 23 ? 4 : n == 25 ? 0 : n == 28 ? 1 : n == 31 ? 2 : n == 33 ? 3 : n == 35 ? 5 : n == 38 ? 6 : -1; }
constexpr int op_g(int R, int n) { return R == 0 && n >= 79 && n <= 81; }
constexpr bool op_cs(int R, int n) { return R == 0 && cs_piece(n) >= 0; }
#define ET2 500
constexpr bool op_e(int R, int n) { return R == 0 && (n == CT0 || n == ET1 || n == ET2); }
constexpr bool op_ft(int R, int n) { return R != 2 && n == 12 * 6 + 10; }
constexpr bool op_fo(int R, int n) { return R == 0 && n % 12 == 8 && n / 12 >= 25 && n / 12 <= 31; }
constexpr bool drain(int R, int s) { return R == 2 && s == 18; }
constexpr int n_fill(int R, int n) { return op_w(R, n) + op_x(R, n) + op_a(R, n); }
constexpr int n_lazy(int R, int n) { return op_pf(R, n) + op_cp(R, n) + op_st(R, n) + op_fo(R, n) + op_cs(R, n) + op_e(R, n) + op_g(R, n) + op_ft(R, n); }
constexpr int last_fill(int R, int L) { while (L >= 0 && n_fill(R, L) == 0) --L; return L; }
constexpr int ops_between(int R, int a, int e) { int n = 0; for (int i = a; i <= e; ++i) n += n_fill(R, i) + n_lazy(R, i); return n; }
constexpr int vm_n(int R, int L, int e) { const int l = last_fill(R, L); return (l >= 0 ? n_lazy(R, l) : 0) + ops_between(R, l + 1, e); }
constexpr int done_slot(int R, int m) { int s = m / 12; while (!drain(R, s) && last_fill(R, 12 * s - 1) <= m && s < 40) ++s; return s + 1; }
constexpr int pub_at(int R, int s) {
  if (s == 6) return 1;
  if (R == 0) { for (int T = 0; T < 6; ++T) if (done_slot(0, st_step(T, 1)) == s) return T + 2; }
  else if (R == 1) { if (s == 17) return 2; if (done_slot(1, 12 * 21 + 10) == s) return 3; }
  else { for (int b = 0; b < 2; ++b) if (done_slot(R, 12 * (11 + 6 * b) + 10) == s) return b + 2; }
  return 0;
}
static_assert(done_slot(0, 12 * 31 + 8) <= 34, "schedule");
static_assert(pub_at(1, 24) == 3 && pub_at(2, 14) == 2 && pub_at(2, 19) == 3, "schedule");
static_assert(done_slot(0, st_step(0, 1)) <= 14 && done_slot(0, st_step(5, 1)) <= 29, "schedule");
constexpr unsigned chk_need(int R, int s) {
  if (R == 0) return (s >= 15 && s <= 30 && (s - 15) % 3 == 0) ? (s - 15) / 3 + 2 : 0;
  if (R == 1) return s == 26 ? 3 : s == 20 ? 2 : s == 22 ? 3 : s == 30 ? 4 : 0;
  return 0;
}
constexpr int u_hlp(int u) { return (u >= 6 && u <= 9) ? (u - 6) >> 1 : u >= 16 ? 2 : -1; }
constexpr int u_mem(int u) { return (u < 6 ? u : u - 10) >> 1; }
constexpr int u_blk(int u) { return u < 6 ? 0 : 1; }
constexpr unsigned F_FIN = 8;

struct Params {
  const u16 *x, *lre, *lim, *bre, *bim, *cre, *cim, *ldt, *dsk, *w1, *b1, *w2;
  char* ws;
  float* out;
};

template <int R>
DI void body(const Params& P, const unsigned lds0, const int c, const int j) {
  constexpr bool SSM = R != 0;
  constexpr int PSTR = R == 2 ? 192 : 64;
  char* const ws = P.ws;
  const int tid = threadIdx.x, lane = tid & 63;
  const int w = __builtin_amdgcn_readfirstlane(tid >> 6);
  const int rg = w & 3, set = w >> 2;
  const int r = lane & 31, h = lane >> 5;
  unsigned stampv = 0; (void)stampv;
  if constexpr (R == 0) asm volatile("fk_mlp_beg:");
  RSTAMP(60); STAMP(0);
  const int gq = SSM ? 4 : j / 3, mi = j - 3 * gq;

  char* const wc = ws + WS_C0 + (size_t)c * WS_CS;
  u32x4 xr[2][9];
  unsigned aux[3], epoch, tag;
  const int ww = (w + (SSM ? 2 * c : c)) & 7;
  const int rb0 = 8 * ((w + (SSM ? 4 * mi : 4 * gq)) & 15), krot = mi;
  const int rb1_ = rb0 ^ 64;
  const char* xb1 = (const char*)P.x + (size_t)(128 * c + rb1_) * (D * 2);
  const unsigned xoff = (lane >> 3) * (D * 2) + (lane & 7) * 16;
  const char* xb0 = (const char*)P.x + (size_t)(128 * c + rb0) * (D * 2);
  unsigned voffx = (lane >> 3) * (D * 2) + (((lane & 7) ^ ((4 * (w & 1) + (lane >> 4)) & 7)) * 16);
  auto dmaX = [&](int third, int i) INL {
    int kb = (i >> 1) + krot; kb = kb >= 3 ? kb - 3 : kb;
    gldsW(((i & 1) ? xb1 : xb0) + (3 * third + kb) * 128, voffx, lds0 + kb * 16384 + ((i & 1) ? rb1_ : rb0) * 128, X_OFF);
  };
  auto early_touch = [&](int half) INL {
    const unsigned l = lane_now();
    unsigned id = w * 16 + (l >> 2); id = id < 108 ? id : 107;
    glds4((const char*)P.w1 + (size_t)(36 * c) * (F * 2) + 768 * j + 384 * half, (id / 3) * (F * 2) + (id % 3) * 128 + (l & 3) * 4, lds0 + JUNK_OFF);
  };
  constexpr int NAUX = 4;
  if constexpr (SSM) {
    __builtin_amdgcn_s_setprio(2);
    static_for<0, 9>([&](auto I) INL { xr[0][I.value] = gload16<I.value * 128>(xb0, xoff); });
    epoch = gload4_sc1(ws + WS_EPOCH, 0u);
    const unsigned o = (R == 2 ? 128 + 192 * (w < 3 ? w : 0) + lane : 192 * mi + (tid < 192 ? tid : 0)) * 2;
    aux[0] = gload2(P.lre, o); aux[1] = gload2(P.lim, o); aux[2] = gload2(P.ldt, o);
  } else {
  }

  unsigned pvo = lane * 16;
  unsigned voff1, voff2;
  const char *wa[2], *wb[3][2], *acm[3];
  if constexpr (SSM) {
    voff1 = voff2 = voffx;
    wa[0] = (const char*)P.bre + (size_t)((R == 2 ? 128 : 192 * mi) + 8 * ww) * (D * 2);
    wa[1] = (const char*)P.bim + (size_t)((R == 2 ? 128 : 192 * mi) + 8 * ww) * (D * 2);
  } else {
    const unsigned swz = ((lane & 7) ^ (((lane >> 4) & 1) << 2)) * 16;
    voff1 = (lane >> 3) * (F * 2) + swz;
    voff2 = (lane >> 3) * (D * 2) + (((lane & 7) ^ (((lane >> 4) & 3) << 1)) * 16);
    wa[0] = wa[1] = (const char*)P.w1 + ((size_t)(8 * ww) * F + 384 * j) * 2;
  }
#pragma unroll
  for (int k = 0; k < 3; ++k) {
    const int mm = (mi + k >= 3) ? mi + k - 3 : mi + k;
    if constexpr (SSM) {
      wb[k][0] = (const char*)P.cre + (size_t)(192 * mi + 8 * ww) * (D * 2) + 384 * mm;
      wb[k][1] = (const char*)P.cim + (size_t)(192 * mi + 8 * ww) * (D * 2) + 384 * mm;
    } else {
      wb[k][0] = wb[k][1] = (const char*)P.w2 + ((size_t)(384 * (3 * gq + mm) + 8 * ww) * D + 192 * mi) * 2;
    }
    acm[k] = wc + C_ACT + ((3 * gq + mm) * 6) * 16384 + w * 1024;
  }
  const unsigned ldw = lds0 + ww * 1024, lda = lds0 + w * 1024;
  auto dmaW = [&](int s, int i) INL {
    const int dimm = (s & 3) * SLOT + i * 8192;
    const int u = s - 18;
    if constexpr (SSM) {
      if (s < 18) gldsW(wa[p1_tile(R, s) & 1] + (PSTR * (p1_tile(R, s) >> 1)) * (D * 2) + (192 * p1_kt(R, s) + 64 * i) * 2, voff1, ldw, dimm);
      else if (u_hlp(u) < 0) gldsW(wb[u_mem(u)][u & 1] + (64 * i) * (D * 2) + 128 * u_blk(u), voff2, ldw, dimm);
      else gldsW(wb[0][u & 1] - 384 * mi + (64 * i) * (D * 2) + 384 * u_hlp(u) + 256, voff2, ldw, dimm);
    } else {
      if (s < 18) gldsW(wa[0] + ((size_t)(192 * p1_kt(R, s) + 64 * i) * F + 64 * p1_tile(R, s)) * 2, voff1, ldw, dimm);
      else gldsW(wb[u % 3][0] + ((size_t)(64 * (u / 3)) * D + 64 * i) * 2, voff2, ldw, dimm);
    }
  };
  auto code_touch = [&](int lines_per_wave_log2, unsigned skip, unsigned dst) INL {
    unsigned long long pc; unsigned len;
    if constexpr (R == 2) asm volatile("s_getpc_b64 %0\n\ts_mov_b32 %1, fk_hlp_end-." : "=s"(pc), "=s"(len));
    else if constexpr (R == 1) asm volatile("s_getpc_b64 %0\n\ts_mov_b32 %1, fk_ssm_end-." : "=s"(pc), "=s"(len));
    else asm volatile("s_getpc_b64 %0\n\ts_mov_b32 %1, fk_mlp_end-." : "=s"(pc), "=s"(len));
    const unsigned co = SSM ? skip + (((unsigned)w << lines_per_wave_log2) + ((unsigned)lane_now() >> (6 - lines_per_wave_log2))) * 128
                            : skip + ((((blockIdx.x >> 3) & ((1u << (lines_per_wave_log2 - 1)) - 1)) * 8 + w) * 2 + ((unsigned)lane_now() >> 5)) * 128;
    glds4((const void*)pc, co < len ? co : 0u, dst);
  };
  const unsigned ximg = (lane >> 3) * 128 + (((lane & 7) ^ ((4 * (w & 1) + (lane >> 4)) & 7)) * 16);
  float rs[2], nb[2], mn[2];
  unsigned xw = lds0 + X_OFF + rb0 * 128 + ximg;
  auto xwrite1 = [&](int ps, int i) INL { lds_wr((ps ? xw ^ 8192 : xw) + (i % 3) * 16384, xr[ps][i]); };
  auto stats = [&](auto PI) INL {
    constexpr int ps = PI.value;
    f32x4 s1 = {}, s2 = {}, s3 = {};
    const unsigned selv = (((lane >> 4) & 3) == (lane & 3)) ? 0x3f803f80u : 0u;
    const u32x4 sel = {selv, selv, selv, selv};
    static_for<0, 9>([&](auto GI) INL {
      constexpr int i = GI.value;
      if constexpr (ps == 0) { xr[1][i] = gload16<i * 128>(xb1, xoff); if constexpr (i == 8) __builtin_amdgcn_s_setprio(0); wait1<9 + NAUX>(xr[0][i]); }
      else wait1<8 - i + 5>(xr[1][i]);
      const u32x2 t0 = {xr[ps][i][0], xr[ps][i][1]}, t1 = {xr[ps][i][2], xr[ps][i][3]};
      s2 = __builtin_amdgcn_mfma_f32_4x4x4bf16_1k(__builtin_bit_cast(s16x4, t0), __builtin_bit_cast(s16x4, t0), s2, 0, 0, 0);
      s3 = __builtin_amdgcn_mfma_f32_4x4x4bf16_1k(__builtin_bit_cast(s16x4, t1), __builtin_bit_cast(s16x4, t1), s3, 0, 0, 0);
      s1 = MFMA16(BF8(sel), BF8(xr[ps][i]), s1);
      SCHED();
    });
    if constexpr (ps == 0) {
      dmaW(0, 0); dmaW(0, 1); dmaW(0, 2);
      unsigned id = rg * 16 + (lane >> 2); id = id < 36 ? id : 35;
      glds4(wa[0] + (set ? wa[1] - wa[0] : 0) - 8 * ww * (D * 2) + c * 36 * 128, id * 128 + (lane & 3) * 4, lds0 + JUNK_OFF);
      code_touch(5, 0, lds0 + JUNK_OFF);
    }
    s2 += s3;
    const float e0 = s2[0], e1 = s2[1], e2 = s2[2], e3 = s2[3];
    const float lo2 = (lane & 1) ? e1 : e0, hi2 = (lane & 1) ? e3 : e2;
    float a2 = (lane & 2) ? hi2 : lo2;
    const float f0 = s1[0], f1 = s1[1], f2 = s1[2], f3 = s1[3];
    const float lo1 = (lane & 16) ? f1 : f0, hi1 = (lane & 16) ? f3 : f2;
    float a1 = (lane & 32) ? hi1 : lo1;
    a1 = dpp_add<0xB1>(a1); a2 = dpp_add<0xB1>(a2);
    a1 = dpp_add<0x4E>(a1); a2 = dpp_add<0x4E>(a2);
    a1 = dpp_add<0x141>(a1); a2 = dpp_add<0x141>(a2);
    const float m = a1 * (1.f / D);
    const float var = fmaxf(a2 * (1.f / D) - m * m, 0.f);
    rs[ps] = __builtin_amdgcn_rsqf(var + 1e-5f);
    nb[ps] = -m * rs[ps];
    mn[ps] = m;
    if constexpr (ps == 1) {
      opaque(epoch);
      tag = __builtin_amdgcn_readfirstlane(epoch) + 1u;
      if constexpr (R == 2) {
        gstore_sc1(wc + C_STAT + rb0 * 16, (lane >> 3) * 16, u32x4{__float_as_uint(mn[0]), tag, __float_as_uint(rs[0]), tag});
        gstore_sc1(wc + C_STAT + rb1_ * 16, (lane >> 3) * 16, u32x4{__float_as_uint(mn[1]), tag, __float_as_uint(rs[1]), tag});
      }
    }
#pragma unroll
    for (int i = 0; i < 3; ++i) { xr[ps][i] = norm16(xr[ps][i], rs[ps], nb[ps]); xwrite1(ps, i); }
  };
  if constexpr (!SSM) {
    __builtin_amdgcn_s_setprio(0);
    dmaW(1, 0); dmaW(1, 1); dmaW(1, 2);
    STAMP(1); STAMP(2);
  } else {
    stats(IC<0>{});
    STAMP(1);
    stats(IC<1>{});
    STAMP(2);
    dmaW(1, 0); dmaW(1, 1); dmaW(1, 2);
    opaque(aux[0]); opaque(aux[1]); opaque(aux[2]);
    if (w < 3) {
      const float lr = bflo(aux[0]), li = bflo(aux[1]);
      const float dt = __builtin_amdgcn_exp2f(bflo(aux[2]) * LOG2E);
      const float mag = __builtin_amdgcn_exp2f(dt * lr * LOG2E);
      const float rev = __builtin_amdgcn_fractf(dt * li * 0.15915494309189535f);
      f32x2 L = {mag * __builtin_amdgcn_cosf(rev), mag * __builtin_amdgcn_sinf(rev)};
      const float inv = 2.f / (lr * lr + li * li);
      const float nr = L[0] - 1.f, ni = L[1];
      const f32x2 c2 = {(nr * lr + ni * li) * inv, (ni * lr - nr * li) * inv};
      const f32x2 L16 = csq(csq(csq(csq(L))));
      const f32x2 L128 = csq(csq(csq(L16)));
      lds_wr8(lds0 + LT_OFF + tid * 8, L); lds_wr8(lds0 + LT_OFF + 1536 + tid * 8, c2);
      lds_wr8(lds0 + LT_OFF + 2 * 1536 + tid * 8, L16); lds_wr8(lds0 + LT_OFF + 3 * 1536 + tid * 8, L128);
    }
  }
  u32x2 flg;
  const int rperm = 16 * ((r >> 2) & 1) + (r & 3) + 4 * (r >> 3);
  bf16x8 xn[36];
  unsigned xn_ad[4];
  { const int row = 32 * rg + (SSM ? rperm : r); xn_ad[0] = lds0 + X_OFF + row * 128 + ((h ^ ((row >> 1) & 7)) * 16); }
  xn_ad[1] = xn_ad[0] ^ 32; xn_ad[2] = xn_ad[0] ^ 64; xn_ad[3] = xn_ad[0] ^ 96;
  auto xn_read = [&](int rd, int ks) INL { xn[12 * rd + ks] = BF8(lds_rd(xn_ad[ks & 3] + (ks >> 2) * 16384)); };

  const int G1 = (lane >> 4) & 1, q = (lane >> 2) & 3, p4 = lane & 3;
  unsigned rb1[4], rb2[2][2], ab[4];
  if constexpr (SSM) {
    const int row = 32 * set + r;
    rb1[0] = lds0 + row * 128 + ((h ^ ((row >> 1) & 7)) * 16);
    rb1[1] = rb1[0] ^ 32; rb1[2] = rb1[0] ^ 64; rb1[3] = rb1[0] ^ 96;
    opaque(rb1[0]); opaque(rb1[1]); opaque(rb1[2]); opaque(rb1[3]);
  } else {
    rb1[0] = lds0 + (8 * h + q) * 128 + (((set * 4 + G1 * 2 + (p4 >> 1)) ^ (((q >> 1) & 1) << 2)) * 16) + (p4 & 1) * 8;
    rb1[1] = rb1[0] + 2 * SLOT;
    opaque(rb1[0]); opaque(rb1[1]);
  }
  unsigned tabb = lds0 + TAB_OFF + (32 * set + r) * (SSM ? 8 : 16);
  opaque(tabb);
  unsigned tabh = tabb + h * 2048 + rg * 512;
  if constexpr (SSM) opaque(tabh);
  auto wfrag = [&](int n) INL {
    const int s = n / 12, ks = n % 12;
    if constexpr (SSM) {
      return BF8(lds_rd(rb1[ks & 3] + (s & 1) * SLOT + (ks >> 2) * 8192));
    } else {
      unsigned a = rb1[(s >> 1) & 1] + (s & 1) * SLOT + ks * 2048; return tr_frag(a, a + 512);
    }
  };
  auto w2half = [&](int g, int t) INL {
    const int s = 18 + g / 6;
    return __builtin_amdgcn_ds_read_tr16_b64_v4bf16((LAS bf16x4*)(rb2[0][(s >> 1) & 1] + (s & 1) * SLOT + g_cb(g) * 8192 + g_kk(g) * 4096 + t * 1024));
  };
  auto c2frag = [&](int g) INL { const int s = 18 + g / 6; return BF8(lds_rd(rb2[g_kk(g)][(s >> 1) & 1] + (s & 1) * SLOT + g_cb(g) * 8192)); };
  auto a2frag = [&](int e, int mt) INL {
    if constexpr (SSM) {
      const int v = (e & 1) ? e >> 1 : (e >> 1) - 1;
      const unsigned off = (v == 17 ? 3 : (v + 1) % 3) * 16384 + ((e & 1) ? 0 : 8192) + (mt >> 1) * 2048 + (mt & 1) * 512;
      return tr_frag(ab[2 * (mt & 1)] + off, ab[2 * (mt & 1) + 1] + off);
    } else {
      return BF8(lds_rd(ab[0] + ((e >> 1) & 3) * 16384 + (e & 1) * 8192 + (mt >> 1) * 1024 + (mt & 1) * 256));
    }
  };
  WAIT_L0();
  STAMP(3);
  WAIT_V(SSM ? 5 : 4);
  BAR();
  STAMP(4);

  f32x16 acc[4];
  constexpr int NA = KM + 1;
  u32x4 actp[2], gr[2];
  float tn, tr, tprev, tprev2;
  f32x2 Lam, Lx, ee, cc, cl, cq, g0, g1, g2;
  const unsigned long long gm0 = (c - 1 - 4 * rg >= 0 ? 0xffffffffull : 0ull) | (c - 3 - 4 * rg >= 0 ? 0xffffffff00000000ull : 0ull);
  const unsigned long long gm1 = (c - 2 - 4 * rg >= 0 ? 0xffffffffull : 0ull) | (c - 4 - 4 * rg >= 0 ? 0xffffffff00000000ull : 0ull);
  bf16x8 fr[4], a2[3][4];
  bf16x4 wl[3], wh[3];
  f32x4 o2[3][4];
  u32x2 fx[3], fdk[3], gc[2];
  u32x4 fst, corrA, corrB;
  float cs[8], rsl;
  const int w67 = (w + 2) >> 3, cu0 = (w << w67) * 512, cu1 = cu0 + (6 - 5 * w67) * 512;
  float sk[12], xf[12];
  unsigned sto;
  const f32x16 zero = {};
  const char* amine = wc + C_ACT + (j * 6) * 16384 + set * 8192 + rg * (SSM ? 2048 : 1024);
  const char* flagb = wc + C_FLAG + (3 * gq) * 128;
  fr[0] = wfrag(0); fr[1] = wfrag(1);
  if constexpr (!SSM || SPD == 3) fr[2] = wfrag(2);

  auto pstore = [&](int cb, int hh) INL {
    u32x4 v;
#pragma unroll
    for (int e = 0; e < 4; ++e) v[e] = pk2(o2[cb][2 * hh + (e >> 1)][2 * (e & 1)], o2[cb][2 * hh + (e >> 1)][2 * (e & 1) + 1]);
    const unsigned o = pvo ^ (rg * 64 + ((pvo >> 8) << 4));
    if (!SSM && 2 * set + hh == gq) lds_wr(lds0 + ARING + 32768 + cb * 4096 + rg * 1024 + o, v);
    else gstore_sc1(wc + C_PART + ((2 * set + hh) * 9 + 3 * mi + cb) * 20480 + gq * 4096 + rg * 1024, o, v);
  };
  const int fpk = (w >> 1) < 3 ? (gq + 1 + (w >> 1)) & 3 : 4;
  const char* ffl = wc + C_FLGW + (3 * fpk + mi) * 1024 + (4 * (gq >> 1) + 2 * (w & 1)) * 128;
  bool got = false;
  auto fin_ready = [&]() INL { return __builtin_amdgcn_ballot_w64(flg[1] != tag || flg[0] < F_FIN) == 0ull; };
  auto fin_fetch = [&]() INL {
    const char* src = wc + C_PART + (gq * 9 + 3 * mi) * 20480 + fpk * 4096 + (w & 1) * 2048;
#pragma unroll
    for (int i = 0; i < 6; ++i) gldsA(src + (i >> 1) * 20480 + (i & 1) * 1024, pvo, lds0 + (w >> 1) * 12288 + (w & 1) * 2048, (i >> 1) * 4096 + (i & 1) * 1024);
  };
  auto step = [&](auto NI) INL {
    constexpr int n = NI.value;
    constexpr int s = n / 12, ks = n % 12;
    if constexpr (R != 2 && n == (SSM ? 212 : 209)) {
      const int lane = lane_now(), h = lane >> 5, G1 = (lane >> 4) & 1, q = (lane >> 2) & 3, p4 = lane & 3;
      if constexpr (SSM) {
        const int nn = lane & 15, o = p4 >> 1;
        rb2[0][0] = lds0 + (16 * rg + nn) * 128 + (((lane >> 4) ^ (nn >> 1)) * 16);
        rb2[1][0] = rb2[0][0] ^ 64;
#pragma unroll
        for (int k = 0; k < 2; ++k) { rb2[k][1] = rb2[k][0] + 2 * SLOT; opaque(rb2[k][0]); opaque(rb2[k][1]); }
        ab[0] = lds0 + ARING + set * 4096 + o * 1024 + h * 256 + G1 * 128 + o * 64 + q * 16 + (p4 & 1) * 8;
        ab[1] = ab[0] ^ 64; ab[2] = ab[0] ^ 128; ab[3] = ab[0] ^ 192;
        opaque(ab[0]); opaque(ab[1]); opaque(ab[2]); opaque(ab[3]);
      } else {
        rb2[0][0] = lds0 + (16 * h + 4 * G1 + q) * 128 + ((rg ^ (2 * G1 + (q >> 1))) * 32) + p4 * 8;
        rb2[0][1] = rb2[0][0] + 2 * SLOT;
        ab[0] = lds0 + ARING + h * 4096 + set * 2048 + G1 * 512 + (lane & 15) * 16;
        opaque(rb2[0][0]); opaque(rb2[0][1]); opaque(ab[0]);
      }
    }
    if constexpr (SSM && n == 16 * 12) pvo = lane_now() * 16;
    if constexpr (SSM && ks == 12 - SPD && (s & 1) == 1 && s < (R == 1 ? 11 : 17)) {
#pragma unroll
      for (int k = 0; k < 4; ++k) { rb1[k] += (s & 2) ? -2 * SLOT : 2 * SLOT; opaque(rb1[k]); }
    }
    constexpr int ss = (R == 1 && s >= 16) ? s - 4 : s;
    constexpr int pb = (ss >= 6) ? (ss - 6) / 6 : 0, ph = (ss >= 6 && ss < (R == 1 ? 18 : 24)) ? (ss - 6) % 6 : -1;
    constexpr bool mm_on = R == 0 || (R == 1 ? (n < 144 || n >= 216) : n < 216);
    if constexpr (ks == 0) {
      constexpr int pub = pub_at(R, s);
#define EPOLL 0
      constexpr bool poll = chk_need(R, (SSM || !EPOLL) ? s : s + 1) != 0;
      if constexpr (poll) { if (w == 0) { const int l = lane_now(); flg = gload8_sc1(flagb, (l & 3) * 128); } }
      if constexpr (pub > 0) { if (w == 1) gstore8_sc1(flagb + mi * 128, (lane_now() & 15) * 8, u32x2{(unsigned)pub, tag}); }
      if constexpr (SSM && ph == 3) {
        const unsigned go = 3 * tabb - tabh;
        const char* gb = ws + WS_GRAN + ((R == 2 ? pb * 3 + 2 : mi * 3 + pb) * 16 + c - 1 - 4 * rg) * 1024 + rg * 512 - 2 * (lds0 + TAB_OFF);
        gr[0] = gload16_sc1(gb, go);
        gr[1] = gload16_sc1(gb - 1024, go);
      }
    }
    if constexpr (s < 3 * km(R)) {
      constexpr int kt = s / km(R), v = n - 12 * km(R) * kt;
      if constexpr (v == 0) { xn_read(kt, 0); xn_read(kt, 1); xn_read(kt, 2); xn_read(kt, 3); }
      if constexpr (SSM) {
        if constexpr (v >= 1 && v + 3 < 12) xn_read(kt, v + 3);
        constexpr int ch = v == 21 ? 5 : (v >= 12 && v <= 20 && v % 2 == 0) ? (v - 12) / 2 : -1;
        if constexpr (kt < 2 && ch >= 0) { xwrite1(ch / 3, 3 * (kt + 1) + ch % 3); SCHED(); }
      } else if constexpr (v >= 1 && v <= 4) { xn_read(kt, 2 * v + 2); xn_read(kt, 2 * v + 3); }
    }
    if constexpr (SSM && ks == 4 && (s == 8 || s == 11 || (R == 2 && (s == 14 || s == 17)))) {
      const int l = lane_now(), r = l & 31;
      const int row = 32 * rg + 16 * ((r >> 2) & 1) + (r & 3) + 4 * (r >> 3);
      const unsigned a = lds0 + X_OFF + 32768 + row * 128 + (((l >> 5) ^ ((row >> 1) & 7)) * 16);
      xn[32] = BF8(lds_rd(a)); xn[33] = BF8(lds_rd(a ^ 32)); xn[34] = BF8(lds_rd(a ^ 64)); xn[35] = BF8(lds_rd(a ^ 96));
    }
    if constexpr (op_w(R, n)) { if constexpr (s == 0) dmaW(2 + ks / 3, ks % 3); else dmaW(w_slot(R, s), ks / 3); }
    if constexpr (op_x(R, n)) dmaX(n / 36 + 1, x_piece(n));
    if constexpr (op_a(R, n)) {
      if constexpr (SSM) {
        constexpr int pc = a_code(n) & 1, v = (a_code(n) - pc) / 2, u = v + pc;
        gldsA((u_hlp(u) < 0 ? acm[u_hlp(u) < 0 ? u_mem(u) : 0] + (2 * u_blk(u) + (u & 1)) * 16384 : wc + C_ACT + (15 * 6 + 2 * u_hlp(u) + (u & 1)) * 16384 + w * 1024) + (pc ? 0 : 8192), pvo,
              lda, ARING + (v == 17 ? 3 : (v + 1) % 3) * 16384 + pc * 8192);
      } else {
        constexpr int u = s + 3 - 18;
        gldsA(acm[u % 3] + (u / 3) * 16384 + (ks / 3) * 8192, pvo, lda, ARING + (u & 3) * 16384 + (ks / 3) * 8192);
      }
    }
    if constexpr (op_e(R, n)) { if constexpr (n == CT0) code_touch(5, 0, lds0 + JUNK_OFF); else early_touch(n != ET1); }
    if constexpr (op_g(R, n)) {
      if constexpr (n == 79) fst = gload16_sc1(wc + C_STAT, (32 * rg) * 16 + (pvo & 496));
      else gc[n - 80] = gload8_sc1(ws + WS_CSUM + j * 8192 + (n == 80 ? cu0 : cu1), pvo >> 1);
    }
    if constexpr (op_cs(R, n)) {
      constexpr int i = cs_piece(n);
      const char* cb_ = (const char*)P.w1 + (size_t)(288 * ((c / 6) & 1) + 36 * w + 8 * i) * (F * 2) + 768 * j + (c % 6) * 128;
      if constexpr (i < 5) xr[0][i] = gload16<0>(cb_, ((pvo >> 7) & (i < 4 ? 7 : 3)) * (F * 2) + (pvo & 112));
      else if constexpr (i == 5) epoch = gload4_sc1(ws + WS_EPOCH, 0u);
      else aux[0] = gload2((const char*)P.b1 + 768 * j + (w < 6 ? w : 0) * 128, pvo >> 3);
    }
    if constexpr (op_ft(R, n)) gstore8_sc1(wc + C_FLGW + j * 1024 + w * 128, (lane_now() & 15) * 8, u32x2{0u, tag});
    if constexpr (op_cp(R, n)) code_touch(3, 3072, ldw + ((s + 3) & 3) * SLOT + 2 * 8192);
    if constexpr (op_pf(R, n)) {
      const unsigned l = lane_now();
      constexpr int sl = SSM ? (s < 3 ? s : s - 3) : (s < 6 ? s - 5 : s - 6);
      if constexpr (SSM) {
        constexpr unsigned lim = s < 3 ? 72 : 108;
        unsigned id = sl * 32 + rg * 8 + (l >> 3); id = id < lim ? id : lim - 1;
        const u16* mat = s < 3 ? (set ? P.bim : P.bre) : (set ? P.cim : P.cre);
        glds4((const char*)mat + (size_t)(192 * mi + (s < 3 ? 64 : 0)) * (D * 2) + c * lim * 128, id * 128 + (l & 7) * 4, lds0 + JUNK_OFF);
      } else if constexpr (s < 3) {
        unsigned id = sl * 64 + w * 8 + (l >> 3); id = id < 144 ? id : 143;
        glds4((const char*)P.w1 + (size_t)(36 * c) * (F * 2) + 768 * j + 256, (id >> 2) * (F * 2) + (id & 3) * 128 + (l & 7) * 4, lds0 + JUNK_OFF);
      } else {
        unsigned id = sl * 64 + w * 8 + (l >> 3); id = id < 216 ? id : 215;
        glds4((const char*)P.w2 + (size_t)(384 * j) * (D * 2) + c * 216 * 128, id * 128 + (l & 7) * 4, lds0 + JUNK_OFF);
      }
    }
    if constexpr (SSM && n + SPD < (R == 1 ? 144 : 216)) fr[(n + SPD) & 3] = wfrag(n + SPD);
    if constexpr (R == 0 && n + 3 < 216) fr[(n + 3) & 3] = wfrag(n + 3);
    if constexpr (R == 2) {
    } else if constexpr (SSM) {
      if constexpr (n == 213 || n == 214) { fr[n - 213] = c2frag(n - 213); a2[0][2 * (n - 213)] = a2frag(0, 2 * (n - 213)); a2[0][2 * (n - 213) + 1] = a2frag(0, 2 * (n - 213) + 1); }
    } else {
      if constexpr (n >= 210 && n < 214) a2[0][n - 210] = a2frag(0, n - 210);
      if constexpr (n == 212 || n == 214) wl[(n - 212) / 2] = w2half((n - 212) / 2, 0);
      if constexpr (n == 213 || n == 215) wh[(n - 212) / 2] = w2half((n - 212) / 2, 1);
      if constexpr (op_fo(R, n)) {
        constexpr int i = s - 25;
        const unsigned rw = w * 4 + (pvo >> 8), co = (pvo >> 1) & 120;
        if constexpr (i < 3) fx[i] = gload8((const char*)P.x + (size_t)(128 * c + 32 * gq) * (D * 2) + 384 * mi + i * 128, rw * (D * 2) + co);
        else if constexpr (i < 6) fdk[i - 3] = gload8((const char*)P.dsk + 384 * mi + (i - 3) * 128, co);
        else fst = gload16_sc1(wc + C_STAT, (32 * gq + rw) * 16);
      }
      if constexpr (n == 408) { pin2(fx[0]); pin2(fx[1]); pin2(fx[2]); pin2(fdk[0]); pin2(fdk[1]); pin2(fdk[2]); wait1<63>(fst); flg = gload8_sc1(ffl, pvo & 128); rsl = __uint_as_float(fst[2]) * LOG2E; tn = __uint_as_float(fst[2]) * (1.f / LOG2E); opaquef(rsl); opaquef(tn); }
      if constexpr (n >= 408 && n < 420) {
        constexpr int e = n - 408;
        const unsigned xv = fx[e >> 2][(e >> 1) & 1], dv = fdk[e >> 2][(e >> 1) & 1];
        xf[e] = (e & 1) ? bfhi(xv) : bflo(xv);
        sk[e] = (xf[e] - __uint_as_float(fst[0])) * tn * ((e & 1) ? bfhi(dv) : bflo(dv));
        opaquef(xf[e]); opaquef(sk[e]);
      }
#define FCHK 422
      if constexpr (n == FCHK) {
        SCHED();
        wait0_2(flg);
        got = fin_ready();
        if (got) fin_fetch();
      }
    }
    if constexpr (!mm_on) {
    } else if constexpr (s < 18) {
      constexpr int T = p1_tile(R, s), kt = p1_kt(R, s);
      if constexpr (SSM) { acc[T & 3] = MFMA32(xn[12 * kt + ks], fr[n & 3], (kt == 0 && ks == 0) ? zero : acc[T & 3]); opaque16(acc[T & 3]); }
      else { acc[T % NA] = MFMA32(fr[n & 3], xn[12 * kt + ks], (kt == 0 && ks == 0) ? zero : acc[T % NA]); opaque16(acc[T % NA]); }
      if constexpr (SSM && s < 2 * km(R)) {
        constexpr int v = n - 12 * km(R) * kt, q = km(R) == 3 ? 4 : 6;
        constexpr int d0 = (q * v + 4) / 5, d1 = (q * (v + 1) + 4) / 5;
        if constexpr (d0 < d1 && d0 < 24) { constexpr int ch = d0 >> 2; xr[ch / 3][3 * (kt + 1) + ch % 3][d0 & 3] = norm4(xr[ch / 3][3 * (kt + 1) + ch % 3][d0 & 3], rs[ch / 3], nb[ch / 3]); }
        if constexpr (d0 + 1 < d1 && d0 + 1 < 24) { constexpr int d = d0 + 1, ch = d >> 2; xr[ch / 3][3 * (kt + 1) + ch % 3][d & 3] = norm4(xr[ch / 3][3 * (kt + 1) + ch % 3][d & 3], rs[ch / 3], nb[ch / 3]); }
      }
    } else {
      constexpr int g = (n - 216) / 2, cb = g_cb(g), e = 2 * (s - 18) + g_kk(g), m0 = 2 * (n & 1);
      bf16x8 wf;
      if constexpr (SSM) wf = fr[g % 3]; else wf = __builtin_shufflevector(wl[g % 3], wh[g % 3], 0, 1, 2, 3, 4, 5, 6, 7);
      const f32x4 z4 = {};
      SCHED();
      o2[cb][m0] = MFMA16(wf, a2[e_buf(e)][m0], g < 3 ? z4 : o2[cb][m0]); opaque4(o2[cb][m0]);
      SCHED();
      if constexpr (g + 2 < 108) {
        if constexpr (SSM) { if constexpr ((n & 1) == 0) fr[(g + 2) % 3] = c2frag(g + 2); }
        else if constexpr (n & 1) wh[(g + 2) % 3] = w2half(g + 2, 1);
        else wl[(g + 2) % 3] = w2half(g + 2, 0);
      }
      SCHED();
      o2[cb][m0 + 1] = MFMA16(wf, a2[e_buf(e)][m0 + 1], g < 3 ? z4 : o2[cb][m0 + 1]); opaque4(o2[cb][m0 + 1]);
      SCHED();
      constexpr int en = (n - 216) / 6 + 1;
      if constexpr (n < 420 && ks % 6 < 4) a2[e_buf(en)][ks % 6] = a2frag(en, ks % 6);
      if constexpr (n == 412 || n == 413 || n == 418 || n == 419) a2[2][(n & 1) + 2 * (n >= 418)] = a2frag(35, (n & 1) + 2 * (n >= 418));
      if constexpr (n == 425 || n == 426 || n == 429 || n == 430) pstore((n - 425) / 4, (n - 425) & 1);
    }
    if constexpr (!SSM) {
      if constexpr (n >= 36 && n <= 53) {
        if constexpr (n == 36) { SCHED(); wait2<ops_between(0, 26, 36)>(xr[0][4], xr[0][0]); }
        if constexpr (n == 42) { SCHED(); wait1<ops_between(0, 29, 42)>(xr[0][1]); }
        if constexpr (n == 46) { SCHED(); wait1<ops_between(0, 32, 46)>(xr[0][2]); }
        if constexpr (n == 50) { SCHED(); wait1<ops_between(0, 34, 50)>(xr[0][3]); }
        if constexpr (n < 38) {
#pragma unroll
          for (int d = 2 * (n - 36); d < 2 * (n - 35); ++d) { const unsigned v = msel_u(xr[0][4][d], 0xffffffffull); cs[2 * d] = bflo(v); cs[2 * d + 1] = bfhi(v); opaquef(cs[2 * d]); opaquef(cs[2 * d + 1]); }
        } else {
          constexpr int d = n - 38;
          const unsigned v = xr[0][d >> 2][d & 3]; cs[2 * (d & 3)] += bflo(v); opaquef(cs[2 * (d & 3)]); cs[2 * (d & 3) + 1] += bfhi(v); opaquef(cs[2 * (d & 3) + 1]);
        }
      }
      if constexpr (n == 54) {
#pragma unroll
        for (int e = 0; e < 4; ++e) { auto t = __builtin_amdgcn_permlane32_swap(__float_as_uint(cs[e]), __float_as_uint(cs[e + 4]), false, false); cs[e] = __uint_as_float(t[0]) + __uint_as_float(t[1]); opaquef(cs[e]); }
      }
      if constexpr (n == 55) {
#pragma unroll
        for (int e = 0; e < 2; ++e) { auto t = __builtin_amdgcn_permlane16_swap(__float_as_uint(cs[e]), __float_as_uint(cs[e + 2]), false, false); cs[e] = dpp_add<0x128>(__uint_as_float(t[0]) + __uint_as_float(t[1])); opaquef(cs[e]); }
        const unsigned l = pvo >> 4;
        *(LAS float*)(lds0 + TAB_OFF + 8192 + w * 256 + (8 * (l & 7) + (l >> 3)) * 4) = bsel(cs[0], cs[1], 0xff00ff00ff00ff00ull);
      }
      if constexpr (n == 60) {
        opaque(epoch); opaque(aux[0]); tag = __builtin_amdgcn_readfirstlane(epoch) + 1u;
        if (w == 2) {
          float t = 0.f;
#pragma unroll
          for (int k = 0; k < 8; ++k) t += *(LAS float*)(lds0 + TAB_OFF + 8192 + k * 256 + (pvo >> 2));
          gstore8_sc1(ws + WS_CSUM + j * 8192 + c * 512, pvo >> 1, u32x2{__float_as_uint(t), tag});
        }
      }
      if constexpr (n == 91) {
        SCHED();
        waitg<ops_between(0, 82, 91)>(fst, gc[0], gc[1]);
        auto stale = [&]() INL { return __builtin_amdgcn_ballot_w64(((fst[1] ^ tag) | (fst[3] ^ tag) | (gc[0][1] ^ tag) | (gc[1][1] ^ tag)) != 0u) != 0ull; };
        if (__builtin_expect(stale(), 0)) {
          const char* gb = ws + WS_CSUM + j * 8192;
          int spins = 200000;
          do { reloadg(fst, gc[0], gc[1], wc + C_STAT + (32 * rg) * 16, gb + cu0, gb + cu1, pvo & 496, pvo >> 1); } while (stale() && --spins > 0);
        }
        const float Sv = __uint_as_float(gc[0][0]) + __uint_as_float(gc[1][0]);
        const unsigned Sh = pk2(Sv, Sv), Sl = pk2(Sv - bfhi(Sh), 0.f);
        lds_wr(lds0 + TAB_OFF + w * 1024 + pvo, u32x4{Sh, (Sl & 0xffffu) | (aux[0] << 16), aux[0], 0u});
      }
      if constexpr (n == 93) { tn = __builtin_amdgcn_rcpf(__uint_as_float(fst[2])); rsl = __uint_as_float(fst[2]) * LOG2E; opaquef(tn); opaquef(rsl); }
      if constexpr (n == 94) { const float mu = -__uint_as_float(fst[0]); gc[0][0] = pk2(mu, tn); gc[0][1] = pk2(mu - bflo(gc[0][0]), tn - bfhi(gc[0][0])); pin2(gc[0]); }
      if constexpr (n == 95) {
        const unsigned hi = gc[0][0], lo = gc[0][1];
        corrB = u32x4{msel_u((hi & 0xffffu) | (lo << 16), 0xffffffffull), msel_u(hi, 0xffffffffull), msel_u(lo >> 16, 0xffffffffull), msel_u(lo, 0ull)};
        wait1<63>(corrB);
      }
      if constexpr (corr_tile(n + (n == 96 ? 3 : 6)) >= (n == 96 ? 0 : 1)) corrA = lds_rd(tabb + corr_tile(n + (n == 96 ? 3 : 6)) * 1024);
      if constexpr (corr_tile(n) >= 0) { acc[corr_tile(n) % NA] = MFMA32(BF8(corrA), BF8(corrB), acc[corr_tile(n) % NA]); opaque16(acc[corr_tile(n) % NA]); }
      if constexpr (n >= EPI0 && n < EPI0 + 6 * EP) {
        constexpr int T = (n - EPI0) / EP, u = (n - EPI0) % EP, e = u >> 1;
        const f32x16& pa = acc[T % NA];
        if constexpr ((u & 1) == 0) {
          if constexpr (e < 16) { tn = __builtin_amdgcn_exp2f(pa[e] * rsl); opaquef(tn); }
          if constexpr (e >= 1) {
            const float a = __builtin_fmaf(-pa[e - 1], tr, pa[e - 1]);
            if constexpr (((e - 1) & 1) == 0) { tprev = a; opaquef(tprev); }
            else { unsigned pk = pk2(tprev, a); opaque(pk); actp[(e - 1) >> 3][((e - 1) >> 1) & 3] = pk; }
          }
        } else { tr = __builtin_amdgcn_rcpf(__builtin_fmaf(tn, __builtin_fmaf(tn, 0.5f, 1.f), 1.f)); opaquef(tr); }
      }
      if constexpr (op_st(0, n)) { constexpr int id = st_id(n); gstore_sc1(amine + (id >> 1) * 16384 + (id & 1) * 4096, pvo, actp[id & 1]); }
    } else if constexpr (ph >= 0) {
      f32x16& ur = acc[(2 * pb) & 3];
      f32x16& ui = acc[(2 * pb + 1) & 3];
      const unsigned lt = tabb + pb * 512;
      const unsigned wt = tabb + (WT_OFF - TAB_OFF) + 2048, gt = tabb + (GT_OFF - TAB_OFF);
      if constexpr (ph == 0) {
        if constexpr (ks == 0) { ee = cfma(Lam, f32x2{ur[0], ui[0]}, f32x2{ur[1], ui[1]}); opaquec(ee); }
        if constexpr (ks >= 1 && ks <= 9) {
          constexpr int t0 = 2 + ((ks - 1) * 14) / 9, t1 = 2 + (ks * 14) / 9;
          ee = cfma(Lam, ee, f32x2{ur[t0], ui[t0]});
          if constexpr (t0 + 1 < t1) ee = cfma(Lam, ee, f32x2{ur[t0 + 1], ui[t0 + 1]});
          opaquec(ee);
        }
        if constexpr (ks == 7) Lx = lds_rd8(lt + 2 * 1536);
        if constexpr (ks == 10) {
          const f32x2 elo = {up32(ee[0]), up32(ee[1])};
          lds_wr8(tabh + (WT_OFF - TAB_OFF), cfma_s(Lx, elo, ee));
        }
        if constexpr (ks == 11) { Lam = csq(Lx); opaquec(Lam); }
      }
      if constexpr (ph == 1) {
        if constexpr (ks == 0) { g0 = lds_rd8(wt); g1 = lds_rd8(wt + 512); g2 = lds_rd8(wt + 1024); ee = lds_rd8(wt + 1536); }
        if constexpr (ks == 2) {
          cl = f32x2{0.f, 0.f}; cq = f32x2{1.f, 0.f};
          if (rg > 0) { cl = g0; cq = Lam; }
          if (rg > 1) { cl = cfma_s(Lam, cl, g1); cq = cmul_s(Lam, Lam); }
          if (rg > 2) {
            cl = cfma_s(Lam, cl, g2); cq = cmul_s(Lam, cq);
            const f32x2 E = cfma_s(Lam, cl, ee);
            gstore_sc1(ws + WS_GRAN + ((R == 2 ? pb * 3 + 2 : mi * 3 + pb) * 16 + c) * 1024 - 2 * (lds0 + TAB_OFF), 2 * tabb,
                       u32x4{__float_as_uint(E[0]), tag, __float_as_uint(E[1]), tag});
          }
          opaquec(cl); opaquec(cq);
          g0 = lds_rd8(wt - 2048 + rg * 512); cc = lds_rd8(lt + 1536);
        }
        if constexpr (ks == 4) {
          const f32x2 a = cfma_s(Lx, cl, g0);
          cl = f32x2{usel(cl[0], a[0]), usel(cl[1], a[1])};
          opaquec(cl);
        }
        if constexpr (ks == 5) { const f32x2 a = cmul_s(Lx, cq); cq = f32x2{usel(cq[0], a[0]), usel(cq[1], a[1])}; opaquec(cq); }
        if constexpr (ks == 6) { cl = cmul_s(cc, cl); opaquec(cl); }
        if constexpr (ks == 7) { cq = cmul_s(cc, cq); opaquec(cq); }
      }
      if constexpr ((ph == 1 && ks >= 8) || ph == 2) {
        constexpr int t = (ph == 1) ? ks - 8 : ks + 4;
        const float t1 = cc[1] * ui[t], t2 = cc[1] * ur[t];
        float a = __builtin_fmaf(cc[0], ur[t], -t1), b = __builtin_fmaf(-cc[0], ui[t], -t2);
        opaquef(a); opaquef(b);
        ur[t] = a; ui[t] = b;
      }
      if constexpr (ph == 3) {
        if constexpr (ks == 4) cc = lds_rd8(lt + 3 * 1536);
        if constexpr (ks == 7) { Lx = csq(cc); opaquec(Lx); }
        if constexpr (ks == 9) {
          SCHED();
          wait2<ops_between(R, 12 * s, n)>(gr[0], gr[1]);
          auto stale = [&]() INL {
            const unsigned b0 = (gr[0][1] ^ tag) | (gr[0][3] ^ tag), b1 = (gr[1][1] ^ tag) | (gr[1][3] ^ tag);
            return ((__builtin_amdgcn_ballot_w64(b0 != 0u) & gm0) | (__builtin_amdgcn_ballot_w64(b1 != 0u) & gm1)) != 0ull;
          };
          if (__builtin_expect(stale(), 0)) {
            const unsigned go = 3 * tabb - tabh;
            const char* gb = ws + WS_GRAN + ((R == 2 ? pb * 3 + 2 : mi * 3 + pb) * 16 + c - 1 - 4 * rg) * 1024 + rg * 512 - 2 * (lds0 + TAB_OFF);
            int spins = 200000;
            do { reload2(gr[0], gr[1], gb, gb - 1024, go); } while (stale() && --spins > 0);
          }
          const f32x2 a0 = {msel(gr[0][0], gm0), msel(gr[0][2], gm0)}, a1 = {msel(gr[1][0], gm1), msel(gr[1][2], gm1)};
          ee = cfma_s(cc, a1, a0); opaquec(ee);
        }
        if constexpr (ks == 10) {
          const f32x2 lo = {up32(ee[0]), up32(ee[1])};
          lds_wr8(tabh + (GT_OFF - TAB_OFF), cfma_s(Lx, ee, lo));
        }
        if constexpr (ks == 11) { cc = csq(Lx); opaquec(cc); }
      }
      if constexpr (ph == 4 && ks <= 3) {
        if constexpr (ks == 0) { ee = lds_rd8(gt + 2048 + 1536); g0 = lds_rd8(gt + 2048 + 1024); g1 = lds_rd8(gt + 2048 + 512); g2 = lds_rd8(gt + 2048); Lam = lds_rd8(lt); }
        if constexpr (ks == 2) { ee = cfma(cc, cfma(cc, ee, g0), g1); opaquec(ee); }
        if constexpr (ks == 3) { ee = cfma(cq, cfma(cc, ee, g2), cl); ee[1] = -ee[1]; opaquec(ee); }
      }
      if constexpr ((ph == 4 && ks >= 4) || (ph == 5 && ks <= 7)) {
        constexpr int tt = (ph == 4) ? ks - 4 : ks + 8;
        ee = cfma(f32x2{Lam[0], -Lam[1]}, ee, f32x2{ur[tt], ui[tt]});
        opaquec(ee);
        if constexpr ((tt & 1) == 0) { tprev = ee[0]; tprev2 = ee[1]; }
        else {
          unsigned p0 = pk2(tprev, ee[0]), p1 = pk2(tprev2, ee[1]); opaque(p0); opaque(p1);
          if constexpr (tt < 8) { gr[0][tt >> 1] = p0; gr[1][tt >> 1] = p1; }
          else { actp[0][(tt >> 1) & 3] = p0; actp[1][(tt >> 1) & 3] = p1; }
        }
      }
      if constexpr (op_st(R, n)) {
        constexpr int o = (ks - 7) >> 1, part = (ks - 7) & 1;
        if constexpr (ks == 7) { const int l = lane_now(); sto = (l ^ (8 * (l >> 5))) * 16; }
        if constexpr (ks == 9) sto ^= 64;
        gstore_sc1(amine + (2 * pb + part) * 16384 + o * 1024, sto, o ? actp[part] : gr[part]);
      }
    }
    if constexpr (SSM && ks == 9 && (s == 5 || s == 11 || (R == 2 && s == 17))) Lam = lds_rd8(tabb + ((s - 5) / 6) * 512);
    if constexpr (n == 8) { SCHED(); WAIT_V(ops_between(R, 0, 8)); BAR(); }
    if constexpr (R == 1 && n == 212) {
      SCHED(); WAIT_V(0); BAR();
      if (w == 0) { const int l = lane_now(); flg = gload8_sc1(flagb, (l & 3) * 128); }
    }
    if constexpr (R == 1 && n == 221) {
      SCHED();
      if (w == 0) {
        wait0_2(flg);
        const int l = lane_now();
        int spins = 200000;
        while ((__builtin_amdgcn_ballot_w64(flg[1] != tag || flg[0] < 2u) & 7ull) != 0ull && --spins > 0) { flg = gload8_sc1(flagb, (l & 3) * 128); wait0_2(flg); }
      }
      BAR();
    }
    if constexpr (R == 1 && n == 233) { SCHED(); WAIT_V(vm_n(1, 222, 233) + 2); BAR(); }
    if constexpr (!SSM && n == 44) { SCHED(); BAR(); }
    if constexpr (ks == 11 && s < 34) {
      SCHED();
      if constexpr (SSM && (s == 1 || s == 3)) WAIT_L0();
      if constexpr (SSM && (ph == 0 || ph == 3 || s == 17)) WAIT_L0();
      else if constexpr (R == 1 && s >= 18) asm volatile("s_waitcnt lgkmcnt(1)" ::: "memory");
      WAIT_V(drain(R, s) ? 0 : (R == 1 && s == 16) ? ops_between(1, 192, n) : vm_n(R, s == 0 ? 2 : (R == 0 && (s == 1 || s == 4)) ? 12 * s - 4 : 12 * s - 1, n));
      if constexpr (chk_need(R, s) != 0) {
        if (w == 0) {
          constexpr unsigned need = chk_need(R, s);
          constexpr unsigned long long lanes = (R == 1 && s != 26) ? 8ull : 7ull;
          if constexpr (SSM || !EPOLL) WAIT_V(ops_between(R, 12 * s, n));
          pin2(flg);
          auto late = [&]() INL { return (__builtin_amdgcn_ballot_w64(flg[1] != tag || flg[0] < need) & lanes) != 0ull; };
          if (__builtin_expect(late(), 0)) {
            const int l = lane_now();
            int spins = 200000;
            do { flg = gload8_sc1(flagb, (l & 3) * 128); wait0_2(flg); } while (late() && --spins > 0);
          }
        }
      }
      BAR();
      STAMP(5 + s);
    }
    SCHED();
  };
  run_seq<0>(step, __make_integer_seq<ISeq, int, 144>{});
  if constexpr (R != 1) run_seq<144>(step, __make_integer_seq<ISeq, int, 48>{});
  else { STAMP(17); STAMP(18); STAMP(19); STAMP(20); }
  run_seq<192>(step, __make_integer_seq<ISeq, int, 24>{});
  run_seq<216>(step, __make_integer_seq<ISeq, int, R == 2 ? 71 : 216>{});
  if constexpr (R != 2) { pstore(2, 0); pstore(2, 1); }
  STAMP(39);
  WAIT_V(0);
  if constexpr (R == 2) { BAR(); if (w == 1) gstore8_sc1(flagb + mi * 128, (lane_now() & 15) * 8, u32x2{F_FIN, tag}); }
  else gstore8_sc1(wc + C_FLGW + j * 1024 + w * 128, (lane_now() & 15) * 8, u32x2{F_FIN, tag});
  STAMP(40);
  if constexpr (!SSM) {
    const int l = lane_now(), t = w * 64 + l;
    const int c4 = t & 15;
    const unsigned rz = lds0 + (c4 >> 2) * 1024 + (16 * (c4 & 3) + (((t >> 4) & 15) ^ c4)) * 16 + (t >> 8) * 8;
    const char* ob = (const char*)(P.out + (size_t)(128 * c + 32 * gq) * D + 192 * mi);
    const unsigned oo = (t >> 4) * (D * 4) + c4 * 16;
    if (!got) {
      int spins = 400000;
      do { flg = gload8_sc1(ffl, pvo & 128); wait0_2(flg); } while (!fin_ready() && --spins > 0);
      fin_fetch();
    }
    STAMP(41);
    STAMP(42);
    WAIT_L0();
    WAIT_V(0);
    STAMP(43);
    BAR();
    STAMP(44);
#pragma unroll
    for (int cb = 0; cb < 3; ++cb) {
      u32x2 pv[4];
#pragma unroll
      for (int k = 0; k < 4; ++k) pv[k] = *(LAS u32x2*)(rz + k * 12288 + cb * 4096);
      const u32x2 ov = *(LAS u32x2*)(rz + ARING + 32768 + cb * 4096);
      f32x4 o;
#pragma unroll
      for (int e = 0; e < 4; ++e) {
        auto up = [&](unsigned v) INL { return (e & 1) ? bfhi(v) : bflo(v); };
        const float z = ((up(ov[e >> 1]) + up(pv[0][e >> 1])) + (up(pv[1][e >> 1]) + up(pv[2][e >> 1]))) * rsl;
        const float gt = z * __builtin_amdgcn_rcpf(1.f + __builtin_amdgcn_exp2f(-z));
        o[e] = __builtin_fmaf(__builtin_fmaf(up(pv[3][e >> 1]), 0.693147180559945f, sk[4 * cb + e]), gt, xf[4 * cb + e]);
      }
      gstore_sc1(ob + 256 * cb, oo, __builtin_bit_cast(u32x4, o));
    }
    if (c == 15 && j == 0 && t == 0) *(unsigned*)(ws + WS_EPOCH) = tag;
  }
  if constexpr (R == 2) asm volatile("fk_hlp_end:"); else if constexpr (R == 1) asm volatile("fk_ssm_end:"); else asm volatile("fk_mlp_end:");
  if constexpr (SSM) { STAMP(41); STAMP(42); STAMP(43); STAMP(44); }
  STAMP(45);
  STAMP(46);
  RSTAMP(61);
  STAMP_OUT();
}

#if defined(__HIP_DEVICE_COMPILE__)
#define NOPK __attribute__((target("no-packed-fp32-ops")))
#else
#define NOPK
#endif
__global__ NOPK __launch_bounds__(512, 2) void main_kernel(const Params P) {
  __shared__ __attribute__((aligned(16))) char smem[163840];
  const unsigned lds0 = (unsigned)(size_t)(LAS char*)smem;
  const int b = blockIdx.x, xcd = b & 7, idx = b >> 3;
#define MAP 2
  int j, c;
  if (MAP == 0) { j = idx >> 1; c = 2 * xcd + (idx & 1); }
  else if (idx >= 24) { j = 12 + ((idx - 24) >> 1); c = 2 * xcd + (idx & 1); }
  else if (MAP == 1) { j = 6 * (xcd & 1) + (idx >> 2); c = 4 * (xcd >> 1) + (idx & 3); }
  else { j = 3 * (xcd & 3) + (idx >> 3); c = 8 * (xcd >> 2) + (idx & 7); }
#if defined(__HIP_DEVICE_COMPILE__)
  int jj = j;
  asm volatile("" : "+s"(jj));
  if (jj < 12) {
    const int lane = threadIdx.x & 63, w = __builtin_amdgcn_readfirstlane(threadIdx.x >> 6);
    const int gq = xcd & 3, mi = idx >> 3, rb0 = 8 * ((w + 4 * gq) & 15), ww = (w + c) & 7;
    const unsigned voffx = (lane >> 3) * (D * 2) + (((lane & 7) ^ ((4 * (w & 1) + (lane >> 4)) & 7)) * 16);
    const unsigned voff1 = (lane >> 3) * (F * 2) + (((lane & 7) ^ (((lane >> 4) & 1) << 2)) * 16);
    __builtin_amdgcn_s_setprio(2);
#pragma unroll
    for (int i = 0; i < 6; ++i) {
      int kb = (i >> 1) + mi; kb = kb >= 3 ? kb - 3 : kb;
      const int rb = (i & 1) ? rb0 ^ 64 : rb0;
      gldsW((const char*)P.x + (size_t)(128 * c + rb) * (D * 2) + kb * 128, voffx, lds0 + kb * 16384 + rb * 128, X_OFF);
    }
#pragma unroll
    for (int i = 0; i < 3; ++i) gldsW((const char*)P.w1 + ((size_t)(8 * ww + 64 * i) * F + 384 * j) * 2, voff1, lds0 + ww * 1024, i * 8192);
    unsigned long long pc; unsigned beg, end;
    asm volatile("s_getpc_b64 %0\nfk_entry:\n\ts_mov_b32 %1, fk_mlp_beg-fk_entry\n\ts_mov_b32 %2, fk_mlp_end-fk_entry" : "=s"(pc), "=s"(beg), "=s"(end));
    const unsigned co = beg + (((idx & 15) * 8 + w) * 2 + (lane >> 5)) * 128;
    glds4((const void*)pc, co < end ? co : 0u, lds0 + JUNK_OFF);
  }
  if (j < 12) body<0>(P, lds0, c, j);
  else if (j == 15) body<2>(P, lds0, c, j);
  else body<1>(P, lds0, c, j);
#endif
}

extern "C" void kernel_launch(void* const* d_in, const int* in_sizes, int n_in, void* d_out, int out_size, void* d_ws, size_t ws_size,
                              hipStream_t stream) {
  Params P;
  P.x = (const u16*)d_in[0]; P.lre = (const u16*)d_in[1]; P.lim = (const u16*)d_in[2];
  P.bre = (const u16*)d_in[3]; P.bim = (const u16*)d_in[4]; P.cre = (const u16*)d_in[5]; P.cim = (const u16*)d_in[6];
  P.ldt = (const u16*)d_in[7]; P.w1 = (const u16*)d_in[10]; P.b1 = (const u16*)d_in[11]; P.w2 = (const u16*)d_in[12];
  P.dsk = (const u16*)d_in[8]; P.ws = (char*)d_ws; P.out = (float*)d_out;
  main_kernel<<<256, 512, 0, stream>>>(P);
}
```

```cpp
#include <hip/hip_runtime.h>
#include <hip/hip_bf16.h>
#pragma clang diagnostic ignored "-Wint-to-pointer-cast"

typedef unsigned short u16;
typedef __attribute__((ext_vector_type(2))) __bf16 bf16x2;
typedef __attribute__((ext_vector_type(4))) __bf16 bf16x4;
typedef __attribute__((ext_vector_type(8))) __bf16 bf16x8;
typedef __attribute__((ext_vector_type(4))) short s16x4;
typedef __attribute__((ext_vector_type(2))) float f32x2;
typedef __attribute__((ext_vector_type(4))) float f32x4;
typedef __attribute__((ext_vector_type(16))) float f32x16;
typedef __attribute__((ext_vector_type(2))) unsigned u32x2;
typedef __attribute__((ext_vector_type(4))) unsigned u32x4;
#define LAS __attribute__((address_space(3)))
#define DI __device__ __forceinline__
#define INL __attribute__((always_inline))

constexpr int D = 576, F = 4608;
constexpr float LOG2E = 1.44269504088896f;
constexpr int SLOT = 24576;
constexpr int X_OFF = 4 * SLOT;
constexpr int ARING = X_OFF;
constexpr int TAB_OFF = ARING + 49152;
constexpr int LT_OFF = TAB_OFF;
constexpr int WT_OFF = TAB_OFF + 6144;
constexpr int JUNK_OFF = TAB_OFF + 14336;
constexpr int GT_OFF = TAB_OFF + 10240;
constexpr size_t WS_EPOCH = 0;
constexpr size_t WS_GRAN = 32768;
constexpr size_t WS_CSUM = 196608;
constexpr size_t WS_C0 = 327680, WS_CS = 2334720;
constexpr int C_FLAG = 0;
constexpr int C_FLGW = 8192;
constexpr int C_STAT = 4096;
constexpr int C_ACT = 24576;
constexpr int C_PART = C_ACT + 1572864;

DI float bflo(unsigned v) { return __uint_as_float(v << 16); }
DI float bfhi(unsigned v) { return __uint_as_float(v & 0xffff0000u); }
DI unsigned pk2(float a, float b) { f32x2 v = {a, b}; return __builtin_bit_cast(unsigned, __builtin_convertvector(v, bf16x2)); }
#define WAIT_V(n) asm volatile("s_waitcnt vmcnt(%0)" ::"n"(n) : "memory")
#define WAIT_L0() asm volatile("s_waitcnt lgkmcnt(0)" ::: "memory")
#define BAR() asm volatile("s_barrier" ::: "memory")
#define SCHED() __builtin_amdgcn_sched_barrier(0)
#define BF8(x) __builtin_bit_cast(bf16x8, (x))
#define MFMA16(a, b, c) __builtin_amdgcn_mfma_f32_16x16x32_bf16((a), (b), (c), 0, 0, 0)
#define MFMA32(a, b, c) __builtin_amdgcn_mfma_f32_32x32x16_bf16((a), (b), (c), 0, 0, 0)
template <int I> struct IC { static constexpr int value = I; };
template <int B, int E, class Fn> DI void static_for(Fn&& f) { if constexpr (B < E) { f(IC<B>{}); static_for<B + 1, E>(f); } }

template <class T, T... I> struct ISeq {};
template <int B, class Fn, int... I> DI void run_seq(Fn&& f, ISeq<int, I...>) { (f(IC<B + I>{}), ...); }

DI void gldsW(const void* sbase, unsigned voff, unsigned lds_base, int imm) {
  asm volatile("s_add_u32 m0, %2, %3\n\ts_nop 0\n\tglobal_load_lds_dwordx4 %0, %1" ::"v"(voff), "s"(sbase), "s"(lds_base), "n"(imm) : "memory", "scc");
}
DI void gldsA(const void* sbase, unsigned voff, unsigned lds_base, int imm) {
  asm volatile("s_add_u32 m0, %2, %3\n\ts_nop 0\n\tglobal_load_lds_dwordx4 %0, %1 sc1" ::"v"(voff), "s"(sbase), "s"(lds_base), "n"(imm) : "memory", "scc");
}
DI void glds4(const void* sbase, unsigned voff, unsigned lds_addr) {
  asm volatile("s_mov_b32 m0, %2\n\ts_nop 3\n\tglobal_load_lds_dword %0, %1" ::"v"(voff), "s"(sbase), "s"(lds_addr) : "memory");
}
DI void gstore_sc1(const void* sbase, unsigned voff, u32x4 v) {
  asm volatile("s_nop 4\n\tglobal_store_dwordx4 %0, %1, %2 sc1\n\ts_nop 1" ::"v"(voff), "v"(v), "s"(sbase) : "memory");
}
DI void gstore8_sc1(const void* sbase, unsigned voff, u32x2 v) {
  asm volatile("s_nop 4\n\tglobal_store_dwordx2 %0, %1, %2 sc1\n\ts_nop 1" ::"v"(voff), "v"(v), "s"(sbase) : "memory");
}
template <int OFF> DI u32x4 gload16(const void* sbase, unsigned voff) {
  u32x4 v; asm volatile("global_load_dwordx4 %0, %1, %2 offset:%3" : "=v"(v) : "v"(voff), "s"(sbase), "n"(OFF) : "memory"); return v;
}
DI u32x4 gload16_sc1(const void* sbase, unsigned voff) {
  u32x4 v; asm volatile("s_nop 4\n\tglobal_load_dwordx4 %0, %1, %2 sc1" : "=v"(v) : "v"(voff), "s"(sbase) : "memory"); return v;
}
DI u32x2 gload8_sc1(const void* sbase, unsigned voff) {
  u32x2 v; asm volatile("s_nop 4\n\tglobal_load_dwordx2 %0, %1, %2 sc1" : "=v"(v) : "v"(voff), "s"(sbase) : "memory"); return v;
}
DI u32x2 gload8(const void* sbase, unsigned voff) {
  u32x2 v; asm volatile("s_nop 4\n\tglobal_load_dwordx2 %0, %1, %2" : "=v"(v) : "v"(voff), "s"(sbase) : "memory"); return v;
}
DI unsigned gload4_sc1(const void* sbase, unsigned voff) {
  unsigned v; asm volatile("s_nop 4\n\tglobal_load_dword %0, %1, %2 sc1" : "=v"(v) : "v"(voff), "s"(sbase) : "memory"); return v;
}
DI unsigned gload2(const void* sbase, unsigned voff) {
  unsigned v; asm volatile("s_nop 4\n\tglobal_load_ushort %0, %1, %2" : "=v"(v) : "v"(voff), "s"(sbase) : "memory"); return v;
}
template <int N> DI void wait1(u32x4& a) { asm volatile("s_waitcnt vmcnt(%1)" : "+v"(a) : "n"(N) : "memory"); }
template <int N> DI void wait3(u32x4& a, u32x4& b, u32x4& c) { asm volatile("s_waitcnt vmcnt(%3)" : "+v"(a), "+v"(b), "+v"(c) : "n"(N) : "memory"); }
template <int N> DI void wait2(u32x4& a, u32x4& b) { asm volatile("s_waitcnt vmcnt(%2)" : "+v"(a), "+v"(b) : "n"(N) : "memory"); }
DI void reload2(u32x4& a, u32x4& b, const void* pa, const void* pb, unsigned voff) {
  asm volatile("s_nop 4\n\tglobal_load_dwordx4 %0, %2, %3 sc1\n\tglobal_load_dwordx4 %1, %2, %4 sc1\n\ts_waitcnt vmcnt(0)" : "+&v"(a), "+&v"(b) : "v"(voff), "s"(pa), "s"(pb) : "memory");
}
DI float msel(unsigned v, unsigned long long m) { float o; asm("v_cndmask_b32 %0, 0, %1, %2" : "=v"(o) : "v"(v), "s"(m)); return o; }
DI unsigned msel_u(unsigned v, unsigned long long m) { unsigned o; asm("v_cndmask_b32 %0, 0, %1, %2" : "=v"(o) : "v"(v), "s"(m)); return o; }
template <int N> DI void waitg(u32x4& a, u32x2& b, u32x2& c) { asm volatile("s_waitcnt vmcnt(%3)" : "+v"(a), "+v"(b), "+v"(c) : "n"(N) : "memory"); }
DI void reloadg(u32x4& a, u32x2& b, u32x2& c, const void* pa, const void* pb, const void* pc, unsigned oa, unsigned ob) {
  asm volatile("s_nop 4\n\tglobal_load_dwordx4 %0, %3, %5 sc1\n\tglobal_load_dwordx2 %1, %4, %6 sc1\n\tglobal_load_dwordx2 %2, %4, %7 sc1\n\ts_waitcnt vmcnt(0)"
               : "+&v"(a), "+&v"(b), "+&v"(c) : "v"(oa), "v"(ob), "s"(pa), "s"(pb), "s"(pc) : "memory");
}
DI void pin2(u32x2& a) { asm volatile("" : "+v"(a)::"memory"); }
DI void wait0_2(u32x2& a) { asm volatile("s_waitcnt vmcnt(0)" : "+v"(a)::"memory"); }
DI void opaque(unsigned& a) { asm volatile("" : "+v"(a)); }
DI void opaquef(float& a) { asm volatile("" : "+v"(a)); }
DI int lane_now() { int l; asm volatile("v_mbcnt_lo_u32_b32 %0, -1, 0\n\tv_mbcnt_hi_u32_b32 %0, -1, %0" : "=v"(l)); return l; }
DI u32x4 mask3(unsigned a, unsigned b, unsigned c, unsigned long long m) {
  unsigned o0, o1, o2, o3;
  asm volatile("v_cndmask_b32 %0, 0, %4, %7\n\tv_cndmask_b32 %1, 0, %5, %7\n\tv_cndmask_b32 %2, 0, %6, %7\n\tv_mov_b32 %3, 0\n\ts_nop 1"
               : "=&v"(o0), "=&v"(o1), "=&v"(o2), "=&v"(o3) : "v"(a), "v"(b), "v"(c), "s"(m));
  return u32x4{o0, o1, o2, o3};
}
template <bool FIRST> DI void mfma16_acc(f32x4& c, bf16x8 a, bf16x8 b) {
  if constexpr (FIRST) asm volatile("v_mfma_f32_16x16x32_bf16 %0, %1, %2, 0" : "=&v"(c) : "v"(a), "v"(b));
  else asm volatile("v_mfma_f32_16x16x32_bf16 %0, %1, %2, %0" : "+v"(c) : "v"(a), "v"(b));
}
DI void opaque4(f32x4& a) { asm volatile("" : "+v"(a)); }
DI void opaque16(f32x16& a) { asm volatile("" : "+v"(a)); }
DI void opaquec(f32x2& a) { float p = a[0], q = a[1]; asm volatile("" : "+v"(p), "+v"(q)); a[0] = p; a[1] = q; }
DI bf16x8 tr_frag(unsigned a0, unsigned a1) {
  bf16x4 lo = __builtin_amdgcn_ds_read_tr16_b64_v4bf16((LAS bf16x4*)a0);
  bf16x4 hi = __builtin_amdgcn_ds_read_tr16_b64_v4bf16((LAS bf16x4*)a1);
  return __builtin_shufflevector(lo, hi, 0, 1, 2, 3, 4, 5, 6, 7);
}
DI u32x4 lds_rd(unsigned a) { return *(LAS u32x4*)a; }
DI void lds_wr(unsigned a, u32x4 v) { *(LAS u32x4*)a = v; }
DI f32x2 lds_rd8(unsigned a) { return *(LAS f32x2*)a; }
DI void lds_wr8(unsigned a, f32x2 v) { *(LAS f32x2*)a = v; }
DI u32x4 norm16(u32x4 v, float a, float b) {
  unsigned a0 = v[0], a1 = v[1], a2 = v[2], a3 = v[3], t0, t1, t2, t3;
  asm("v_lshlrev_b32 %4, 16, %0\n\tv_lshlrev_b32 %5, 16, %1\n\tv_lshlrev_b32 %6, 16, %2\n\tv_lshlrev_b32 %7, 16, %3\n\t"
      "v_and_b32 %0, 0xffff0000, %0\n\tv_and_b32 %1, 0xffff0000, %1\n\tv_and_b32 %2, 0xffff0000, %2\n\tv_and_b32 %3, 0xffff0000, %3\n\t"
      "v_fma_f32 %4, %4, %8, %9\n\tv_fma_f32 %5, %5, %8, %9\n\tv_fma_f32 %6, %6, %8, %9\n\tv_fma_f32 %7, %7, %8, %9\n\t"
      "v_fma_f32 %0, %0, %8, %9\n\tv_fma_f32 %1, %1, %8, %9\n\tv_fma_f32 %2, %2, %8, %9\n\tv_fma_f32 %3, %3, %8, %9\n\t"
      "v_cvt_pk_bf16_f32 %0, %4, %0\n\tv_cvt_pk_bf16_f32 %1, %5, %1\n\tv_cvt_pk_bf16_f32 %2, %6, %2\n\tv_cvt_pk_bf16_f32 %3, %7, %3"
      : "+v"(a0), "+v"(a1), "+v"(a2), "+v"(a3), "=&v"(t0), "=&v"(t1), "=&v"(t2), "=&v"(t3) : "v"(a), "v"(b));
  return u32x4{a0, a1, a2, a3};
}
DI unsigned norm4(unsigned a, float sc, float bi) {
  unsigned t;
  asm volatile("v_lshlrev_b32 %1, 16, %0\n\tv_and_b32 %0, 0xffff0000, %0\n\tv_fma_f32 %1, %1, %2, %3\n\tv_fma_f32 %0, %0, %2, %3\n\tv_cvt_pk_bf16_f32 %0, %1, %0"
               : "+v"(a), "=&v"(t) : "v"(sc), "v"(bi));
  return a;
}
template <int CTRL> DI float dpp_add(float a) {
  return a + __builtin_bit_cast(float, __builtin_amdgcn_update_dpp(0, __builtin_bit_cast(int, a), CTRL, 0xf, 0xf, true));
}
DI f32x2 cfma(f32x2 a, f32x2 b, f32x2 c) {
  f32x2 o;
  o[0] = __builtin_fmaf(a[0], b[0], __builtin_fmaf(-a[1], b[1], c[0]));
  o[1] = __builtin_fmaf(a[0], b[1], __builtin_fmaf(a[1], b[0], c[1]));
  return o;
}
DI f32x2 cfma_s(f32x2 a, f32x2 b, f32x2 c) {
  float o0, o1;
  asm("v_fma_f32 %0, -%3, %5, %6\n\tv_fma_f32 %1, %3, %4, %7\n\tv_fmac_f32 %0, %2, %4\n\tv_fmac_f32 %1, %2, %5"
      : "=&v"(o0), "=&v"(o1) : "v"(a[0]), "v"(a[1]), "v"(b[0]), "v"(b[1]), "v"(c[0]), "v"(c[1]));
  return f32x2{o0, o1};
}
DI f32x2 cmul_s(f32x2 a, f32x2 b) {
  float o0, o1;
  asm("v_mul_f32_e64 %0, -%3, %5\n\tv_mul_f32 %1, %3, %4\n\tv_fmac_f32 %0, %2, %4\n\tv_fmac_f32 %1, %2, %5"
      : "=&v"(o0), "=&v"(o1) : "v"(a[0]), "v"(a[1]), "v"(b[0]), "v"(b[1]));
  return f32x2{o0, o1};
}
DI float usel(float lo, float hi) { float o; asm("v_cndmask_b32 %0, %1, %2, %3" : "=v"(o) : "v"(lo), "v"(hi), "s"(0xffffffff00000000ull)); return o; }
DI f32x2 csq(f32x2 a) { f32x2 o; o[0] = __builtin_fmaf(a[0], a[0], -a[1] * a[1]); o[1] = 2.f * a[0] * a[1]; return o; }
DI float up32(float v) {
  auto t = __builtin_amdgcn_permlane32_swap(0u, __float_as_uint(v), false, false);
  return __uint_as_float(t[0]);
}

constexpr int KM = 3;
constexpr int SPD = 2;
constexpr int km(int R) { return R == 0 ? KM : 2; }
constexpr int p1_tile(int R, int s) { return s < 3 * km(R) ? s % km(R) : s / 3; }
constexpr int p1_kt(int R, int s) { return s < 3 * km(R) ? s / km(R) : s % 3; }
constexpr int g_cb(int g) { return g >= 102 ? (g - 102) / 2 : g % 3; }
constexpr int g_kk(int g) { return g >= 102 ? g & 1 : (g % 6) / 3; }
constexpr int e_buf(int e) { return e == 35 ? 2 : e & 1; }
constexpr int EPS = 7, AXS = 8, CSA = 40;
constexpr int EPI0 = 101;
constexpr int corr_tile(int n) { return n == 98 ? 0 : n == 106 ? 1 : n == 114 ? 2 : n == 145 ? 3 : n == 181 ? 4 : n == 217 ? 5 : -1; }
constexpr int EP = 33;
constexpr int st_step(int T, int o) { int r = EPI0 + EP * T + (o ? 33 : 17); while (r % 12 != 1 && r % 12 != 4 && r % 12 != 7 && r % 12 != 9) ++r; return r; }
constexpr int st_id(int n) { for (int i = 0; i < 12; ++i) if (st_step(i >> 1, i & 1) == n) return i; return -1; }
constexpr bool op_w(int R, int n) {
  const int s = n / 12, k = n % 12;
  if ((R == 1 && s >= 12 && s <= 18 && s != 16) || (R == 2 && s >= 15)) return false;
  return s == 0 ? k < 6 : (s <= 32 && k % 3 == 0 && k <= 6);
}
constexpr int w_slot(int R, int s) { return R == 1 && s >= 9 && s <= 11 ? (s == 11 ? 18 : s + 11) : s + 3; }
constexpr int x_piece(int n) { const int t = n % 36; return n >= 72 ? -1 : t == 9 ? 0 : t == 11 ? 1 : t == 14 ? 2 : t == 17 ? 3 : t == 20 ? 4 : t == 22 ? 5 : -1; }
constexpr bool op_x(int R, int n) { return R == 0 && x_piece(n) >= 0; }
constexpr int a_code(int n) {
  const int s = n / 12, k = n % 12;
  if (s == 17) return k < 4 ? k - 1 : -9;
  if (s == 18) return (k >= 6 && k <= 8) ? k - 3 : -9;
  if (n == 12 * 32 + 4) return 34;
  return (s >= 19 && s <= 32 && (k == 2 || k == 5)) ? 2 * (s - 16) + (k == 5) : -9;
}
constexpr bool op_a(int R, int n) {
  const int s = n / 12, k = n % 12;
  if (R == 2) return false;
  if (R == 1) return a_code(n) != -9;
  return (k == 2 || k == 5) && s >= 15 && s <= 32;
}
constexpr bool op_pf(int R, int n) {
  return n % 12 == 8 && (R == 1 ? n / 12 <= 6 : R == 0 && n / 12 >= 5 && n / 12 <= 9 && n / 12 != 6);
}
constexpr bool op_cp(int R, int n) { const int s = n / 12; return n % 12 == 5 && s >= 7 && s <= 28 && s % 7 == 0 && !(R == 1 && s == 14); }
constexpr bool op_st(int R, int n) {
  const int s = n / 12, k = n % 12;
  if (R) return (s == 11 || (R == 1 ? s == 21 : s == 17 || s == 23)) && k >= 7 && k <= 10;
  return st_id(n) >= 0;
}
constexpr int cs_piece(int n) { return n == 23 ? 4 : n == 25 ? 0 : n == 28 ? 1 : n == 31 ? 2 : n == 33 ? 3 : n == EPS ? 5 : n == AXS ? 6 : -1; }
constexpr int op_g(int R, int n) { return R == 0 && n >= 79 && n <= 81; }
constexpr bool op_cs(int R, int n) { return R == 0 && cs_piece(n) >= 0; }
constexpr bool op_ft(int R, int n) { return R != 2 && n == 12 * 6 + 10; }
constexpr bool op_fo(int R, int n) { return R == 0 && n % 12 == 8 && n / 12 >= 25 && n / 12 <= 31; }
constexpr bool drain(int R, int s) { return R == 2 && s == 18; }
constexpr int n_fill(int R, int n) { return op_w(R, n) + op_x(R, n) + op_a(R, n); }
constexpr int n_lazy(int R, int n) { return op_pf(R, n) + op_cp(R, n) + op_st(R, n) + op_fo(R, n) + op_cs(R, n) + op_g(R, n) + op_ft(R, n); }
constexpr int last_fill(int R, int L) { while (L >= 0 && n_fill(R, L) == 0) --L; return L; }
constexpr int ops_between(int R, int a, int e) { int n = 0; for (int i = a; i <= e; ++i) n += n_fill(R, i) + n_lazy(R, i); return n; }
constexpr int vm_n(int R, int L, int e) { const int l = last_fill(R, L); return (l >= 0 ? n_lazy(R, l) : 0) + ops_between(R, l + 1, e); }
constexpr int done_slot(int R, int m) { int s = m / 12; while (!drain(R, s) && last_fill(R, 12 * s - 1) <= m && s < 40) ++s; return s + 1; }
constexpr int pub_at(int R, int s) {
  if (s == 6) return 1;
  if (R == 0) { for (int T = 0; T < 6; ++T) if (done_slot(0, st_step(T, 1)) == s) return T + 2; }
  else if (R == 1) { if (s == 17) return 2; if (done_slot(1, 12 * 21 + 10) == s) return 3; }
  else { for (int b = 0; b < 2; ++b) if (done_slot(R, 12 * (11 + 6 * b) + 10) == s) return b + 2; }
  return 0;
}
static_assert(done_slot(0, 12 * 31 + 8) <= 34, "schedule");
static_assert(pub_at(1, 24) == 3 && pub_at(2, 14) == 2 && pub_at(2, 19) == 3, "schedule");
static_assert(done_slot(0, st_step(0, 1)) <= 14 && done_slot(0, st_step(5, 1)) <= 29, "schedule");
constexpr unsigned chk_need(int R, int s) {
  if (R == 0) return (s >= 15 && s <= 30 && (s - 15) % 3 == 0) ? (s - 15) / 3 + 2 : 0;
  if (R == 1) return s == 26 ? 3 : s == 20 ? 2 : s == 22 ? 3 : s == 30 ? 4 : 0;
  return 0;
}
constexpr int u_hlp(int u) { return (u >= 6 && u <= 9) ? (u - 6) >> 1 : u >= 16 ? 2 : -1; }
constexpr int u_mem(int u) { return (u < 6 ? u : u - 10) >> 1; }
constexpr int u_blk(int u) { return u < 6 ? 0 : 1; }
constexpr unsigned F_FIN = 8;

struct Params {
  const u16 *x, *lre, *lim, *bre, *bim, *cre, *cim, *ldt, *dsk, *w1, *b1, *w2;
  char* ws;
  float* out;
};

template <int R>
DI void body(const Params& P, const unsigned lds0, const int c, const int j) {
  constexpr bool SSM = R != 0;
  constexpr int PSTR = R == 2 ? 192 : 64;
  char* const ws = P.ws;
  const int tid = threadIdx.x, lane = tid & 63;
  const int w = __builtin_amdgcn_readfirstlane(tid >> 6);
  const int rg = w & 3, set = w >> 2;
  const int r = lane & 31, h = lane >> 5;
  if constexpr (R == 0) asm volatile("fk_mlp_beg:");
  const int gq = SSM ? 4 : j / 3, mi = j - 3 * gq;

  char* const wc = ws + WS_C0 + (size_t)c * WS_CS;
  u32x4 xr[2][9];
  unsigned aux[3], epoch, tag;
  const int ww = (w + (SSM ? 2 * c : c)) & 7;
  const int rb0 = 8 * ((w + (SSM ? 4 * mi : 4 * gq)) & 15), krot = mi;
  const int rb1_ = rb0 ^ 64;
  const char* xb1 = (const char*)P.x + (size_t)(128 * c + rb1_) * (D * 2);
  const unsigned xoff = (lane >> 3) * (D * 2) + (lane & 7) * 16;
  const char* xb0 = (const char*)P.x + (size_t)(128 * c + rb0) * (D * 2);
  unsigned voffx = (lane >> 3) * (D * 2) + (((lane & 7) ^ ((4 * (w & 1) + (lane >> 4)) & 7)) * 16);
  auto dmaX = [&](int third, int i) INL {
    int kb = (i >> 1) + krot; kb = kb >= 3 ? kb - 3 : kb;
    gldsW(((i & 1) ? xb1 : xb0) + (3 * third + kb) * 128, voffx, lds0 + kb * 16384 + ((i & 1) ? rb1_ : rb0) * 128, X_OFF);
  };
  constexpr int NAUX = 4;
  if constexpr (SSM) {
    __builtin_amdgcn_s_setprio(2);
    static_for<0, 9>([&](auto I) INL { xr[0][I.value] = gload16<I.value * 128>(xb0, xoff); });
    epoch = gload4_sc1(ws + WS_EPOCH, 0u);
    const unsigned o = (R == 2 ? 128 + 192 * (w < 3 ? w : 0) + lane : 192 * mi + (tid < 192 ? tid : 0)) * 2;
    aux[0] = gload2(P.lre, o); aux[1] = gload2(P.lim, o); aux[2] = gload2(P.ldt, o);
  }

  unsigned pvo = lane * 16;
  unsigned voff1, voff2;
  const char *wa[2], *wb[3][2], *acm[3];
  if constexpr (SSM) {
    voff1 = voff2 = voffx;
    wa[0] = (const char*)P.bre + (size_t)((R == 2 ? 128 : 192 * mi) + 8 * ww) * (D * 2);
    wa[1] = (const char*)P.bim + (size_t)((R == 2 ? 128 : 192 * mi) + 8 * ww) * (D * 2);
  } else {
    voff1 = (lane >> 3) * (F * 2) + (((lane & 7) ^ ((((lane >> 4) & 1) | ((ww & 1) << 1)) << 1)) * 16);
    voff2 = (lane >> 3) * (D * 2) + (((lane & 7) ^ (((lane >> 4) & 3) << 1)) * 16);
    wa[0] = wa[1] = (const char*)P.w1 + ((size_t)(8 * ww) * F + 384 * j) * 2;
  }
#pragma unroll
  for (int k = 0; k < 3; ++k) {
    const int mm = (mi + k >= 3) ? mi + k - 3 : mi + k;
    if constexpr (SSM) {
      wb[k][0] = (const char*)P.cre + (size_t)(192 * mi + 8 * ww) * (D * 2) + 384 * mm;
      wb[k][1] = (const char*)P.cim + (size_t)(192 * mi + 8 * ww) * (D * 2) + 384 * mm;
    } else {
      wb[k][0] = wb[k][1] = (const char*)P.w2 + ((size_t)(384 * (3 * gq + mm) + 8 * ww) * D + 192 * mi) * 2;
    }
    acm[k] = wc + C_ACT + ((3 * gq + mm) * 6) * 16384 + w * 1024;
  }
  const unsigned ldw = lds0 + ww * 1024, lda = lds0 + w * 1024;
  auto dmaW = [&](int s, int i) INL {
    const int dimm = (s & 3) * SLOT + i * 8192;
    const int u = s - 18;
    if constexpr (SSM) {
      if (s < 18) gldsW(wa[p1_tile(R, s) & 1] + (PSTR * (p1_tile(R, s) >> 1)) * (D * 2) + (192 * p1_kt(R, s) + 64 * i) * 2, voff1, ldw, dimm);
      else if (u_hlp(u) < 0) gldsW(wb[u_mem(u)][u & 1] + (64 * i) * (D * 2) + 128 * u_blk(u), voff2, ldw, dimm);
      else gldsW(wb[0][u & 1] - 384 * mi + (64 * i) * (D * 2) + 384 * u_hlp(u) + 256, voff2, ldw, dimm);
    } else {
      if (s < 18) gldsW(wa[0] + ((size_t)(192 * p1_kt(R, s) + 64 * i) * F + 64 * p1_tile(R, s)) * 2, voff1, ldw, dimm);
      else gldsW(wb[u % 3][0] + ((size_t)(64 * (u / 3)) * D + 64 * i) * 2, voff2, ldw, dimm);
    }
  };
  auto code_touch = [&](int lines_per_wave_log2, unsigned skip, unsigned dst) INL {
    unsigned long long pc; unsigned len;
    if constexpr (R == 2) asm volatile("s_getpc_b64 %0\n\ts_mov_b32 %1, fk_hlp_end-." : "=s"(pc), "=s"(len));
    else if constexpr (R == 1) asm volatile("s_getpc_b64 %0\n\ts_mov_b32 %1, fk_ssm_end-." : "=s"(pc), "=s"(len));
    else asm volatile("s_getpc_b64 %0\n\ts_mov_b32 %1, fk_mlp_end-." : "=s"(pc), "=s"(len));
    const unsigned co = SSM ? skip + (((unsigned)w << lines_per_wave_log2) + ((unsigned)lane_now() >> (6 - lines_per_wave_log2))) * 128
                            : skip + ((((blockIdx.x >> 3) & ((1u << (lines_per_wave_log2 - 1)) - 1)) * 8 + w) * 2 + ((unsigned)lane_now() >> 5)) * 128;
    glds4((const void*)pc, co < len ? co : 0u, dst);
  };
  const unsigned ximg = (lane >> 3) * 128 + (((lane & 7) ^ ((4 * (w & 1) + (lane >> 4)) & 7)) * 16);
  float rs[2], nb[2], mn[2];
  unsigned xw = lds0 + X_OFF + rb0 * 128 + ximg;
  auto xwrite1 = [&](int ps, int i) INL { lds_wr((ps ? xw ^ 8192 : xw) + (i % 3) * 16384, xr[ps][i]); };
  auto stats = [&](auto PI) INL {
    constexpr int ps = PI.value;
    f32x4 s1 = {}, s2 = {}, s3 = {};
    const unsigned selv = (((lane >> 4) & 3) == (lane & 3)) ? 0x3f803f80u : 0u;
    const u32x4 sel = {selv, selv, selv, selv};
    static_for<0, 9>([&](auto GI) INL {
      constexpr int i = GI.value;
      if constexpr (ps == 0) { xr[1][i] = gload16<i * 128>(xb1, xoff); if constexpr (i == 8) __builtin_amdgcn_s_setprio(0); wait1<9 + NAUX>(xr[0][i]); }
      else wait1<8 - i + 5>(xr[1][i]);
      const u32x2 t0 = {xr[ps][i][0], xr[ps][i][1]}, t1 = {xr[ps][i][2], xr[ps][i][3]};
      s2 = __builtin_amdgcn_mfma_f32_4x4x4bf16_1k(__builtin_bit_cast(s16x4, t0), __builtin_bit_cast(s16x4, t0), s2, 0, 0, 0);
      s3 = __builtin_amdgcn_mfma_f32_4x4x4bf16_1k(__builtin_bit_cast(s16x4, t1), __builtin_bit_cast(s16x4, t1), s3, 0, 0, 0);
      s1 = MFMA16(BF8(sel), BF8(xr[ps][i]), s1);
      SCHED();
    });
    if constexpr (ps == 0) {
      dmaW(0, 0); dmaW(0, 1); dmaW(0, 2);
      unsigned id = rg * 16 + (lane >> 2); id = id < 36 ? id : 35;
      glds4(wa[0] + (set ? wa[1] - wa[0] : 0) - 8 * ww * (D * 2) + c * 36 * 128, id * 128 + (lane & 3) * 4, lds0 + JUNK_OFF);
      code_touch(5, 0, lds0 + JUNK_OFF);
    }
    s2 += s3;
    const float e0 = s2[0], e1 = s2[1], e2 = s2[2], e3 = s2[3];
    const float lo2 = (lane & 1) ? e1 : e0, hi2 = (lane & 1) ? e3 : e2;
    float a2 = (lane & 2) ? hi2 : lo2;
    const float f0 = s1[0], f1 = s1[1], f2 = s1[2], f3 = s1[3];
    const float lo1 = (lane & 16) ? f1 : f0, hi1 = (lane & 16) ? f3 : f2;
    float a1 = (lane & 32) ? hi1 : lo1;
    a1 = dpp_add<0xB1>(a1); a2 = dpp_add<0xB1>(a2);
    a1 = dpp_add<0x4E>(a1); a2 = dpp_add<0x4E>(a2);
    a1 = dpp_add<0x141>(a1); a2 = dpp_add<0x141>(a2);
    const float m = a1 * (1.f / D);
    const float var = fmaxf(a2 * (1.f / D) - m * m, 0.f);
    rs[ps] = __builtin_amdgcn_rsqf(var + 1e-5f);
    nb[ps] = -m * rs[ps];
    mn[ps] = m;
    if constexpr (ps == 1) {
      opaque(epoch);
      tag = __builtin_amdgcn_readfirstlane(epoch) + 1u;
      if constexpr (R == 2) {
        gstore_sc1(wc + C_STAT + rb0 * 16, (lane >> 3) * 16, u32x4{__float_as_uint(mn[0]), tag, __float_as_uint(rs[0]), tag});
        gstore_sc1(wc + C_STAT + rb1_ * 16, (lane >> 3) * 16, u32x4{__float_as_uint(mn[1]), tag, __float_as_uint(rs[1]), tag});
      }
    }
#pragma unroll
    for (int i = 0; i < 3; ++i) { xr[ps][i] = norm16(xr[ps][i], rs[ps], nb[ps]); xwrite1(ps, i); }
  };
  if constexpr (!SSM) {
    __builtin_amdgcn_s_setprio(0);
    dmaW(1, 0); dmaW(1, 1); dmaW(1, 2);
  } else {
    stats(IC<0>{});
    stats(IC<1>{});
    dmaW(1, 0); dmaW(1, 1); dmaW(1, 2);
    opaque(aux[0]); opaque(aux[1]); opaque(aux[2]);
    if (w < 3) {
      const float lr = bflo(aux[0]), li = bflo(aux[1]);
      const float dt = __builtin_amdgcn_exp2f(bflo(aux[2]) * LOG2E);
      const float mag = __builtin_amdgcn_exp2f(dt * lr * LOG2E);
      const float rev = __builtin_amdgcn_fractf(dt * li * 0.15915494309189535f);
      f32x2 L = {mag * __builtin_amdgcn_cosf(rev), mag * __builtin_amdgcn_sinf(rev)};
      const float inv = 2.f / (lr * lr + li * li);
      const float nr = L[0] - 1.f, ni = L[1];
      const f32x2 c2 = {(nr * lr + ni * li) * inv, (ni * lr - nr * li) * inv};
      const f32x2 L16 = csq(csq(csq(csq(L))));
      const f32x2 L128 = csq(csq(csq(L16)));
      lds_wr8(lds0 + LT_OFF + tid * 8, L); lds_wr8(lds0 + LT_OFF + 1536 + tid * 8, c2);
      lds_wr8(lds0 + LT_OFF + 2 * 1536 + tid * 8, L16); lds_wr8(lds0 + LT_OFF + 3 * 1536 + tid * 8, L128);
    }
  }
  u32x2 flg;
  const int rperm = 16 * ((r >> 2) & 1) + (r & 3) + 4 * (r >> 3);
  bf16x8 xn[36];
  unsigned xn_ad[4];
  if constexpr (SSM) {
    const int row = 32 * rg + rperm;
    xn_ad[0] = lds0 + X_OFF + row * 128 + ((h ^ ((row >> 1) & 7)) * 16);
    xn_ad[1] = xn_ad[0] ^ 32; xn_ad[2] = xn_ad[0] ^ 64; xn_ad[3] = xn_ad[0] ^ 96;
  } else {
    xn_ad[0] = lds0 + X_OFF + (32 * rg + (lane & 15)) * 128 + (((lane >> 4) ^ ((lane & 15) >> 1)) * 16);
    xn_ad[1] = xn_ad[0] ^ 64;
  }
  auto xn_read = [&](int rd, int ks) INL {
    if constexpr (SSM) xn[12 * rd + ks] = BF8(lds_rd(xn_ad[ks & 3] + (ks >> 2) * 16384));
    else xn[12 * rd + ks] = BF8(lds_rd(xn_ad[(ks >> 1) & 1] + (ks >> 2) * 16384 + (ks & 1) * 2048));
  };

  const int G1 = (lane >> 4) & 1, q = (lane >> 2) & 3, p4 = lane & 3;
  unsigned rb1[4], rb2[2][2], ab[4];
  if constexpr (SSM) {
    const int row = 32 * set + r;
    rb1[0] = lds0 + row * 128 + ((h ^ ((row >> 1) & 7)) * 16);
    rb1[1] = rb1[0] ^ 32; rb1[2] = rb1[0] ^ 64; rb1[3] = rb1[0] ^ 96;
    opaque(rb1[0]); opaque(rb1[1]); opaque(rb1[2]); opaque(rb1[3]);
  } else {
    rb1[0] = lds0 + (8 * (lane >> 4) + q) * 128 + (((2 * set) ^ ((q >> 1) | (G1 << 1))) * 32) + p4 * 8;
    rb1[1] = rb1[0] ^ 32; rb1[2] = rb1[0] + 2 * SLOT; rb1[3] = rb1[1] + 2 * SLOT;
    opaque(rb1[0]); opaque(rb1[1]); opaque(rb1[2]); opaque(rb1[3]);
  }
  unsigned tabb = lds0 + TAB_OFF + (SSM ? (32 * set + r) * 8 : (32 * set + (lane & 15)) * 16);
  opaque(tabb);
  unsigned tabh = tabb + h * 2048 + rg * 512;
  if constexpr (SSM) opaque(tabh);
  auto wfrag = [&](int n) INL { const int s = n / 12, ks = n % 12; return BF8(lds_rd(rb1[ks & 3] + (s & 1) * SLOT + (ks >> 2) * 8192)); };
  auto w1half = [&](int n, int t) INL {
    const int s = n / 12, ks = n % 12;
    return __builtin_amdgcn_ds_read_tr16_b64_v4bf16((LAS bf16x4*)(rb1[2 * ((s >> 1) & 1) + (ks & 1)] + (s & 1) * SLOT + (ks >> 1) * 4096 + t * 512));
  };
  auto w2half = [&](int g, int t) INL {
    const int s = 18 + g / 6;
    return __builtin_amdgcn_ds_read_tr16_b64_v4bf16((LAS bf16x4*)(rb2[0][(s >> 1) & 1] + (s & 1) * SLOT + g_cb(g) * 8192 + g_kk(g) * 4096 + t * 2048));
  };
  auto c2frag = [&](int g) INL { const int s = 18 + g / 6; return BF8(lds_rd(rb2[g_kk(g)][(s >> 1) & 1] + (s & 1) * SLOT + g_cb(g) * 8192)); };
  auto a2frag = [&](int e, int mt) INL {
    if constexpr (SSM) {
      const int v = (e & 1) ? e >> 1 : (e >> 1) - 1;
      const unsigned off = (v == 17 ? 3 : (v + 1) % 3) * 16384 + ((e & 1) ? 0 : 8192) + (mt >> 1) * 2048 + (mt & 1) * 512;
      return tr_frag(ab[2 * (mt & 1)] + off, ab[2 * (mt & 1) + 1] + off);
    } else {
      return BF8(lds_rd(ab[0] + ((e >> 1) & 3) * 16384 + (e & 1) * 8192 + mt * 1024));
    }
  };
  WAIT_L0();
  WAIT_V(SSM ? 5 : 4);
  BAR();
  f32x16 acc[4];
  constexpr int NA = KM + 1;
  u32x4 actp[2], gr[2];
  float tn, tr, tprev, tprev2;
  f32x2 Lam, Lx, ee, cc, cl, cq, g0, g1, g2;
  const unsigned long long gm0 = (c - 1 - 4 * rg >= 0 ? 0xffffffffull : 0ull) | (c - 3 - 4 * rg >= 0 ? 0xffffffff00000000ull : 0ull);
  const unsigned long long gm1 = (c - 2 - 4 * rg >= 0 ? 0xffffffffull : 0ull) | (c - 4 - 4 * rg >= 0 ? 0xffffffff00000000ull : 0ull);
  bf16x8 fr[4], a2[3][4];
  bf16x4 wl[3], wh[3], vl[4], vh[4];
  f32x4 a1[4][2][2];
  f32x4 o2[3][4];
  u32x2 fx[3], fdk[3], gc[2];
  u32x4 fst, corrA[2];
  unsigned corrV[3];
  float rsl, rsl2[2], tp;
  f32x4 csum;
  const int w67 = (w + 2) >> 3, cu0 = (w << w67) * 512, cu1 = cu0 + (6 - 5 * w67) * 512;
  float sk[12], xf[12];
  unsigned sto;
  const f32x16 zero = {};
  const char* amine = wc + C_ACT + (j * 6) * 16384 + set * 8192 + rg * 2048;
  const char* flagb = wc + C_FLAG + (3 * gq) * 128;
  if constexpr (SSM) { fr[0] = wfrag(0); fr[1] = wfrag(1); if constexpr (SPD == 3) fr[2] = wfrag(2); }
  else {
#pragma unroll
    for (int i = 0; i < 3; ++i) { vl[i] = w1half(i, 0); vh[i] = w1half(i, 1); }
  }

  auto pstore = [&](int cb, int hh) INL {
    u32x4 v;
#pragma unroll
    for (int e = 0; e < 4; ++e) v[e] = pk2(o2[cb][2 * hh + (e >> 1)][2 * (e & 1)], o2[cb][2 * hh + (e >> 1)][2 * (e & 1) + 1]);
    const unsigned o = pvo ^ (rg * 64 + ((pvo >> 8) << 4));
    if (!SSM && 2 * set + hh == gq) lds_wr(lds0 + ARING + 32768 + cb * 4096 + rg * 1024 + o, v);
    else gstore_sc1(wc + C_PART + ((2 * set + hh) * 9 + 3 * mi + cb) * 20480 + gq * 4096 + rg * 1024, o, v);
  };
  const int fpk = (w >> 1) < 3 ? (gq + 1 + (w >> 1)) & 3 : 4;
  const char* ffl = wc + C_FLGW + (3 * fpk + mi) * 1024 + (4 * (gq >> 1) + 2 * (w & 1)) * 128;
  bool got = false;
  auto fin_ready = [&]() INL { return __builtin_amdgcn_ballot_w64(flg[1] != tag || flg[0] < F_FIN) == 0ull; };
  auto fin_fetch = [&]() INL {
    const char* src = wc + C_PART + (gq * 9 + 3 * mi) * 20480 + fpk * 4096 + (w & 1) * 2048;
#pragma unroll
    for (int i = 0; i < 6; ++i) gldsA(src + (i >> 1) * 20480 + (i & 1) * 1024, pvo, lds0 + (w >> 1) * 12288 + (w & 1) * 2048, (i >> 1) * 4096 + (i & 1) * 1024);
  };
  auto step = [&](auto NI) INL {
    constexpr int n = NI.value;
    constexpr int s = n / 12, ks = n % 12;
    if constexpr (R != 2 && n == (SSM ? 212 : 209)) {
      const int lane = lane_now(), h = lane >> 5, G1 = (lane >> 4) & 1, q = (lane >> 2) & 3, p4 = lane & 3;
      if constexpr (SSM) {
        const int nn = lane & 15, o = p4 >> 1;
        rb2[0][0] = lds0 + (16 * rg + nn) * 128 + (((lane >> 4) ^ (nn >> 1)) * 16);
        rb2[1][0] = rb2[0][0] ^ 64;
#pragma unroll
        for (int k = 0; k < 2; ++k) { rb2[k][1] = rb2[k][0] + 2 * SLOT; opaque(rb2[k][0]); opaque(rb2[k][1]); }
        ab[0] = lds0 + ARING + set * 4096 + o * 1024 + h * 256 + G1 * 128 + o * 64 + q * 16 + (p4 & 1) * 8;
        ab[1] = ab[0] ^ 64; ab[2] = ab[0] ^ 128; ab[3] = ab[0] ^ 192;
        opaque(ab[0]); opaque(ab[1]); opaque(ab[2]); opaque(ab[3]);
      } else {
        rb2[0][0] = lds0 + (8 * h + 4 * G1 + q) * 128 + ((rg ^ (2 * G1 + (q >> 1))) * 32) + p4 * 8;
        rb2[0][1] = rb2[0][0] + 2 * SLOT;
        ab[0] = lds0 + ARING + set * 4096 + lane * 16;
        opaque(rb2[0][0]); opaque(rb2[0][1]); opaque(ab[0]);
      }
    }
    if constexpr (SSM && n == 16 * 12) pvo = lane_now() * 16;
    if constexpr (SSM && ks == 12 - SPD && (s & 1) == 1 && s < (R == 1 ? 11 : 17)) {
#pragma unroll
      for (int k = 0; k < 4; ++k) { rb1[k] += (s & 2) ? -2 * SLOT : 2 * SLOT; opaque(rb1[k]); }
    }
    constexpr int ss = (R == 1 && s >= 16) ? s - 4 : s;
    constexpr int pb = (ss >= 6) ? (ss - 6) / 6 : 0, ph = (ss >= 6 && ss < (R == 1 ? 18 : 24)) ? (ss - 6) % 6 : -1;
    constexpr bool mm_on = R == 0 || (R == 1 ? (n < 144 || n >= 216) : n < 216);
    if constexpr (ks == 0) {
      constexpr int pub = pub_at(R, s);
      constexpr bool poll = chk_need(R, s) != 0;
      if constexpr (poll) { if (w == 0) { const int l = lane_now(); flg = gload8_sc1(flagb, (l & 3) * 128); } }
      if constexpr (pub > 0) { if (w == 1) gstore8_sc1(flagb + mi * 128, (lane_now() & 15) * 8, u32x2{(unsigned)pub, tag}); }
      if constexpr (SSM && ph == 3) {
        const unsigned go = 3 * tabb - tabh;
        const char* gb = ws + WS_GRAN + ((R == 2 ? pb * 3 + 2 : mi * 3 + pb) * 16 + c - 1 - 4 * rg) * 1024 + rg * 512 - 2 * (lds0 + TAB_OFF);
        gr[0] = gload16_sc1(gb, go);
        gr[1] = gload16_sc1(gb - 1024, go);
      }
    }
    if constexpr (s < 3 * km(R)) {
      constexpr int kt = s / km(R), v = n - 12 * km(R) * kt;
      if constexpr (v == 0) { xn_read(kt, 0); xn_read(kt, 1); xn_read(kt, 2); xn_read(kt, 3); }
      if constexpr (SSM) {
        if constexpr (v >= 1 && v + 3 < 12) xn_read(kt, v + 3);
        constexpr int ch = v == 21 ? 5 : (v >= 12 && v <= 20 && v % 2 == 0) ? (v - 12) / 2 : -1;
        if constexpr (kt < 2 && ch >= 0) { xwrite1(ch / 3, 3 * (kt + 1) + ch % 3); SCHED(); }
      } else if constexpr (v >= 1 && v <= 4) { xn_read(kt, 2 * v + 2); xn_read(kt, 2 * v + 3); }
    }
    if constexpr (SSM && ks == 4 && (s == 8 || s == 11 || (R == 2 && (s == 14 || s == 17)))) {
      const int l = lane_now(), r = l & 31;
      const int row = 32 * rg + 16 * ((r >> 2) & 1) + (r & 3) + 4 * (r >> 3);
      const unsigned a = lds0 + X_OFF + 32768 + row * 128 + (((l >> 5) ^ ((row >> 1) & 7)) * 16);
      xn[32] = BF8(lds_rd(a)); xn[33] = BF8(lds_rd(a ^ 32)); xn[34] = BF8(lds_rd(a ^ 64)); xn[35] = BF8(lds_rd(a ^ 96));
    }
    if constexpr (op_w(R, n)) { if constexpr (s == 0) dmaW(2 + ks / 3, ks % 3); else dmaW(w_slot(R, s), ks / 3); }
    if constexpr (op_x(R, n)) dmaX(n / 36 + 1, x_piece(n));
    if constexpr (op_a(R, n)) {
      if constexpr (SSM) {
        constexpr int pc = a_code(n) & 1, v = (a_code(n) - pc) / 2, u = v + pc;
        gldsA((u_hlp(u) < 0 ? acm[u_hlp(u) < 0 ? u_mem(u) : 0] + (2 * u_blk(u) + (u & 1)) * 16384 : wc + C_ACT + (15 * 6 + 2 * u_hlp(u) + (u & 1)) * 16384 + w * 1024) + (pc ? 0 : 8192), pvo,
              lda, ARING + (v == 17 ? 3 : (v + 1) % 3) * 16384 + pc * 8192);
      } else {
        constexpr int u = s + 3 - 18;
        gldsA(acm[u % 3] + (u / 3) * 16384 + (ks / 3) * 8192, pvo, lda, ARING + (u & 3) * 16384 + (ks / 3) * 8192);
      }
    }
    if constexpr (op_g(R, n)) {
      if constexpr (n == 79) fst = gload16_sc1(wc + C_STAT, (32 * rg) * 16 + (pvo & 496));
      else gc[n - 80] = gload8_sc1(ws + WS_CSUM + j * 8192 + (n == 80 ? cu0 : cu1), pvo >> 1);
    }
    if constexpr (op_cs(R, n)) {
      constexpr int i = cs_piece(n);
      const char* cb_ = (const char*)P.w1 + (size_t)(288 * ((c / 6) & 1) + 36 * w + 8 * i) * (F * 2) + 768 * j + (c % 6) * 128;
      if constexpr (i < 5) xr[0][i] = gload16<0>(cb_, ((pvo >> 7) & (i < 4 ? 7 : 3)) * (F * 2) + (pvo & 112));
      else if constexpr (i == 5) epoch = gload4_sc1(ws + WS_EPOCH, 0u);
      else aux[0] = gload2((const char*)P.b1 + 768 * j + (w < 6 ? w : 0) * 128, pvo >> 3);
    }
    if constexpr (op_ft(R, n)) gstore8_sc1(wc + C_FLGW + j * 1024 + w * 128, (lane_now() & 15) * 8, u32x2{0u, tag});
    if constexpr (op_cp(R, n)) code_touch(SSM ? 3 : 4, 2048, ldw + ((s + 3) & 3) * SLOT + 2 * 8192);
    if constexpr (op_pf(R, n)) {
      const unsigned l = lane_now();
      constexpr int sl = SSM ? (s < 3 ? s : s - 3) : (s < 6 ? s - 5 : s - 6);
      if constexpr (SSM) {
        constexpr unsigned lim = s < 3 ? 72 : 108;
        unsigned id = sl * 32 + rg * 8 + (l >> 3); id = id < lim ? id : lim - 1;
        const u16* mat = s < 3 ? (set ? P.bim : P.bre) : (set ? P.cim : P.cre);
        glds4((const char*)mat + (size_t)(192 * mi + (s < 3 ? 64 : 0)) * (D * 2) + c * lim * 128, id * 128 + (l & 7) * 4, lds0 + JUNK_OFF);
      } else if constexpr (s < 3) {
        unsigned id = sl * 64 + w * 8 + (l >> 3); id = id < 144 ? id : 143;
        glds4((const char*)P.w1 + (size_t)(36 * c) * (F * 2) + 768 * j + 256, (id >> 2) * (F * 2) + (id & 3) * 128 + (l & 7) * 4, lds0 + JUNK_OFF);
      } else {
        unsigned id = sl * 64 + w * 8 + (l >> 3); id = id < 216 ? id : 215;
        glds4((const char*)P.w2 + (size_t)(384 * j) * (D * 2) + c * 216 * 128, id * 128 + (l & 7) * 4, lds0 + JUNK_OFF);
      }
    }
    if constexpr (SSM && n + SPD < (R == 1 ? 144 : 216)) fr[(n + SPD) & 3] = wfrag(n + SPD);
    if constexpr (R == 2) {
    } else if constexpr (SSM) {
      if constexpr (n == 213 || n == 214) { fr[n - 213] = c2frag(n - 213); a2[0][2 * (n - 213)] = a2frag(0, 2 * (n - 213)); a2[0][2 * (n - 213) + 1] = a2frag(0, 2 * (n - 213) + 1); }
    } else {
      if constexpr (n >= 210 && n < 214) a2[0][n - 210] = a2frag(0, n - 210);
      if constexpr (n == 212 || n == 214) wl[(n - 212) / 2] = w2half((n - 212) / 2, 0);
      if constexpr (n == 213 || n == 215) wh[(n - 212) / 2] = w2half((n - 212) / 2, 1);
      if constexpr (op_fo(R, n)) {
        constexpr int i = s - 25;
        const unsigned rw = w * 4 + (pvo >> 8), co = (pvo >> 1) & 120;
        if constexpr (i < 3) fx[i] = gload8((const char*)P.x + (size_t)(128 * c + 32 * gq) * (D * 2) + 384 * mi + i * 128, rw * (D * 2) + co);
        else if constexpr (i < 6) fdk[i - 3] = gload8((const char*)P.dsk + 384 * mi + (i - 3) * 128, co);
        else fst = gload16_sc1(wc + C_STAT, (32 * gq + rw) * 16);
      }
      if constexpr (n == 408) { pin2(fx[0]); pin2(fx[1]); pin2(fx[2]); pin2(fdk[0]); pin2(fdk[1]); pin2(fdk[2]); wait1<63>(fst); flg = gload8_sc1(ffl, pvo & 128); rsl = __uint_as_float(fst[2]) * LOG2E; tn = __uint_as_float(fst[2]) * (1.f / LOG2E); opaquef(rsl); opaquef(tn); }
      if constexpr (n >= 408 && n < 420) {
        constexpr int e = n - 408;
        const unsigned xv = fx[e >> 2][(e >> 1) & 1], dv = fdk[e >> 2][(e >> 1) & 1];
        xf[e] = (e & 1) ? bfhi(xv) : bflo(xv);
        sk[e] = (xf[e] - __uint_as_float(fst[0])) * tn * ((e & 1) ? bfhi(dv) : bflo(dv));
        opaquef(xf[e]); opaquef(sk[e]);
      }
      if constexpr (n == 422) {
        SCHED();
        wait0_2(flg);
        got = fin_ready();
        if (got) fin_fetch();
      }
    }
    auto epi = [&](auto PI) INL {
      if constexpr (!SSM && n >= EPI0 && n < EPI0 + 6 * EP) {
        constexpr int part = PI.value, T = (n - EPI0) / EP, u = (n - EPI0) % EP, e = u >> 1, e1 = e >= 1 ? e - 1 : 0, e0 = e & 15;
        if constexpr ((u & 1) == 0) {
          if constexpr (part == 0 && e >= 1) {
            const float x_ = a1[T % NA][(e1 >> 2) & 1][e1 >> 3][e1 & 3], a = __builtin_fmaf(-x_, tr, x_);
            if constexpr ((e1 & 1) == 0) { tprev = a; opaquef(tprev); }
            else { unsigned pk = pk2(tprev, a); opaque(pk); actp[e1 >> 3][(e1 >> 1) & 3] = pk; }
          }
          if constexpr (part == 1 && e < 16) { tn = __builtin_amdgcn_exp2f(a1[T % NA][(e0 >> 2) & 1][e0 >> 3][e0 & 3] * rsl2[e0 >> 3]); opaquef(tn); }
        } else if constexpr (part == 0) { tp = __builtin_fmaf(tn, __builtin_fmaf(tn, 0.5f, 1.f), 1.f); opaquef(tp); }
        else { tr = __builtin_amdgcn_rcpf(tp); opaquef(tr); }
      }
    };
    if constexpr (!mm_on) {
    } else if constexpr (s < 18) {
      constexpr int T = p1_tile(R, s), kt = p1_kt(R, s);
      if constexpr (SSM) { acc[T & 3] = MFMA32(xn[12 * kt + ks], fr[n & 3], (kt == 0 && ks == 0) ? zero : acc[T & 3]); opaque16(acc[T & 3]); }
      else {
        const bf16x8 wf = __builtin_shufflevector(vl[n & 3], vh[n & 3], 0, 1, 2, 3, 4, 5, 6, 7);
        f32x4(&ac)[2] = a1[T % NA][n & 1];
        SCHED();
        mfma16_acc<kt == 0 && ks < 2>(ac[0], wf, xn[12 * kt + (ks & ~1)]);
        SCHED();
        if constexpr (n + 3 < 216) vl[(n + 3) & 3] = w1half(n + 3, 0);
        SCHED();
        epi(IC<0>{});
        SCHED();
        mfma16_acc<kt == 0 && ks < 2>(ac[1], wf, xn[12 * kt + (ks | 1)]);
        SCHED();
        if constexpr (n + 3 < 216) vh[(n + 3) & 3] = w1half(n + 3, 1);
        SCHED();
        epi(IC<1>{});
      }
      if constexpr (SSM && s < 2 * km(R)) {
        constexpr int v = n - 12 * km(R) * kt, q = km(R) == 3 ? 4 : 6;
        constexpr int d0 = (q * v + 4) / 5, d1 = (q * (v + 1) + 4) / 5;
        if constexpr (d0 < d1 && d0 < 24) { constexpr int ch = d0 >> 2; xr[ch / 3][3 * (kt + 1) + ch % 3][d0 & 3] = norm4(xr[ch / 3][3 * (kt + 1) + ch % 3][d0 & 3], rs[ch / 3], nb[ch / 3]); }
        if constexpr (d0 + 1 < d1 && d0 + 1 < 24) { constexpr int d = d0 + 1, ch = d >> 2; xr[ch / 3][3 * (kt + 1) + ch % 3][d & 3] = norm4(xr[ch / 3][3 * (kt + 1) + ch % 3][d & 3], rs[ch / 3], nb[ch / 3]); }
      }
    } else {
      constexpr int g = (n - 216) / 2, cb = g_cb(g), e = 2 * (s - 18) + g_kk(g), m0 = 2 * (n & 1);
      bf16x8 wf;
      if constexpr (SSM) wf = fr[g % 3]; else wf = __builtin_shufflevector(wl[g % 3], wh[g % 3], 0, 1, 2, 3, 4, 5, 6, 7);
      const f32x4 z4 = {};
      SCHED();
      o2[cb][m0] = MFMA16(wf, a2[e_buf(e)][m0], g < 3 ? z4 : o2[cb][m0]); opaque4(o2[cb][m0]);
      SCHED();
      if constexpr (g + 2 < 108) {
        if constexpr (SSM) { if constexpr ((n & 1) == 0) fr[(g + 2) % 3] = c2frag(g + 2); }
        else if constexpr (n & 1) wh[(g + 2) % 3] = w2half(g + 2, 1);
        else wl[(g + 2) % 3] = w2half(g + 2, 0);
      }
      SCHED();
      epi(IC<0>{});
      SCHED();
      o2[cb][m0 + 1] = MFMA16(wf, a2[e_buf(e)][m0 + 1], g < 3 ? z4 : o2[cb][m0 + 1]); opaque4(o2[cb][m0 + 1]);
      SCHED();
      constexpr int en = (n - 216) / 6 + 1;
      if constexpr (n < 420 && ks % 6 < 4) a2[e_buf(en)][ks % 6] = a2frag(en, ks % 6);
      if constexpr (n == 412 || n == 413 || n == 418 || n == 419) a2[2][(n & 1) + 2 * (n >= 418)] = a2frag(35, (n & 1) + 2 * (n >= 418));
      SCHED();
      epi(IC<1>{});
      if constexpr (n == 425 || n == 426 || n == 429 || n == 430) pstore((n - 425) / 4, (n - 425) & 1);
    }
    if constexpr (!SSM) {
      if constexpr (n == CSA - 2) {
        const unsigned l = pvo >> 4, one = 0x3f80u << ((l & 1) * 16);
#pragma unroll
        for (int d = 0; d < 4; ++d) { unsigned v = ((l >> 1) & 3) == d ? one : 0u; opaque(v); corrA[0][d] = v; }
      }
      if constexpr (n == CSA - 1) {
#pragma unroll
        for (int d = 0; d < 4; ++d) { unsigned v = msel_u(corrA[0][d], 0xffffffffull); opaque(v); corrA[1][d] = v; }
      }
      if constexpr (n == CSA) {
        SCHED();
        wait3<ops_between(0, 34, CSA)>(xr[0][0], xr[0][1], xr[0][2]); wait2<63>(xr[0][3], xr[0][4]);
        const f32x4 z4 = {};
        csum = MFMA16(BF8(corrA[1]), BF8(xr[0][4]), z4); csum = MFMA16(BF8(corrA[0]), BF8(xr[0][0]), csum); opaque4(csum);
      }
      if constexpr (n == CSA + 1) { csum = MFMA16(BF8(corrA[0]), BF8(xr[0][1]), csum); csum = MFMA16(BF8(corrA[0]), BF8(xr[0][2]), csum); opaque4(csum); }
      if constexpr (n == CSA + 2) { csum = MFMA16(BF8(corrA[0]), BF8(xr[0][3]), csum); opaque4(csum); }
      if constexpr (n == CSA + 3) {
        f32x4 v;
#pragma unroll
        for (int e = 0; e < 4; ++e) v[e] = dpp_add<0x128>(csum[e]);
        *(LAS f32x4*)(lds0 + TAB_OFF + 8192 + w * 256 + ((pvo & 112) << 1) + ((pvo >> 4) & 16)) = v;
      }
      if constexpr (n == 48) {
        opaque(epoch); opaque(aux[0]); tag = __builtin_amdgcn_readfirstlane(epoch) + 1u;
        if (w == 2) {
          float t = 0.f;
#pragma unroll
          for (int k = 0; k < 8; ++k) t += *(LAS float*)(lds0 + TAB_OFF + 8192 + k * 256 + (pvo >> 2));
          gstore8_sc1(ws + WS_CSUM + j * 8192 + c * 512, pvo >> 1, u32x2{__float_as_uint(t), tag});
        }
      }
      if constexpr (n == 91) {
        SCHED();
        waitg<ops_between(0, 82, 91)>(fst, gc[0], gc[1]);
        auto stale = [&]() INL { return __builtin_amdgcn_ballot_w64(((fst[1] ^ tag) | (fst[3] ^ tag) | (gc[0][1] ^ tag) | (gc[1][1] ^ tag)) != 0u) != 0ull; };
        if (__builtin_expect(stale(), 0)) {
          const char* gb = ws + WS_CSUM + j * 8192;
          int spins = 200000;
          do { reloadg(fst, gc[0], gc[1], wc + C_STAT + (32 * rg) * 16, gb + cu0, gb + cu1, pvo & 496, pvo >> 1); } while (stale() && --spins > 0);
        }
        const float Sv = __uint_as_float(gc[0][0]) + __uint_as_float(gc[1][0]);
        const unsigned Sh = pk2(Sv, Sv), Sl = pk2(Sv - bfhi(Sh), 0.f);
        lds_wr(lds0 + TAB_OFF + w * 1024 + pvo, u32x4{Sh, (Sl & 0xffffu) | (aux[0] << 16), aux[0], 0u});
      }
      if constexpr (n == 93) { tn = __builtin_amdgcn_rcpf(__uint_as_float(fst[2])); rsl = __uint_as_float(fst[2]) * LOG2E; opaquef(tn); opaquef(rsl); }
      if constexpr (n == 94) { const float mu = -__uint_as_float(fst[0]); gc[0][0] = pk2(mu, tn); gc[0][1] = pk2(mu - bflo(gc[0][0]), tn - bfhi(gc[0][0])); pin2(gc[0]); }
      if constexpr (n == 95) {
        const unsigned hi = gc[0][0], lo = gc[0][1];
        corrV[0] = (hi & 0xffffu) | (lo << 16); corrV[1] = hi; corrV[2] = lo >> 16;
        opaque(corrV[0]); opaque(corrV[1]); opaque(corrV[2]);
        auto t = __builtin_amdgcn_permlane16_swap(__float_as_uint(rsl), __float_as_uint(rsl), false, false);
        rsl2[0] = __uint_as_float(t[0]); rsl2[1] = __uint_as_float(t[1]); opaquef(rsl2[0]); opaquef(rsl2[1]);
      }
      if constexpr (corr_tile(n + (n == 96 ? 2 : 6)) >= (n == 96 ? 0 : 1)) {
        constexpr int T = corr_tile(n + (n == 96 ? 2 : 6));
        corrA[0] = lds_rd(tabb + T * 1024); corrA[1] = lds_rd(tabb + T * 1024 + 256);
      }
      if constexpr (corr_tile(n) >= 0 || corr_tile(n - 1) >= 0) {
        constexpr int mt = corr_tile(n) < 0, T = corr_tile(n - mt);
        constexpr unsigned long long mk = mt ? 0xffff0000ull : 0xffffull;
        const u32x4 B = mask3(corrV[0], corrV[1], corrV[2], mk);
        mfma16_acc<false>(a1[T % NA][0][mt], BF8(corrA[0]), BF8(B));
        mfma16_acc<false>(a1[T % NA][1][mt], BF8(corrA[1]), BF8(B));
      }
      if constexpr (op_st(0, n)) { constexpr int id = st_id(n); gstore_sc1(amine + (id >> 1) * 16384 + (id & 1) * 1024, pvo, actp[id & 1]); }
    } else if constexpr (ph >= 0) {
      f32x16& ur = acc[(2 * pb) & 3];
      f32x16& ui = acc[(2 * pb + 1) & 3];
      const unsigned lt = tabb + pb * 512;
      const unsigned wt = tabb + (WT_OFF - TAB_OFF) + 2048, gt = tabb + (GT_OFF - TAB_OFF);
      if constexpr (ph == 0) {
        if constexpr (ks == 0) { ee = cfma(Lam, f32x2{ur[0], ui[0]}, f32x2{ur[1], ui[1]}); opaquec(ee); }
        if constexpr (ks >= 1 && ks <= 9) {
          constexpr int t0 = 2 + ((ks - 1) * 14) / 9, t1 = 2 + (ks * 14) / 9;
          ee = cfma(Lam, ee, f32x2{ur[t0], ui[t0]});
          if constexpr (t0 + 1 < t1) ee = cfma(Lam, ee, f32x2{ur[t0 + 1], ui[t0 + 1]});
          opaquec(ee);
        }
        if constexpr (ks == 7) Lx = lds_rd8(lt + 2 * 1536);
        if constexpr (ks == 10) {
          const f32x2 elo = {up32(ee[0]), up32(ee[1])};
          lds_wr8(tabh + (WT_OFF - TAB_OFF), cfma_s(Lx, elo, ee));
        }
        if constexpr (ks == 11) { Lam = csq(Lx); opaquec(Lam); }
      }
      if constexpr (ph == 1) {
        if constexpr (ks == 0) { g0 = lds_rd8(wt); g1 = lds_rd8(wt + 512); g2 = lds_rd8(wt + 1024); ee = lds_rd8(wt + 1536); }
        if constexpr (ks == 2) {
          cl = f32x2{0.f, 0.f}; cq = f32x2{1.f, 0.f};
          if (rg > 0) { cl = g0; cq = Lam; }
          if (rg > 1) { cl = cfma_s(Lam, cl, g1); cq = cmul_s(Lam, Lam); }
          if (rg > 2) {
            cl = cfma_s(Lam, cl, g2); cq = cmul_s(Lam, cq);
            const f32x2 E = cfma_s(Lam, cl, ee);
            gstore_sc1(ws + WS_GRAN + ((R == 2 ? pb * 3 + 2 : mi * 3 + pb) * 16 + c) * 1024 - 2 * (lds0 + TAB_OFF), 2 * tabb,
                       u32x4{__float_as_uint(E[0]), tag, __float_as_uint(E[1]), tag});
          }
          opaquec(cl); opaquec(cq);
          g0 = lds_rd8(wt - 2048 + rg * 512); cc = lds_rd8(lt + 1536);
        }
        if constexpr (ks == 4) {
          const f32x2 a = cfma_s(Lx, cl, g0);
          cl = f32x2{usel(cl[0], a[0]), usel(cl[1], a[1])};
          opaquec(cl);
        }
        if constexpr (ks == 5) { const f32x2 a = cmul_s(Lx, cq); cq = f32x2{usel(cq[0], a[0]), usel(cq[1], a[1])}; opaquec(cq); }
        if constexpr (ks == 6) { cl = cmul_s(cc, cl); opaquec(cl); }
        if constexpr (ks == 7) { cq = cmul_s(cc, cq); opaquec(cq); }
      }
      if constexpr ((ph == 1 && ks >= 8) || ph == 2) {
        constexpr int t = (ph == 1) ? ks - 8 : ks + 4;
        const float t1 = cc[1] * ui[t], t2 = cc[1] * ur[t];
        float a = __builtin_fmaf(cc[0], ur[t], -t1), b = __builtin_fmaf(-cc[0], ui[t], -t2);
        opaquef(a); opaquef(b);
        ur[t] = a; ui[t] = b;
      }
      if constexpr (ph == 3) {
        if constexpr (ks == 4) cc = lds_rd8(lt + 3 * 1536);
        if constexpr (ks == 7) { Lx = csq(cc); opaquec(Lx); }
        if constexpr (ks == 9) {
          SCHED();
          wait2<ops_between(R, 12 * s, n)>(gr[0], gr[1]);
          auto stale = [&]() INL {
            const unsigned b0 = (gr[0][1] ^ tag) | (gr[0][3] ^ tag), b1 = (gr[1][1] ^ tag) | (gr[1][3] ^ tag);
            return ((__builtin_amdgcn_ballot_w64(b0 != 0u) & gm0) | (__builtin_amdgcn_ballot_w64(b1 != 0u) & gm1)) != 0ull;
          };
          if (__builtin_expect(stale(), 0)) {
            const unsigned go = 3 * tabb - tabh;
            const char* gb = ws + WS_GRAN + ((R == 2 ? pb * 3 + 2 : mi * 3 + pb) * 16 + c - 1 - 4 * rg) * 1024 + rg * 512 - 2 * (lds0 + TAB_OFF);
            int spins = 200000;
            do { reload2(gr[0], gr[1], gb, gb - 1024, go); } while (stale() && --spins > 0);
          }
          const f32x2 a0 = {msel(gr[0][0], gm0), msel(gr[0][2], gm0)}, a1 = {msel(gr[1][0], gm1), msel(gr[1][2], gm1)};
          ee = cfma_s(cc, a1, a0); opaquec(ee);
        }
        if constexpr (ks == 10) {
          const f32x2 lo = {up32(ee[0]), up32(ee[1])};
          lds_wr8(tabh + (GT_OFF - TAB_OFF), cfma_s(Lx, ee, lo));
        }
        if constexpr (ks == 11) { cc = csq(Lx); opaquec(cc); }
      }
      if constexpr (ph == 4 && ks <= 3) {
        if constexpr (ks == 0) { ee = lds_rd8(gt + 2048 + 1536); g0 = lds_rd8(gt + 2048 + 1024); g1 = lds_rd8(gt + 2048 + 512); g2 = lds_rd8(gt + 2048); Lam = lds_rd8(lt); }
        if constexpr (ks == 2) { ee = cfma(cc, cfma(cc, ee, g0), g1); opaquec(ee); }
        if constexpr (ks == 3) { ee = cfma(cq, cfma(cc, ee, g2), cl); ee[1] = -ee[1]; opaquec(ee); }
      }
      if constexpr ((ph == 4 && ks >= 4) || (ph == 5 && ks <= 7)) {
        constexpr int tt = (ph == 4) ? ks - 4 : ks + 8;
        ee = cfma(f32x2{Lam[0], -Lam[1]}, ee, f32x2{ur[tt], ui[tt]});
        opaquec(ee);
        if constexpr ((tt & 1) == 0) { tprev = ee[0]; tprev2 = ee[1]; }
        else {
          unsigned p0 = pk2(tprev, ee[0]), p1 = pk2(tprev2, ee[1]); opaque(p0); opaque(p1);
          if constexpr (tt < 8) { gr[0][tt >> 1] = p0; gr[1][tt >> 1] = p1; }
          else { actp[0][(tt >> 1) & 3] = p0; actp[1][(tt >> 1) & 3] = p1; }
        }
      }
      if constexpr (op_st(R, n)) {
        constexpr int o = (ks - 7) >> 1, part = (ks - 7) & 1;
        if constexpr (ks == 7) { const int l = lane_now(); sto = (l ^ (8 * (l >> 5))) * 16; }
        if constexpr (ks == 9) sto ^= 64;
        gstore_sc1(amine + (2 * pb + part) * 16384 + o * 1024, sto, o ? actp[part] : gr[part]);
      }
    }
    if constexpr (SSM && ks == 9 && (s == 5 || s == 11 || (R == 2 && s == 17))) Lam = lds_rd8(tabb + ((s - 5) / 6) * 512);
    if constexpr (n == 8) { SCHED(); WAIT_V(ops_between(R, 0, 8)); BAR(); }
    if constexpr (R == 1 && n == 212) {
      SCHED(); WAIT_V(0); BAR();
      if (w == 0) { const int l = lane_now(); flg = gload8_sc1(flagb, (l & 3) * 128); }
    }
    if constexpr (R == 1 && n == 221) {
      SCHED();
      if (w == 0) {
        wait0_2(flg);
        const int l = lane_now();
        int spins = 200000;
        while ((__builtin_amdgcn_ballot_w64(flg[1] != tag || flg[0] < 2u) & 7ull) != 0ull && --spins > 0) { flg = gload8_sc1(flagb, (l & 3) * 128); wait0_2(flg); }
      }
      BAR();
    }
    if constexpr (R == 1 && n == 233) { SCHED(); WAIT_V(vm_n(1, 222, 233) + 2); BAR(); }
    if constexpr (!SSM && n == 44) { SCHED(); BAR(); }
    if constexpr (ks == 11 && s < 34) {
      SCHED();
      if constexpr (SSM && (s == 1 || s == 3)) WAIT_L0();
      if constexpr (SSM && (ph == 0 || ph == 3 || s == 17)) WAIT_L0();
      else if constexpr (R == 1 && s >= 18) asm volatile("s_waitcnt lgkmcnt(1)" ::: "memory");
      WAIT_V(drain(R, s) ? 0 : (R == 1 && s == 16) ? ops_between(1, 192, n) : vm_n(R, s == 0 ? 2 : (R == 0 && (s == 1 || s == 4)) ? 12 * s - 4 : 12 * s - 1, n));
      if constexpr (chk_need(R, s) != 0) {
        if (w == 0) {
          constexpr unsigned need = chk_need(R, s);
          constexpr unsigned long long lanes = (R == 1 && s != 26) ? 8ull : 7ull;
          WAIT_V(ops_between(R, 12 * s, n));
          pin2(flg);
          auto late = [&]() INL { return (__builtin_amdgcn_ballot_w64(flg[1] != tag || flg[0] < need) & lanes) != 0ull; };
          if (__builtin_expect(late(), 0)) {
            const int l = lane_now();
            int spins = 200000;
            do { flg = gload8_sc1(flagb, (l & 3) * 128); wait0_2(flg); } while (late() && --spins > 0);
          }
        }
      }
      BAR();
    }
    SCHED();
  };
  run_seq<0>(step, __make_integer_seq<ISeq, int, 144>{});
  if constexpr (R != 1) run_seq<144>(step, __make_integer_seq<ISeq, int, 48>{});
  run_seq<192>(step, __make_integer_seq<ISeq, int, 24>{});
  run_seq<216>(step, __make_integer_seq<ISeq, int, R == 2 ? 71 : 216>{});
  if constexpr (R != 2) { pstore(2, 0); pstore(2, 1); }
  WAIT_V(0);
  if constexpr (R == 2) { BAR(); if (w == 1) gstore8_sc1(flagb + mi * 128, (lane_now() & 15) * 8, u32x2{F_FIN, tag}); }
  else gstore8_sc1(wc + C_FLGW + j * 1024 + w * 128, (lane_now() & 15) * 8, u32x2{F_FIN, tag});
  if constexpr (!SSM) {
    const int l = lane_now(), t = w * 64 + l;
    const int c4 = t & 15;
    const unsigned rz = lds0 + (c4 >> 2) * 1024 + (16 * (c4 & 3) + (((t >> 4) & 15) ^ c4)) * 16 + (t >> 8) * 8;
    const char* ob = (const char*)(P.out + (size_t)(128 * c + 32 * gq) * D + 192 * mi);
    const unsigned oo = (t >> 4) * (D * 4) + c4 * 16;
    if (!got) {
      int spins = 400000;
      do { flg = gload8_sc1(ffl, pvo & 128); wait0_2(flg); } while (!fin_ready() && --spins > 0);
      fin_fetch();
    }
    WAIT_L0();
    WAIT_V(0);
    BAR();
#pragma unroll
    for (int cb = 0; cb < 3; ++cb) {
      u32x2 pv[4];
#pragma unroll
      for (int k = 0; k < 4; ++k) pv[k] = *(LAS u32x2*)(rz + k * 12288 + cb * 4096);
      const u32x2 ov = *(LAS u32x2*)(rz + ARING + 32768 + cb * 4096);
      f32x4 o;
#pragma unroll
      for (int e = 0; e < 4; ++e) {
        auto up = [&](unsigned v) INL { return (e & 1) ? bfhi(v) : bflo(v); };
        const float z = ((up(ov[e >> 1]) + up(pv[0][e >> 1])) + (up(pv[1][e >> 1]) + up(pv[2][e >> 1]))) * rsl;
        const float gt = z * __builtin_amdgcn_rcpf(1.f + __builtin_amdgcn_exp2f(-z));
        o[e] = __builtin_fmaf(__builtin_fmaf(up(pv[3][e >> 1]), 0.693147180559945f, sk[4 * cb + e]), gt, xf[4 * cb + e]);
      }
      gstore_sc1(ob + 256 * cb, oo, __builtin_bit_cast(u32x4, o));
    }
    if (c == 15 && j == 0 && w == 0) gstore8_sc1(ws + WS_EPOCH, 0u, u32x2{tag, tag});
  }
  if constexpr (R == 2) asm volatile("fk_hlp_end:"); else if constexpr (R == 1) asm volatile("fk_ssm_end:"); else asm volatile("fk_mlp_end:");
}

#if defined(__HIP_DEVICE_COMPILE__)
#define NOPK __attribute__((target("no-packed-fp32-ops")))
#else
#define NOPK
#endif
__global__ NOPK __launch_bounds__(512, 2) void main_kernel(const Params P) {
  __shared__ __attribute__((aligned(16))) char smem[163840];
  const unsigned lds0 = (unsigned)(size_t)(LAS char*)smem;
  const int b = blockIdx.x, xcd = b & 7, idx = b >> 3;
  int j, c;
  if (idx >= 24) { j = 12 + ((idx - 24) >> 1); c = 2 * xcd + (idx & 1); }
  else { j = 3 * (xcd & 3) + (idx >> 3); c = 8 * (xcd >> 2) + (idx & 7); }
#if defined(__HIP_DEVICE_COMPILE__)
  int jj = j;
  asm volatile("" : "+s"(jj));
  if (jj < 12) {
    const int lane = threadIdx.x & 63, w = __builtin_amdgcn_readfirstlane(threadIdx.x >> 6);
    const int gq = xcd & 3, mi = idx >> 3, rb0 = 8 * ((w + 4 * gq) & 15), ww = (w + c) & 7;
    const unsigned voffx = (lane >> 3) * (D * 2) + (((lane & 7) ^ ((4 * (w & 1) + (lane >> 4)) & 7)) * 16);
    const unsigned voff1 = (lane >> 3) * (F * 2) + (((lane & 7) ^ ((((lane >> 4) & 1) | ((ww & 1) << 1)) << 1)) * 16);
    __builtin_amdgcn_s_setprio(2);
#pragma unroll
    for (int i = 0; i < 6; ++i) {
      int kb = (i >> 1) + mi; kb = kb >= 3 ? kb - 3 : kb;
      const int rb = (i & 1) ? rb0 ^ 64 : rb0;
      gldsW((const char*)P.x + (size_t)(128 * c + rb) * (D * 2) + kb * 128, voffx, lds0 + kb * 16384 + rb * 128, X_OFF);
    }
#pragma unroll
    for (int i = 0; i < 3; ++i) gldsW((const char*)P.w1 + ((size_t)(8 * ww + 64 * i) * F + 384 * j) * 2, voff1, lds0 + ww * 1024, i * 8192);
    unsigned long long pc; unsigned beg, end;
    asm volatile("s_getpc_b64 %0\nfk_entry:\n\ts_mov_b32 %1, fk_mlp_beg-fk_entry\n\ts_mov_b32 %2, fk_mlp_end-fk_entry" : "=s"(pc), "=s"(beg), "=s"(end));
    const unsigned co = beg + ((idx * 8 + w) * 2 + (lane >> 5)) * 128;
    glds4((const void*)pc, co < end ? co : 0u, lds0 + JUNK_OFF);
  }
  if (j < 12) body<0>(P, lds0, c, j);
  else if (j == 15) body<2>(P, lds0, c, j);
  else body<1>(P, lds0, c, j);
#endif
}

extern "C" void kernel_launch(void* const* d_in, const int* in_sizes, int n_in, void* d_out, int out_size, void* d_ws, size_t ws_size,
                              hipStream_t stream) {
  Params P;
  P.x = (const u16*)d_in[0]; P.lre = (const u16*)d_in[1]; P.lim = (const u16*)d_in[2];
  P.bre = (const u16*)d_in[3]; P.bim = (const u16*)d_in[4]; P.cre = (const u16*)d_in[5]; P.cim = (const u16*)d_in[6];
  P.ldt = (const u16*)d_in[7]; P.w1 = (const u16*)d_in[10]; P.b1 = (const u16*)d_in[11]; P.w2 = (const u16*)d_in[12];
  P.dsk = (const u16*)d_in[8]; P.ws = (char*)d_ws; P.out = (float*)d_out;
  main_kernel<<<256, 512, 0, stream>>>(P);
}
```
